```python
import torch

def _softmax(x, axis=-1):
    return torch.softmax(x, dim=axis)
def _gelu(x):
    return torch.nn.functional.gelu(x, approximate="tanh")
def _silu(x):
    return torch.nn.functional.silu(x)
def _sigmoid(x):
    return torch.sigmoid(x)
def _layernorm(x, axis=-1, eps=1e-5):
    m = x.mean(dim=axis, keepdim=True)
    v = x.var(dim=axis, keepdim=True, unbiased=False)
    return (x - m) / torch.sqrt(v + eps)
def _scatter_add(tgt, idx, src, axis=0):
    return tgt.clone().index_add_(axis, idx, src)
def _segment_sum(src, seg, n):
    out = torch.zeros((n,) + src.shape[1:], dtype=src.dtype)
    return out.index_add_(0, seg, src)
def _pad(x, pw):
    flat = tuple(v for pair in reversed(pw) for v in pair)
    return torch.nn.functional.pad(x, flat)

kernel_name = 'recurrent_scan__sliding_block_cached__geglu_ffn__prompt'

def setup_inputs():

    _w = (lambda a: (lambda t: t.to(torch.bfloat16) if t.is_floating_point() else t)(torch.as_tensor(a)))
    def _A_setup():
        B, Sq, D = 1, 2048, 512
        Hq, Hkv, Sk, Dh = 32, 8, 2048, 128
        assert Hq % Hkv == 0, (Hq, Hkv)
        return {
            "x": _w((lambda *s: torch.randn(*s, dtype=torch.bfloat16) * 0.05)(B, Sq, D)),
            "k_cache": _w((lambda *s: torch.randn(*s, dtype=torch.bfloat16) * 0.8)(B, Hkv, Sk, Dh)),
            "v_cache": _w((lambda *s: torch.randn(*s, dtype=torch.bfloat16) * 0.2)(B, Hkv, Sk, Dh)),
            "wq": _w((lambda *s: torch.randn(*s, dtype=torch.bfloat16) * 0.8)(D, Hq * Dh) * (D ** -0.5)),
            "wo": _w((lambda *s: torch.randn(*s, dtype=torch.bfloat16) * 0.2)(Hq * Dh, D) * ((Hq * Dh) ** -0.5)),
        }
    _A_in = _A_setup()

    def _B_setup():
        B, S, D, F = 1, 2048, 512, 1792
        return {
            "x": _w((lambda *s: torch.randn(*s, dtype=torch.bfloat16) * 0.05)(B, S, D)),
            "w_gate": _w((lambda *s: torch.randn(*s, dtype=torch.bfloat16) * 0.1)(D, F)),
            "w_up": _w((lambda *s: torch.randn(*s, dtype=torch.bfloat16) * 0.1)(D, F)),
            "w_down": _w((lambda *s: torch.randn(*s, dtype=torch.bfloat16) * 0.1)(F, D)),
        }
    _B_in = _B_setup()

    return {
        "A_x": _A_in["x"],
        "A_k_cache": _A_in["k_cache"],
        "A_v_cache": _A_in["v_cache"],
        "A_wq": _A_in["wq"],
        "A_wo": _A_in["wo"],
        "B_x": _B_in["x"],
        "B_w_gate": _B_in["w_gate"],
        "B_w_up": _B_in["w_up"],
        "B_w_down": _B_in["w_down"],
    }

def reference(A_x, A_k_cache, A_v_cache, A_wq, A_wo, B_x, B_w_gate, B_w_up, B_w_down):
    def _A_ref(x, k_cache, v_cache, wq, wo):
        B, Sq, D = x.shape
        Hq, Dh = 32, 128
        Hkv, Sk = k_cache.shape[1], k_cache.shape[2]
        W = min(4096, max(256, Sk // 4))
        rep = Hq // Hkv
        q = (lambda x, *d: x.permute(*d))((x @ wq).reshape(B, Sq, Hq, Dh), 0, 2, 1, 3)
        k = (lambda x, n, axis: torch.repeat_interleave(x, n, dim=axis))(k_cache[:, :, Sk - W:, :], rep, axis=1)
        v = (lambda x, n, axis: torch.repeat_interleave(x, n, dim=axis))(v_cache[:, :, Sk - W:, :], rep, axis=1)
        scores = torch.einsum("bhqd,bhkd->bhqk", q, k) * (Dh ** -0.5)
        p = _softmax(scores, axis=-1)
        o = torch.einsum("bhqk,bhkd->bhqd", p, v)
        return (lambda x, *d: x.permute(*d))(o, 0, 2, 1, 3).reshape(B, Sq, Hq * Dh) @ wo

    def _B_ref(x, w_gate, w_up, w_down):
        return (_gelu(x @ w_gate) * (x @ w_up)) @ w_down

    x = A_x
    xn = _layernorm(x, axis=-1)
    s = _A_ref(xn, A_k_cache, A_v_cache, A_wq, A_wo)
    g = _silu(_B_ref(xn, B_w_gate, B_w_up, B_w_down))
    return x + s * g
```

```cpp
#include <hip/hip_runtime.h>
#include <hip/hip_bf16.h>
#include <stdint.h>

typedef unsigned short u16;
__device__ __forceinline__ float bf2f(u16 v) { return __uint_as_float(((unsigned)v) << 16); }
__device__ __forceinline__ u16 f2bf(float f) { __bf16 b = (__bf16)f; return __builtin_bit_cast(u16, b); }
__device__ __forceinline__ float rbf(float f) { return bf2f(f2bf(f)); }

constexpr int S = 2048, D = 512, HQ = 32, HKV = 8, DH = 128, SK = 2048, W = 512, F = 1792;

__global__ void ln_kernel(const u16* x, u16* xn) {
  int row = blockIdx.x; int t = threadIdx.x;
  float v[8]; float s = 0;
  for (int i = 0; i < 8; i++) { v[i] = bf2f(x[row * D + t * 8 + i]); s += v[i]; }
  for (int o = 32; o; o >>= 1) s += __shfl_xor(s, o);
  float m = s / D; float ss = 0;
  for (int i = 0; i < 8; i++) { float d = v[i] - m; ss += d * d; }
  for (int o = 32; o; o >>= 1) ss += __shfl_xor(ss, o);
  float var = rbf(ss / D); float mb = rbf(m);
  float den = rbf(sqrtf(rbf(var + 1e-5f)));
  for (int i = 0; i < 8; i++) xn[row * D + t * 8 + i] = f2bf(rbf(v[i] - mb) / den);
}
__global__ void gemm_kernel(const u16* A, const u16* B, u16* C, int M, int N, int K) {
  __shared__ float As[16][17], Bs[16][17];
  int tx = threadIdx.x & 15, ty = threadIdx.x >> 4;
  int r = blockIdx.y * 16 + ty, c = blockIdx.x * 16 + tx;
  float acc = 0;
  for (int k0 = 0; k0 < K; k0 += 16) {
    As[ty][tx] = bf2f(A[(size_t)r * K + k0 + tx]);
    Bs[ty][tx] = bf2f(B[(size_t)(k0 + ty) * N + c]);
    __syncthreads();
    for (int k = 0; k < 16; k++) acc += As[ty][k] * Bs[k][tx];
    __syncthreads();
  }
  C[(size_t)r * N + c] = f2bf(acc);
}
__global__ void attn_kernel(const u16* q, const u16* kc, const u16* vc, u16* o) {
  int row = blockIdx.x, h = blockIdx.y, t = threadIdx.x; int kvh = h / 4;
  __shared__ float qs[DH]; __shared__ float p[W]; __shared__ float red[128];
  qs[t] = bf2f(q[(size_t)row * 4096 + h * DH + t]);
  __syncthreads();
  float mx = -1e30f;
  for (int j = t; j < W; j += 128) {
    const u16* kp = kc + ((size_t)kvh * SK + (SK - W) + j) * DH;
    float s = 0; for (int d = 0; d < DH; d++) s += qs[d] * bf2f(kp[d]);
    s = rbf(rbf(s) * rbf(0.08838834764831845f));
    p[j] = s; mx = fmaxf(mx, s);
  }
  red[t] = mx; __syncthreads();
  for (int o2 = 64; o2; o2 >>= 1) { if (t < o2) red[t] = fmaxf(red[t], red[t + o2]); __syncthreads(); }
  mx = red[0]; __syncthreads();
  float sum = 0;
  for (int j = t; j < W; j += 128) { float e = expf(p[j] - mx); p[j] = e; sum += e; }
  red[t] = sum; __syncthreads();
  for (int o2 = 64; o2; o2 >>= 1) { if (t < o2) red[t] += red[t + o2]; __syncthreads(); }
  sum = red[0]; __syncthreads();
  for (int j = t; j < W; j += 128) p[j] = rbf(p[j] / sum);
  __syncthreads();
  float acc = 0;
  for (int j = 0; j < W; j++) acc += p[j] * bf2f(vc[((size_t)kvh * SK + (SK - W) + j) * DH + t]);
  o[(size_t)row * 4096 + h * DH + t] = f2bf(acc);
}
__global__ void h_kernel(const u16* g, const u16* u, u16* h, int n) {
  int i = blockIdx.x * 256 + threadIdx.x; if (i >= n) return;
  float x = bf2f(g[i]);
  float inner = 0.7978845608028654f * (x + 0.044715f * x * x * x);
  float ge = rbf(0.5f * x * (1.f + tanhf(inner)));
  h[i] = f2bf(ge * bf2f(u[i]));
}
__global__ void final_kernel(const u16* x, const u16* s, const u16* y, u16* out, int n) {
  int i = blockIdx.x * 256 + threadIdx.x; if (i >= n) return;
  float yy = bf2f(y[i]); float g = rbf(yy / (1.f + expf(-yy)));
  out[i] = f2bf(bf2f(x[i]) + rbf(bf2f(s[i]) * g));
}

extern "C" void kernel_launch(void* const* d_in, const int* in_sizes, int n_in,
                              void* d_out, int out_size, void* d_ws, size_t ws_size,
                              hipStream_t stream) {
  const u16* x = (const u16*)d_in[0]; const u16* kc = (const u16*)d_in[1]; const u16* vc = (const u16*)d_in[2];
  const u16* wq = (const u16*)d_in[3]; const u16* wo = (const u16*)d_in[4];
  const u16* wg = (const u16*)d_in[6]; const u16* wu = (const u16*)d_in[7]; const u16* wd = (const u16*)d_in[8];
  u16* ws = (u16*)d_ws;
  u16* xn = ws; u16* q = xn + S * D; u16* o = q + S * 4096; u16* s = o + S * 4096; u16* g = s + S * D; u16* u = g + S * F; u16* h = u + S * F; u16* y = h + S * F;
  ln_kernel<<<S, 64, 0, stream>>>(x, xn);
  gemm_kernel<<<dim3(4096 / 16, S / 16), 256, 0, stream>>>(xn, wq, q, S, 4096, D);
  attn_kernel<<<dim3(S, HQ), 128, 0, stream>>>(q, kc, vc, o);
  gemm_kernel<<<dim3(D / 16, S / 16), 256, 0, stream>>>(o, wo, s, S, D, 4096);
  gemm_kernel<<<dim3(F / 16, S / 16), 256, 0, stream>>>(xn, wg, g, S, F, D);
  gemm_kernel<<<dim3(F / 16, S / 16), 256, 0, stream>>>(xn, wu, u, S, F, D);
  h_kernel<<<(S * F + 255) / 256, 256, 0, stream>>>(g, u, h, S * F);
  gemm_kernel<<<dim3(D / 16, S / 16), 256, 0, stream>>>(h, wd, y, S, D, F);
  final_kernel<<<(S * D + 255) / 256, 256, 0, stream>>>(x, s, y, (u16*)d_out, S * D);
}
```

```cpp
#include <hip/hip_runtime.h>
#include <hip/hip_bf16.h>

__global__ __launch_bounds__(256, 1) void fused_block_kernel(const void* x, const void* kc, const void* vc, const void* wq, const void* wo,
                                                             const void* wg, const void* wu, const void* wd, void* out, void* ws) {
  asm volatile(
    "CSTART_%=:\n"
    "v_lshrrev_b32 v2, 6, %11\n"
    "v_and_b32 v1, 63, %11\n"
    "s_and_b32 s33, %10, 7\n"
    "v_readfirstlane_b32 s32, v2\n"
    "s_lshr_b32 s34, %10, 3\n"
    "s_mov_b64 s[40:41], %0\n"
    "s_and_b32 s41, s41, 0xffff\n"
    "s_mov_b32 s42, 0x200000\n"
    "s_mov_b32 s43, 0x20000\n"
    "v_lshrrev_b32 v40, 3, v1\n"
    "s_lshl_b32 s37, s32, 4\n"
    "v_add_u32 v40, s37, v40\n"
    "v_lshlrev_b32 v40, 10, v40\n"
    "v_and_b32 v41, 7, v1\n"
    "v_lshl_add_u32 v40, v41, 4, v40\n"
    "s_lshl_b32 s38, s34, 16\n"
    "s_add_u32 s39, s38, 0x2000\n"
    "buffer_load_dwordx4 v[64:67], v40, s[40:43], s38 offen\n"
    "buffer_load_dwordx4 v[68:71], v40, s[40:43], s39 offen\n"
    "buffer_load_dwordx4 v[72:75], v40, s[40:43], s38 offen offset:128\n"
    "buffer_load_dwordx4 v[76:79], v40, s[40:43], s39 offen offset:128\n"
    "buffer_load_dwordx4 v[80:83], v40, s[40:43], s38 offen offset:256\n"
    "buffer_load_dwordx4 v[84:87], v40, s[40:43], s39 offen offset:256\n"
    "buffer_load_dwordx4 v[88:91], v40, s[40:43], s38 offen offset:384\n"
    "buffer_load_dwordx4 v[92:95], v40, s[40:43], s39 offen offset:384\n"
    "buffer_load_dwordx4 v[96:99], v40, s[40:43], s38 offen offset:512\n"
    "buffer_load_dwordx4 v[100:103], v40, s[40:43], s39 offen offset:512\n"
    "buffer_load_dwordx4 v[104:107], v40, s[40:43], s38 offen offset:640\n"
    "buffer_load_dwordx4 v[108:111], v40, s[40:43], s39 offen offset:640\n"
    "buffer_load_dwordx4 v[112:115], v40, s[40:43], s38 offen offset:768\n"
    "buffer_load_dwordx4 v[116:119], v40, s[40:43], s39 offen offset:768\n"
    "buffer_load_dwordx4 v[120:123], v40, s[40:43], s38 offen offset:896\n"
    "buffer_load_dwordx4 v[124:127], v40, s[40:43], s39 offen offset:896\n"
    "s_getpc_b64 s[80:81]\n"
    "CPF_%=:\n"
    "s_sub_u32 s80, s80, CPF_%= - CSTART_%=\n"
    "s_subb_u32 s81, s81, 0\n"
    "s_and_b32 s81, s81, 0xffff\n"
    "s_mov_b32 s82, CEND_%= - CSTART_%=\n"
    "s_mov_b32 s83, 0x20000\n"
    "s_lshl_b32 s37, s34, 2\n"
    "s_add_u32 s37, s37, s32\n"
    "s_and_b32 s37, s37, 15\n"
    "s_lshl_b32 s37, s37, 12\n"
    "v_lshlrev_b32 v44, 6, v1\n"
    "buffer_load_dword v136, v44, s[80:83], s37 offen\n"
    "s_mov_b64 s[44:45], %5\n"
    "s_sub_u32 s44, s44, 0x1000\n"
    "s_subb_u32 s45, s45, 0\n"
    "s_and_b32 s45, s45, 0xffff\n"
    "s_mov_b32 s46, 0x1c1000\n"
    "s_mov_b32 s47, 0x20000\n"
    "s_mov_b64 s[48:49], %6\n"
    "s_sub_u32 s48, s48, 0x1000\n"
    "s_subb_u32 s49, s49, 0\n"
    "s_and_b32 s49, s49, 0xffff\n"
    "s_mov_b32 s50, 0x1c1000\n"
    "s_mov_b32 s51, 0x20000\n"
    "v_lshlrev_b32 v2, 4, v1\n"
    "v_bfe_u32 v43, v1, 2, 3\n"
    "v_mul_u32_u24 v42, 0xe00, v43\n"
    "v_lshrrev_b32 v43, 5, v1\n"
    "v_lshl_add_u32 v42, v43, 6, v42\n"
    "v_lshrrev_b32 v43, 3, v1\n"
    "v_and_b32 v43, 2, v43\n"
    "v_xor_b32 v43, v43, v1\n"
    "v_and_b32 v43, 3, v43\n"
    "v_lshl_add_u32 v42, v43, 4, v42\n"
    "s_cmp_eq_u32 s32, 3\n"
    "s_cselect_b32 s37, 0x40000000, 0\n"
    "v_lshrrev_b32 v43, 5, v1\n"
    "v_mul_lo_u32 v43, v43, s37\n"
    "v_add_u32 v42, v42, v43\n"
    "v_add_u32 v25, 0x1000, v42\n"
    "v_add_u32 v26, 0xc00, v42\n"
    "v_add_u32 v27, 0x7800, v42\n"
    "v_add_u32 v28, 0x7400, v42\n"
    "s_lshl_b32 s89, s32, 14\n"
    "s_add_u32 s89, s89, 0x18000\n"
    "s_and_b32 s38, s33, 1\n"
    "s_lshl_b32 s37, s32, 1\n"
    "s_add_u32 s37, s37, s38\n"
    "s_cmp_eq_u32 s38, 1\n"
    "s_cselect_b32 s38, 0, 6\n"
    "s_cmp_eq_u32 s32, 3\n"
    "s_cselect_b32 s37, s38, s37\n"
    "s_mul_i32 s38, s33, 7\n"
    "s_add_u32 s37, s37, s38\n"
    "s_lshl_b32 s88, s37, 6\n"
    "s_add_u32 m0, s89, 0\n"
    "s_nop 0\n"
    "buffer_load_dwordx4 v25, s[44:47], s88 offen lds\n"
    "buffer_load_dwordx4 v26, s[48:51], s88 offen offset:1024 lds\n"
    "buffer_load_dwordx4 v27, s[44:47], s88 offen offset:2048 lds\n"
    "buffer_load_dwordx4 v28, s[48:51], s88 offen offset:3072 lds\n"
    "s_add_u32 s88, s88, 0xe000\n"
    "s_add_u32 m0, s89, 0x1000\n"
    "s_nop 0\n"
    "buffer_load_dwordx4 v25, s[44:47], s88 offen lds\n"
    "buffer_load_dwordx4 v26, s[48:51], s88 offen offset:1024 lds\n"
    "buffer_load_dwordx4 v27, s[44:47], s88 offen offset:2048 lds\n"
    "buffer_load_dwordx4 v28, s[48:51], s88 offen offset:3072 lds\n"
    "s_add_u32 s88, s88, 0xe000\n"
    "s_add_u32 m0, s89, 0x2000\n"
    "s_nop 0\n"
    "buffer_load_dwordx4 v25, s[44:47], s88 offen lds\n"
    "buffer_load_dwordx4 v26, s[48:51], s88 offen offset:1024 lds\n"
    "buffer_load_dwordx4 v27, s[44:47], s88 offen offset:2048 lds\n"
    "buffer_load_dwordx4 v28, s[48:51], s88 offen offset:3072 lds\n"
    "s_add_u32 s88, s88, 0xe000\n"
    "s_add_u32 m0, s89, 0x3000\n"
    "s_nop 0\n"
    "buffer_load_dwordx4 v25, s[44:47], s88 offen lds\n"
    "buffer_load_dwordx4 v26, s[48:51], s88 offen offset:1024 lds\n"
    "buffer_load_dwordx4 v27, s[44:47], s88 offen offset:2048 lds\n"
    "buffer_load_dwordx4 v28, s[48:51], s88 offen offset:3072 lds\n"
    "s_add_u32 s88, s88, 0xe000\n"
    "s_mov_b32 s95, s88\n"
    "s_add_u32 s88, s88, 0x1c000\n"
    "s_mov_b64 s[52:53], %3\n"
    "s_sub_u32 s52, s52, 0x1000\n"
    "s_subb_u32 s53, s53, 0\n"
    "s_and_b32 s53, s53, 0xffff\n"
    "s_mov_b32 s54, 0x401000\n"
    "s_mov_b32 s55, 0x20000\n"
    "s_mul_i32 s37, s33, 0x80000\n"
    "s_add_u32 s37, s37, 0x60000\n"
    "s_mov_b64 s[56:57], %1\n"
    "s_add_u32 s56, s56, s37\n"
    "s_addc_u32 s57, s57, 0\n"
    "s_and_b32 s57, s57, 0xffff\n"
    "s_mov_b32 s58, 0x20000\n"
    "s_mov_b32 s59, 0x20000\n"
    "s_mov_b64 s[60:61], %2\n"
    "s_add_u32 s60, s60, s37\n"
    "s_addc_u32 s61, s61, 0\n"
    "s_sub_u32 s60, s60, 0x1000\n"
    "s_subb_u32 s61, s61, 0\n"
    "s_and_b32 s61, s61, 0xffff\n"
    "s_mov_b32 s62, 0x21000\n"
    "s_mov_b32 s63, 0x20000\n"
    "s_mov_b64 s[64:65], %4\n"
    "s_mov_b64 s[68:69], %7\n"
    "s_mov_b64 s[72:73], %9\n"
    "s_mov_b64 s[76:77], %9\n"
    "s_mov_b64 s[80:81], %8\n"
    "s_mov_b32 s92, 0x3f803f80\n"
    "s_mov_b32 s93, 0x3f80\n"
    "s_mov_b32 s94, 0x3f800000\n"
    "v_mov_b32 v0, 0\n"
    "s_mov_b64 s[84:85], %9\n"
    "s_and_b32 s85, s85, 0xffff\n"
    "s_mov_b32 s86, 0x2000\n"
    "s_mov_b32 s87, 0x20000\n"
    "s_cmp_eq_u32 s32, 0\n"
    "s_cselect_b32 s37, 1, 0\n"
    "v_mov_b32 v38, s37\n"
    "v_mov_b32 v39, 0\n"
    "s_lshl_b32 s38, s34, 8\n"
    "s_mov_b64 exec, 1\n"
    "buffer_atomic_add_x2 v[38:39], v0, s[84:87], s38 offen sc0\n"
    "s_mov_b64 exec, -1\n"
    "; PHASE pro0\n"
    "v_lshrrev_b32 v41, 5, v1\n"
    "v_lshlrev_b32 v42, 11, v41\n"
    "v_bfe_u32 v43, v1, 2, 3\n"
    "v_lshl_add_u32 v42, v43, 6, v42\n"
    "v_bfe_u32 v43, v1, 1, 1\n"
    "v_lshl_add_u32 v42, v43, 4, v42\n"
    "v_and_b32 v43, 1, v1\n"
    "v_lshl_add_u32 v42, v43, 3, v42\n"
    "v_bfe_u32 v43, v1, 4, 1\n"
    "v_lshlrev_b32 v43, 5, v43\n"
    "v_add_u32 v9, v42, v43\n"
    "v_xor_b32 v43, 32, v43\n"
    "v_add_u32 v10, v42, v43\n"
    "s_lshl_b32 s91, s32, 12\n"
    "s_add_u32 s37, s91, 0x10000\n"
    "v_add_u32 v3, s89, v9\n"
    "v_add_u32 v5, s37, v9\n"
    "v_add_u32 v4, s89, v10\n"
    "v_add_u32 v6, s37, v10\n"
    "s_waitcnt vmcnt(33)\n"
    "v_dot2_f32_bf16 v44, v64, s92, 0\n"
    "v_dot2_f32_bf16 v45, v65, s92, 0\n"
    "v_dot2_f32_bf16 v44, v66, s92, v44\n"
    "v_dot2_f32_bf16 v45, v67, s92, v45\n"
    "v_dot2_f32_bf16 v48, v64, v64, 0\n"
    "v_dot2_f32_bf16 v49, v65, v65, 0\n"
    "v_dot2_f32_bf16 v48, v66, v66, v48\n"
    "v_dot2_f32_bf16 v49, v67, v67, v49\n"
    "s_waitcnt vmcnt(32)\n"
    "v_dot2_f32_bf16 v46, v68, s92, 0\n"
    "v_dot2_f32_bf16 v47, v69, s92, 0\n"
    "v_dot2_f32_bf16 v46, v70, s92, v46\n"
    "v_dot2_f32_bf16 v47, v71, s92, v47\n"
    "v_dot2_f32_bf16 v50, v68, v68, 0\n"
    "v_dot2_f32_bf16 v51, v69, v69, 0\n"
    "v_dot2_f32_bf16 v50, v70, v70, v50\n"
    "v_dot2_f32_bf16 v51, v71, v71, v51\n"
    "s_waitcnt vmcnt(31)\n"
    "v_dot2_f32_bf16 v44, v72, s92, v44\n"
    "v_dot2_f32_bf16 v45, v73, s92, v45\n"
    "v_dot2_f32_bf16 v44, v74, s92, v44\n"
    "v_dot2_f32_bf16 v45, v75, s92, v45\n"
    "v_dot2_f32_bf16 v48, v72, v72, v48\n"
    "v_dot2_f32_bf16 v49, v73, v73, v49\n"
    "v_dot2_f32_bf16 v48, v74, v74, v48\n"
    "v_dot2_f32_bf16 v49, v75, v75, v49\n"
    "s_waitcnt vmcnt(30)\n"
    "v_dot2_f32_bf16 v46, v76, s92, v46\n"
    "v_dot2_f32_bf16 v47, v77, s92, v47\n"
    "v_dot2_f32_bf16 v46, v78, s92, v46\n"
    "v_dot2_f32_bf16 v47, v79, s92, v47\n"
    "v_dot2_f32_bf16 v50, v76, v76, v50\n"
    "v_dot2_f32_bf16 v51, v77, v77, v51\n"
    "v_dot2_f32_bf16 v50, v78, v78, v50\n"
    "v_dot2_f32_bf16 v51, v79, v79, v51\n"
    "s_waitcnt vmcnt(29)\n"
    "v_dot2_f32_bf16 v44, v80, s92, v44\n"
    "v_dot2_f32_bf16 v45, v81, s92, v45\n"
    "v_dot2_f32_bf16 v44, v82, s92, v44\n"
    "v_dot2_f32_bf16 v45, v83, s92, v45\n"
    "v_dot2_f32_bf16 v48, v80, v80, v48\n"
    "v_dot2_f32_bf16 v49, v81, v81, v49\n"
    "v_dot2_f32_bf16 v48, v82, v82, v48\n"
    "v_dot2_f32_bf16 v49, v83, v83, v49\n"
    "s_waitcnt vmcnt(28)\n"
    "v_dot2_f32_bf16 v46, v84, s92, v46\n"
    "v_dot2_f32_bf16 v47, v85, s92, v47\n"
    "v_dot2_f32_bf16 v46, v86, s92, v46\n"
    "v_dot2_f32_bf16 v47, v87, s92, v47\n"
    "v_dot2_f32_bf16 v50, v84, v84, v50\n"
    "v_dot2_f32_bf16 v51, v85, v85, v51\n"
    "v_dot2_f32_bf16 v50, v86, v86, v50\n"
    "v_dot2_f32_bf16 v51, v87, v87, v51\n"
    "s_waitcnt vmcnt(27)\n"
    "v_dot2_f32_bf16 v44, v88, s92, v44\n"
    "v_dot2_f32_bf16 v45, v89, s92, v45\n"
    "v_dot2_f32_bf16 v44, v90, s92, v44\n"
    "v_dot2_f32_bf16 v45, v91, s92, v45\n"
    "v_dot2_f32_bf16 v48, v88, v88, v48\n"
    "v_dot2_f32_bf16 v49, v89, v89, v49\n"
    "v_dot2_f32_bf16 v48, v90, v90, v48\n"
    "v_dot2_f32_bf16 v49, v91, v91, v49\n"
    "s_waitcnt vmcnt(26)\n"
    "v_dot2_f32_bf16 v46, v92, s92, v46\n"
    "v_dot2_f32_bf16 v47, v93, s92, v47\n"
    "v_dot2_f32_bf16 v46, v94, s92, v46\n"
    "v_dot2_f32_bf16 v47, v95, s92, v47\n"
    "v_dot2_f32_bf16 v50, v92, v92, v50\n"
    "v_dot2_f32_bf16 v51, v93, v93, v51\n"
    "v_dot2_f32_bf16 v50, v94, v94, v50\n"
    "v_dot2_f32_bf16 v51, v95, v95, v51\n"
    "s_mov_b32 s39, 0x40000000\n"
    "v_lshrrev_b32 v42, 3, v1\n"
    "v_mul_u32_u24 v42, 0xe00, v42\n"
    "v_and_b32 v43, 7, v1\n"
    "v_lshl_add_u32 v42, v43, 6, v42\n"
    "v_lshrrev_b32 v43, 5, v1\n"
    "v_mul_lo_u32 v43, v43, s39\n"
    "v_add_u32 v42, v42, v43\n"
    "v_add_u32 v42, 0x1000, v42\n"
    "s_lshl_b32 s37, s34, 2\n"
    "s_add_u32 s37, s37, s32\n"
    "s_mul_i32 s37, s37, 0x3800\n"
    "s_mul_i32 s38, s33, 0x1c0\n"
    "s_add_u32 s37, s37, s38\n"
    "buffer_load_dword v128, v42, s[44:47], s37 offen\n"
    "buffer_load_dword v129, v42, s[48:51], s37 offen\n"
    "s_waitcnt vmcnt(27)\n"
    "v_dot2_f32_bf16 v44, v96, s92, v44\n"
    "v_dot2_f32_bf16 v45, v97, s92, v45\n"
    "v_dot2_f32_bf16 v44, v98, s92, v44\n"
    "v_dot2_f32_bf16 v45, v99, s92, v45\n"
    "v_dot2_f32_bf16 v48, v96, v96, v48\n"
    "v_dot2_f32_bf16 v49, v97, v97, v49\n"
    "v_dot2_f32_bf16 v48, v98, v98, v48\n"
    "v_dot2_f32_bf16 v49, v99, v99, v49\n"
    "s_waitcnt vmcnt(26)\n"
    "v_dot2_f32_bf16 v46, v100, s92, v46\n"
    "v_dot2_f32_bf16 v47, v101, s92, v47\n"
    "v_dot2_f32_bf16 v46, v102, s92, v46\n"
    "v_dot2_f32_bf16 v47, v103, s92, v47\n"
    "v_dot2_f32_bf16 v50, v100, v100, v50\n"
    "v_dot2_f32_bf16 v51, v101, v101, v51\n"
    "v_dot2_f32_bf16 v50, v102, v102, v50\n"
    "v_dot2_f32_bf16 v51, v103, v103, v51\n"
    "s_waitcnt vmcnt(25)\n"
    "v_dot2_f32_bf16 v44, v104, s92, v44\n"
    "v_dot2_f32_bf16 v45, v105, s92, v45\n"
    "v_dot2_f32_bf16 v44, v106, s92, v44\n"
    "v_dot2_f32_bf16 v45, v107, s92, v45\n"
    "v_dot2_f32_bf16 v48, v104, v104, v48\n"
    "v_dot2_f32_bf16 v49, v105, v105, v49\n"
    "v_dot2_f32_bf16 v48, v106, v106, v48\n"
    "v_dot2_f32_bf16 v49, v107, v107, v49\n"
    "s_waitcnt vmcnt(24)\n"
    "v_dot2_f32_bf16 v46, v108, s92, v46\n"
    "v_dot2_f32_bf16 v47, v109, s92, v47\n"
    "v_dot2_f32_bf16 v46, v110, s92, v46\n"
    "v_dot2_f32_bf16 v47, v111, s92, v47\n"
    "v_dot2_f32_bf16 v50, v108, v108, v50\n"
    "v_dot2_f32_bf16 v51, v109, v109, v51\n"
    "v_dot2_f32_bf16 v50, v110, v110, v50\n"
    "v_dot2_f32_bf16 v51, v111, v111, v51\n"
    "s_waitcnt vmcnt(23)\n"
    "v_dot2_f32_bf16 v44, v112, s92, v44\n"
    "v_dot2_f32_bf16 v45, v113, s92, v45\n"
    "v_dot2_f32_bf16 v44, v114, s92, v44\n"
    "v_dot2_f32_bf16 v45, v115, s92, v45\n"
    "v_dot2_f32_bf16 v48, v112, v112, v48\n"
    "v_dot2_f32_bf16 v49, v113, v113, v49\n"
    "v_dot2_f32_bf16 v48, v114, v114, v48\n"
    "v_dot2_f32_bf16 v49, v115, v115, v49\n"
    "s_waitcnt vmcnt(22)\n"
    "v_dot2_f32_bf16 v46, v116, s92, v46\n"
    "v_dot2_f32_bf16 v47, v117, s92, v47\n"
    "v_dot2_f32_bf16 v46, v118, s92, v46\n"
    "v_dot2_f32_bf16 v47, v119, s92, v47\n"
    "v_dot2_f32_bf16 v50, v116, v116, v50\n"
    "v_dot2_f32_bf16 v51, v117, v117, v51\n"
    "v_dot2_f32_bf16 v50, v118, v118, v50\n"
    "v_dot2_f32_bf16 v51, v119, v119, v51\n"
    "s_waitcnt vmcnt(21)\n"
    "v_dot2_f32_bf16 v44, v120, s92, v44\n"
    "v_dot2_f32_bf16 v45, v121, s92, v45\n"
    "v_dot2_f32_bf16 v44, v122, s92, v44\n"
    "v_dot2_f32_bf16 v45, v123, s92, v45\n"
    "v_dot2_f32_bf16 v48, v120, v120, v48\n"
    "v_dot2_f32_bf16 v49, v121, v121, v49\n"
    "v_dot2_f32_bf16 v48, v122, v122, v48\n"
    "v_dot2_f32_bf16 v49, v123, v123, v49\n"
    "s_waitcnt vmcnt(20)\n"
    "v_dot2_f32_bf16 v46, v124, s92, v46\n"
    "v_dot2_f32_bf16 v47, v125, s92, v47\n"
    "v_dot2_f32_bf16 v46, v126, s92, v46\n"
    "v_dot2_f32_bf16 v47, v127, s92, v47\n"
    "v_dot2_f32_bf16 v50, v124, v124, v50\n"
    "v_dot2_f32_bf16 v51, v125, v125, v51\n"
    "v_dot2_f32_bf16 v50, v126, v126, v50\n"
    "v_dot2_f32_bf16 v51, v127, v127, v51\n"
    "v_lshrrev_b32 v42, 4, v1\n"
    "v_lshlrev_b32 v42, 13, v42\n"
    "v_and_b32 v43, 15, v1\n"
    "v_lshl_add_u32 v42, v43, 6, v42\n"
    "v_add_u32 v42, 0x1000, v42\n"
    "s_lshl_b32 s37, s34, 2\n"
    "s_add_u32 s37, s37, s32\n"
    "s_lshl_b32 s37, s37, 15\n"
    "s_lshl_b32 s38, s33, 10\n"
    "s_add_u32 s37, s37, s38\n"
    "buffer_load_dword v130, v42, s[52:55], s37 offen\n"
    "v_lshlrev_b32 v42, 6, v1\n"
    "v_lshrrev_b32 v43, 4, v1\n"
    "v_min_u32 v43, 1, v43\n"
    "v_lshlrev_b32 v43, 30, v43\n"
    "v_add_u32 v42, v42, v43\n"
    "s_lshl_b32 s39, s34, 12\n"
    "s_lshl_b32 s97, s32, 10\n"
    "s_add_u32 s39, s39, s97\n"
    "buffer_load_dword v131, v42, s[56:59], s39 offen\n"
    "s_add_u32 s39, s39, 0x1000\n"
    "buffer_load_dword v132, v42, s[60:63], s39 offen\n"
    "v_add_f32 v44, v44, v45\n"
    "v_add_f32 v46, v46, v47\n"
    "v_add_f32 v48, v48, v49\n"
    "v_add_f32 v50, v50, v51\n"
    "v_add_f32_dpp v44, v44, v44 quad_perm:[1,0,3,2] row_mask:0xf bank_mask:0xf\n"
    "v_add_f32_dpp v46, v46, v46 quad_perm:[1,0,3,2] row_mask:0xf bank_mask:0xf\n"
    "v_add_f32_dpp v48, v48, v48 quad_perm:[1,0,3,2] row_mask:0xf bank_mask:0xf\n"
    "v_add_f32_dpp v50, v50, v50 quad_perm:[1,0,3,2] row_mask:0xf bank_mask:0xf\n"
    "v_add_f32_dpp v44, v44, v44 quad_perm:[2,3,0,1] row_mask:0xf bank_mask:0xf\n"
    "v_add_f32_dpp v46, v46, v46 quad_perm:[2,3,0,1] row_mask:0xf bank_mask:0xf\n"
    "v_add_f32_dpp v48, v48, v48 quad_perm:[2,3,0,1] row_mask:0xf bank_mask:0xf\n"
    "v_add_f32_dpp v50, v50, v50 quad_perm:[2,3,0,1] row_mask:0xf bank_mask:0xf\n"
    "v_add_f32_dpp v44, v44, v44 row_half_mirror row_mask:0xf bank_mask:0xf\n"
    "v_add_f32_dpp v46, v46, v46 row_half_mirror row_mask:0xf bank_mask:0xf\n"
    "v_add_f32_dpp v48, v48, v48 row_half_mirror row_mask:0xf bank_mask:0xf\n"
    "v_add_f32_dpp v50, v50, v50 row_half_mirror row_mask:0xf bank_mask:0xf\n"
    "v_mul_f32 v44, 0x3b000000, v44\n"
    "v_mul_f32 v48, 0x3b000000, v48\n"
    "v_mul_f32 v46, 0x3b000000, v46\n"
    "v_mul_f32 v50, 0x3b000000, v50\n"
    "v_fma_f32 v48, -v44, v44, v48\n"
    "v_fma_f32 v50, -v46, v46, v50\n"
    "v_max_f32 v48, 0, v48\n"
    "v_max_f32 v50, 0, v50\n"
    "v_cvt_pk_bf16_f32 v44, v44, v44\n"
    "v_cvt_pk_bf16_f32 v46, v46, v46\n"
    "v_cvt_pk_bf16_f32 v48, v48, v48\n"
    "v_cvt_pk_bf16_f32 v50, v50, v50\n"
    "v_lshlrev_b32 v44, 16, v44\n"
    "v_lshlrev_b32 v46, 16, v46\n"
    "v_lshlrev_b32 v48, 16, v48\n"
    "v_lshlrev_b32 v50, 16, v50\n"
    "v_sub_f32 v52, 0, v44\n"
    "v_add_f32 v48, 0x3727c5ac, v48\n"
    "v_sub_f32 v53, 0, v46\n"
    "v_add_f32 v50, 0x3727c5ac, v50\n"
    "v_cvt_pk_bf16_f32 v48, v48, v48\n"
    "v_cvt_pk_bf16_f32 v50, v50, v50\n"
    "v_lshlrev_b32 v48, 16, v48\n"
    "v_lshlrev_b32 v50, 16, v50\n"
    "v_sqrt_f32 v48, v48\n"
    "v_sqrt_f32 v50, v50\n"
    "v_cvt_pk_bf16_f32 v48, v48, v48\n"
    "v_cvt_pk_bf16_f32 v50, v50, v50\n"
    "v_lshlrev_b32 v48, 16, v48\n"
    "v_lshlrev_b32 v50, 16, v50\n"
    "v_lshrrev_b32 v40, 3, v1\n"
    "s_lshl_b32 s37, s32, 4\n"
    "v_add_u32 v40, s37, v40\n"
    "v_lshlrev_b32 v40, 2, v40\n"
    "v_add_u32 v40, 0x10000, v40\n"
    "ds_write_b32 v40, v48\n"
    "ds_write_b32 v40, v50 offset:32\n"
    "v_and_b32 v41, 7, v1\n"
    "v_lshrrev_b32 v42, 2, v41\n"
    "v_lshlrev_b32 v42, 11, v42\n"
    "v_and_b32 v43, 1, v41\n"
    "v_lshl_add_u32 v42, v43, 9, v42\n"
    "v_bfe_u32 v54, v41, 1, 1\n"
    "v_lshl_add_u32 v42, v54, 3, v42\n"
    "v_lshlrev_b32 v43, 2, v43\n"
    "v_lshrrev_b32 v54, 2, v41\n"
    "v_lshl_add_u32 v43, v54, 1, v43\n"
    "v_lshrrev_b32 v54, 3, v1\n"
    "v_xor_b32 v43, v43, v54\n"
    "v_lshl_add_u32 v42, v43, 4, v42\n"
    "s_lshr_b32 s37, s32, 1\n"
    "s_lshl_b32 s37, s37, 15\n"
    "s_and_b32 s38, s32, 1\n"
    "s_lshl_b32 s38, s38, 10\n"
    "s_add_u32 s37, s37, s38\n"
    "v_add_u32 v42, s37, v42\n"
    "v_dot2_f32_bf16 v44, v64, s93, v52\n"
    "v_dot2_f32_bf16 v45, v65, s93, v52\n"
    "v_dot2_f32_bf16 v46, v66, s93, v52\n"
    "v_dot2_f32_bf16 v47, v67, s93, v52\n"
    "v_dot2_f32_bf16 v54, v64, s94, v52\n"
    "v_dot2_f32_bf16 v55, v65, s94, v52\n"
    "v_dot2_f32_bf16 v56, v66, s94, v52\n"
    "v_dot2_f32_bf16 v57, v67, s94, v52\n"
    "v_cvt_pk_bf16_f32 v64, v44, v54\n"
    "v_cvt_pk_bf16_f32 v65, v45, v55\n"
    "v_cvt_pk_bf16_f32 v66, v46, v56\n"
    "v_cvt_pk_bf16_f32 v67, v47, v57\n"
    "ds_write_b64 v42, v[64:65]\n"
    "ds_write_b64 v42, v[66:67] offset:256\n"
    "v_dot2_f32_bf16 v44, v68, s93, v53\n"
    "v_dot2_f32_bf16 v45, v69, s93, v53\n"
    "v_dot2_f32_bf16 v46, v70, s93, v53\n"
    "v_dot2_f32_bf16 v47, v71, s93, v53\n"
    "v_dot2_f32_bf16 v54, v68, s94, v53\n"
    "v_dot2_f32_bf16 v55, v69, s94, v53\n"
    "v_dot2_f32_bf16 v56, v70, s94, v53\n"
    "v_dot2_f32_bf16 v57, v71, s94, v53\n"
    "v_cvt_pk_bf16_f32 v68, v44, v54\n"
    "v_cvt_pk_bf16_f32 v69, v45, v55\n"
    "v_cvt_pk_bf16_f32 v70, v46, v56\n"
    "v_cvt_pk_bf16_f32 v71, v47, v57\n"
    "ds_write_b64 v42, v[68:69] offset:128\n"
    "ds_write_b64 v42, v[70:71] offset:384\n"
    "v_dot2_f32_bf16 v44, v72, s93, v52\n"
    "v_dot2_f32_bf16 v45, v73, s93, v52\n"
    "v_dot2_f32_bf16 v46, v74, s93, v52\n"
    "v_dot2_f32_bf16 v47, v75, s93, v52\n"
    "v_dot2_f32_bf16 v54, v72, s94, v52\n"
    "v_dot2_f32_bf16 v55, v73, s94, v52\n"
    "v_dot2_f32_bf16 v56, v74, s94, v52\n"
    "v_dot2_f32_bf16 v57, v75, s94, v52\n"
    "v_cvt_pk_bf16_f32 v72, v44, v54\n"
    "v_cvt_pk_bf16_f32 v73, v45, v55\n"
    "v_cvt_pk_bf16_f32 v74, v46, v56\n"
    "v_cvt_pk_bf16_f32 v75, v47, v57\n"
    "ds_write_b64 v42, v[72:73] offset:4096\n"
    "ds_write_b64 v42, v[74:75] offset:4352\n"
    "v_dot2_f32_bf16 v44, v76, s93, v53\n"
    "v_dot2_f32_bf16 v45, v77, s93, v53\n"
    "v_dot2_f32_bf16 v46, v78, s93, v53\n"
    "v_dot2_f32_bf16 v47, v79, s93, v53\n"
    "v_dot2_f32_bf16 v54, v76, s94, v53\n"
    "v_dot2_f32_bf16 v55, v77, s94, v53\n"
    "v_dot2_f32_bf16 v56, v78, s94, v53\n"
    "v_dot2_f32_bf16 v57, v79, s94, v53\n"
    "v_cvt_pk_bf16_f32 v76, v44, v54\n"
    "v_cvt_pk_bf16_f32 v77, v45, v55\n"
    "v_cvt_pk_bf16_f32 v78, v46, v56\n"
    "v_cvt_pk_bf16_f32 v79, v47, v57\n"
    "ds_write_b64 v42, v[76:77] offset:4224\n"
    "ds_write_b64 v42, v[78:79] offset:4480\n"
    "v_dot2_f32_bf16 v44, v80, s93, v52\n"
    "v_dot2_f32_bf16 v45, v81, s93, v52\n"
    "v_dot2_f32_bf16 v46, v82, s93, v52\n"
    "v_dot2_f32_bf16 v47, v83, s93, v52\n"
    "v_dot2_f32_bf16 v54, v80, s94, v52\n"
    "v_dot2_f32_bf16 v55, v81, s94, v52\n"
    "v_dot2_f32_bf16 v56, v82, s94, v52\n"
    "v_dot2_f32_bf16 v57, v83, s94, v52\n"
    "v_cvt_pk_bf16_f32 v80, v44, v54\n"
    "v_cvt_pk_bf16_f32 v81, v45, v55\n"
    "v_cvt_pk_bf16_f32 v82, v46, v56\n"
    "v_cvt_pk_bf16_f32 v83, v47, v57\n"
    "ds_write_b64 v42, v[80:81] offset:8192\n"
    "ds_write_b64 v42, v[82:83] offset:8448\n"
    "v_dot2_f32_bf16 v44, v84, s93, v53\n"
    "v_dot2_f32_bf16 v45, v85, s93, v53\n"
    "v_dot2_f32_bf16 v46, v86, s93, v53\n"
    "v_dot2_f32_bf16 v47, v87, s93, v53\n"
    "v_dot2_f32_bf16 v54, v84, s94, v53\n"
    "v_dot2_f32_bf16 v55, v85, s94, v53\n"
    "v_dot2_f32_bf16 v56, v86, s94, v53\n"
    "v_dot2_f32_bf16 v57, v87, s94, v53\n"
    "v_cvt_pk_bf16_f32 v84, v44, v54\n"
    "v_cvt_pk_bf16_f32 v85, v45, v55\n"
    "v_cvt_pk_bf16_f32 v86, v46, v56\n"
    "v_cvt_pk_bf16_f32 v87, v47, v57\n"
    "ds_write_b64 v42, v[84:85] offset:8320\n"
    "ds_write_b64 v42, v[86:87] offset:8576\n"
    "v_dot2_f32_bf16 v44, v88, s93, v52\n"
    "v_dot2_f32_bf16 v45, v89, s93, v52\n"
    "v_dot2_f32_bf16 v46, v90, s93, v52\n"
    "v_dot2_f32_bf16 v47, v91, s93, v52\n"
    "v_dot2_f32_bf16 v54, v88, s94, v52\n"
    "v_dot2_f32_bf16 v55, v89, s94, v52\n"
    "v_dot2_f32_bf16 v56, v90, s94, v52\n"
    "v_dot2_f32_bf16 v57, v91, s94, v52\n"
    "v_cvt_pk_bf16_f32 v88, v44, v54\n"
    "v_cvt_pk_bf16_f32 v89, v45, v55\n"
    "v_cvt_pk_bf16_f32 v90, v46, v56\n"
    "v_cvt_pk_bf16_f32 v91, v47, v57\n"
    "ds_write_b64 v42, v[88:89] offset:12288\n"
    "s_waitcnt lgkmcnt(1)\n"
    "ds_write_b64 v42, v[90:91] offset:12544\n"
    "v_dot2_f32_bf16 v44, v92, s93, v53\n"
    "v_dot2_f32_bf16 v45, v93, s93, v53\n"
    "v_dot2_f32_bf16 v46, v94, s93, v53\n"
    "v_dot2_f32_bf16 v47, v95, s93, v53\n"
    "v_dot2_f32_bf16 v54, v92, s94, v53\n"
    "v_dot2_f32_bf16 v55, v93, s94, v53\n"
    "v_dot2_f32_bf16 v56, v94, s94, v53\n"
    "v_dot2_f32_bf16 v57, v95, s94, v53\n"
    "v_cvt_pk_bf16_f32 v92, v44, v54\n"
    "v_cvt_pk_bf16_f32 v93, v45, v55\n"
    "v_cvt_pk_bf16_f32 v94, v46, v56\n"
    "v_cvt_pk_bf16_f32 v95, v47, v57\n"
    "ds_write_b64 v42, v[92:93] offset:12416\n"
    "ds_write_b64 v42, v[94:95] offset:12672\n"
    "v_dot2_f32_bf16 v44, v96, s93, v52\n"
    "v_dot2_f32_bf16 v45, v97, s93, v52\n"
    "v_dot2_f32_bf16 v46, v98, s93, v52\n"
    "v_dot2_f32_bf16 v47, v99, s93, v52\n"
    "v_dot2_f32_bf16 v54, v96, s94, v52\n"
    "v_dot2_f32_bf16 v55, v97, s94, v52\n"
    "v_dot2_f32_bf16 v56, v98, s94, v52\n"
    "v_dot2_f32_bf16 v57, v99, s94, v52\n"
    "v_cvt_pk_bf16_f32 v96, v44, v54\n"
    "v_cvt_pk_bf16_f32 v97, v45, v55\n"
    "v_cvt_pk_bf16_f32 v98, v46, v56\n"
    "v_cvt_pk_bf16_f32 v99, v47, v57\n"
    "ds_write_b64 v42, v[96:97] offset:16384\n"
    "ds_write_b64 v42, v[98:99] offset:16640\n"
    "v_dot2_f32_bf16 v44, v100, s93, v53\n"
    "v_dot2_f32_bf16 v45, v101, s93, v53\n"
    "v_dot2_f32_bf16 v46, v102, s93, v53\n"
    "v_dot2_f32_bf16 v47, v103, s93, v53\n"
    "v_dot2_f32_bf16 v54, v100, s94, v53\n"
    "v_dot2_f32_bf16 v55, v101, s94, v53\n"
    "v_dot2_f32_bf16 v56, v102, s94, v53\n"
    "v_dot2_f32_bf16 v57, v103, s94, v53\n"
    "v_cvt_pk_bf16_f32 v100, v44, v54\n"
    "v_cvt_pk_bf16_f32 v101, v45, v55\n"
    "v_cvt_pk_bf16_f32 v102, v46, v56\n"
    "v_cvt_pk_bf16_f32 v103, v47, v57\n"
    "ds_write_b64 v42, v[100:101] offset:16512\n"
    "ds_write_b64 v42, v[102:103] offset:16768\n"
    "v_dot2_f32_bf16 v44, v104, s93, v52\n"
    "v_dot2_f32_bf16 v45, v105, s93, v52\n"
    "v_dot2_f32_bf16 v46, v106, s93, v52\n"
    "v_dot2_f32_bf16 v47, v107, s93, v52\n"
    "v_dot2_f32_bf16 v54, v104, s94, v52\n"
    "v_dot2_f32_bf16 v55, v105, s94, v52\n"
    "v_dot2_f32_bf16 v56, v106, s94, v52\n"
    "v_dot2_f32_bf16 v57, v107, s94, v52\n"
    "v_cvt_pk_bf16_f32 v104, v44, v54\n"
    "v_cvt_pk_bf16_f32 v105, v45, v55\n"
    "v_cvt_pk_bf16_f32 v106, v46, v56\n"
    "v_cvt_pk_bf16_f32 v107, v47, v57\n"
    "ds_write_b64 v42, v[104:105] offset:20480\n"
    "ds_write_b64 v42, v[106:107] offset:20736\n"
    "v_dot2_f32_bf16 v44, v108, s93, v53\n"
    "v_dot2_f32_bf16 v45, v109, s93, v53\n"
    "v_dot2_f32_bf16 v46, v110, s93, v53\n"
    "v_dot2_f32_bf16 v47, v111, s93, v53\n"
    "v_dot2_f32_bf16 v54, v108, s94, v53\n"
    "v_dot2_f32_bf16 v55, v109, s94, v53\n"
    "v_dot2_f32_bf16 v56, v110, s94, v53\n"
    "v_dot2_f32_bf16 v57, v111, s94, v53\n"
    "v_cvt_pk_bf16_f32 v108, v44, v54\n"
    "v_cvt_pk_bf16_f32 v109, v45, v55\n"
    "v_cvt_pk_bf16_f32 v110, v46, v56\n"
    "v_cvt_pk_bf16_f32 v111, v47, v57\n"
    "ds_write_b64 v42, v[108:109] offset:20608\n"
    "ds_write_b64 v42, v[110:111] offset:20864\n"
    "v_dot2_f32_bf16 v44, v112, s93, v52\n"
    "v_dot2_f32_bf16 v45, v113, s93, v52\n"
    "v_dot2_f32_bf16 v46, v114, s93, v52\n"
    "v_dot2_f32_bf16 v47, v115, s93, v52\n"
    "v_dot2_f32_bf16 v54, v112, s94, v52\n"
    "v_dot2_f32_bf16 v55, v113, s94, v52\n"
    "v_dot2_f32_bf16 v56, v114, s94, v52\n"
    "v_dot2_f32_bf16 v57, v115, s94, v52\n"
    "v_cvt_pk_bf16_f32 v112, v44, v54\n"
    "v_cvt_pk_bf16_f32 v113, v45, v55\n"
    "v_cvt_pk_bf16_f32 v114, v46, v56\n"
    "v_cvt_pk_bf16_f32 v115, v47, v57\n"
    "ds_write_b64 v42, v[112:113] offset:24576\n"
    "ds_write_b64 v42, v[114:115] offset:24832\n"
    "v_dot2_f32_bf16 v44, v116, s93, v53\n"
    "v_dot2_f32_bf16 v45, v117, s93, v53\n"
    "v_dot2_f32_bf16 v46, v118, s93, v53\n"
    "v_dot2_f32_bf16 v47, v119, s93, v53\n"
    "v_dot2_f32_bf16 v54, v116, s94, v53\n"
    "v_dot2_f32_bf16 v55, v117, s94, v53\n"
    "v_dot2_f32_bf16 v56, v118, s94, v53\n"
    "v_dot2_f32_bf16 v57, v119, s94, v53\n"
    "v_cvt_pk_bf16_f32 v116, v44, v54\n"
    "v_cvt_pk_bf16_f32 v117, v45, v55\n"
    "v_cvt_pk_bf16_f32 v118, v46, v56\n"
    "v_cvt_pk_bf16_f32 v119, v47, v57\n"
    "ds_write_b64 v42, v[116:117] offset:24704\n"
    "s_waitcnt lgkmcnt(1)\n"
    "ds_write_b64 v42, v[118:119] offset:24960\n"
    "v_dot2_f32_bf16 v44, v120, s93, v52\n"
    "v_dot2_f32_bf16 v45, v121, s93, v52\n"
    "v_dot2_f32_bf16 v46, v122, s93, v52\n"
    "v_dot2_f32_bf16 v47, v123, s93, v52\n"
    "v_dot2_f32_bf16 v54, v120, s94, v52\n"
    "v_dot2_f32_bf16 v55, v121, s94, v52\n"
    "v_dot2_f32_bf16 v56, v122, s94, v52\n"
    "v_dot2_f32_bf16 v57, v123, s94, v52\n"
    "v_cvt_pk_bf16_f32 v120, v44, v54\n"
    "v_cvt_pk_bf16_f32 v121, v45, v55\n"
    "v_cvt_pk_bf16_f32 v122, v46, v56\n"
    "v_cvt_pk_bf16_f32 v123, v47, v57\n"
    "ds_write_b64 v42, v[120:121] offset:28672\n"
    "ds_write_b64 v42, v[122:123] offset:28928\n"
    "v_dot2_f32_bf16 v44, v124, s93, v53\n"
    "v_dot2_f32_bf16 v45, v125, s93, v53\n"
    "v_dot2_f32_bf16 v46, v126, s93, v53\n"
    "v_dot2_f32_bf16 v47, v127, s93, v53\n"
    "v_dot2_f32_bf16 v54, v124, s94, v53\n"
    "v_dot2_f32_bf16 v55, v125, s94, v53\n"
    "v_dot2_f32_bf16 v56, v126, s94, v53\n"
    "v_dot2_f32_bf16 v57, v127, s94, v53\n"
    "v_cvt_pk_bf16_f32 v124, v44, v54\n"
    "v_cvt_pk_bf16_f32 v125, v45, v55\n"
    "v_cvt_pk_bf16_f32 v126, v46, v56\n"
    "v_cvt_pk_bf16_f32 v127, v47, v57\n"
    "ds_write_b64 v42, v[124:125] offset:28800\n"
    "ds_write_b64 v42, v[126:127] offset:29056\n"
    "v_lshrrev_b32 v41, 5, v1\n"
    "v_lshlrev_b32 v41, 2, v41\n"
    "v_and_b32 v43, 15, v1\n"
    "v_xor_b32 v41, v41, v43\n"
    "v_and_b32 v43, 48, v1\n"
    "v_add_u32 v40, v41, v43\n"
    "v_lshlrev_b32 v62, 4, v40\n"
    "v_xor_b32 v63, 32, v62\n"
    "s_waitcnt lgkmcnt(0)\n"
    "s_barrier\n"
    "v_and_b32 v40, 15, v1\n"
    "v_lshlrev_b32 v40, 2, v40\n"
    "v_add_u32 v40, 0x10000, v40\n"
    "ds_read_b32 v56, v40\n"
    "ds_read_b32 v57, v40 offset:64\n"
    "ds_read_b32 v58, v40 offset:128\n"
    "ds_read_b32 v59, v40 offset:192\n"
    "; PHASE T1\n"
    "ds_read_b128 a[160:163], v62\n"
    "s_waitcnt vmcnt(18)\n"
    "ds_read_b64_tr_b16 a[128:129], v3\n"
    "s_waitcnt vmcnt(14)\n"
    "ds_read_b64_tr_b16 a[130:131], v3 offset:4096\n"
    "ds_read_b128 a[168:171], v62 offset:1024\n"
    "ds_read_b128 a[164:167], v62 offset:32768\n"
    "ds_read_b128 a[172:175], v62 offset:33792\n"
    "ds_read_b64_tr_b16 a[132:133], v4\n"
    "ds_read_b64_tr_b16 a[134:135], v4 offset:4096\n"
    "ds_read_b64_tr_b16 a[136:137], v3 offset:512\n"
    "ds_read_b64_tr_b16 a[138:139], v3 offset:4608\n"
    "ds_read_b64_tr_b16 a[140:141], v4 offset:512\n"
    "s_waitcnt lgkmcnt(11)\n"
    "ds_read_b64_tr_b16 a[142:143], v4 offset:4608\n"
    "ds_read_b64_tr_b16 a[144:145], v3 offset:1024\n"
    "ds_read_b64_tr_b16 a[146:147], v3 offset:5120\n"
    "ds_read_b64_tr_b16 a[148:149], v4 offset:1024\n"
    "s_waitcnt lgkmcnt(12)\n"
    "ds_read_b64_tr_b16 a[150:151], v4 offset:5120\n"
    "ds_read_b64_tr_b16 a[152:153], v3 offset:1536\n"
    "ds_read_b64_tr_b16 a[154:155], v3 offset:5632\n"
    "s_waitcnt lgkmcnt(11)\n"
    "ds_read_b64_tr_b16 a[156:157], v4 offset:1536\n"
    "ds_read_b64_tr_b16 a[158:159], v4 offset:5632\n"
    "v_mfma_f32_16x16x32_bf16 a[0:3], a[128:131], a[160:163], 0\n"
    "v_mfma_f32_16x16x32_bf16 a[8:11], a[128:131], a[168:171], 0\n"
    "v_mfma_f32_16x16x32_bf16 a[16:19], a[128:131], a[164:167], 0\n"
    "v_rcp_f32 v52, v56\n"
    "v_mul_f32 v48, v56, v56\n"
    "s_waitcnt lgkmcnt(0)\n"
    "s_barrier\n"
    "v_mfma_f32_16x16x32_bf16 a[24:27], a[128:131], a[172:175], 0\n"
    "s_add_u32 m0, s91, 0x10000\n"
    "s_nop 0\n"
    "buffer_load_dwordx4 v25, s[44:47], s95 offen lds\n"
    "buffer_load_dwordx4 v26, s[48:51], s95 offen offset:1024 lds\n"
    "v_rcp_f32 v53, v57\n"
    "v_mul_f32 v49, v57, v57\n"
    "v_rcp_f32 v54, v58\n"
    "v_mul_f32 v50, v58, v58\n"
    "v_mfma_f32_16x16x32_bf16 a[4:7], a[132:135], a[160:163], 0\n"
    "s_waitcnt vmcnt(12)\n"
    "ds_read_b64_tr_b16 a[128:129], v3 offset:8192\n"
    "buffer_load_dwordx4 v27, s[44:47], s95 offen offset:2048 lds\n"
    "buffer_load_dwordx4 v28, s[48:51], s95 offen offset:3072 lds\n"
    "s_add_u32 s95, s95, 0xe000\n"
    "s_add_u32 m0, s91, 0x14000\n"
    "s_nop 0\n"
    "buffer_load_dwordx4 v25, s[44:47], s95 offen lds\n"
    "v_rcp_f32 v55, v59\n"
    "v_mfma_f32_16x16x32_bf16 a[12:15], a[132:135], a[168:171], 0\n"
    "s_waitcnt vmcnt(11)\n"
    "ds_read_b64_tr_b16 a[130:131], v3 offset:12288\n"
    "buffer_load_dwordx4 v26, s[48:51], s95 offen offset:1024 lds\n"
    "buffer_load_dwordx4 v27, s[44:47], s95 offen offset:2048 lds\n"
    "buffer_load_dwordx4 v28, s[48:51], s95 offen offset:3072 lds\n"
    "s_add_u32 s95, s95, 0xe000\n"
    "v_mul_f32 v51, v59, v59\n"
    "v_mul_f32 v40, 0xc0135761, v52\n"
    "v_mfma_f32_16x16x32_bf16 a[20:23], a[132:135], a[164:167], 0\n"
    "ds_read_b128 a[176:179], v63 offset:2048\n"
    "s_add_u32 m0, s89, 0\n"
    "v_mfma_f32_16x16x32_bf16 a[28:31], a[132:135], a[172:175], 0\n"
    "buffer_load_dwordx4 v25, s[44:47], s88 offen lds\n"
    "v_mul_f32 v44, v52, v52\n"
    "v_mul_f32 v44, v44, v52\n"
    "v_mfma_f32_16x16x32_bf16 a[32:35], a[136:139], a[160:163], 0\n"
    "ds_read_b64_tr_b16 a[132:133], v4 offset:8192\n"
    "v_mul_f32 v44, 0xbdd2d3e8, v44\n"
    "v_mul_f32 v52, 0x3e0293ee, v52\n"
    "v_mfma_f32_16x16x32_bf16 a[40:43], a[136:139], a[168:171], 0\n"
    "ds_read_b64_tr_b16 a[134:135], v4 offset:12288\n"
    "v_mul_f32 v41, 0xc0135761, v53\n"
    "v_mul_f32 v45, v53, v53\n"
    "v_mfma_f32_16x16x32_bf16 a[48:51], a[136:139], a[164:167], 0\n"
    "ds_read_b128 a[184:187], v63 offset:3072\n"
    "buffer_load_dwordx4 v26, s[48:51], s88 offen offset:1024 lds\n"
    "v_mfma_f32_16x16x32_bf16 a[56:59], a[136:139], a[172:175], 0\n"
    "v_mul_f32 v45, v45, v53\n"
    "v_mul_f32 v45, 0xbdd2d3e8, v45\n"
    "v_mul_f32 v53, 0x3e0293ee, v53\n"
    "v_mul_f32 v42, 0xc0135761, v54\n"
    "v_mfma_f32_16x16x32_bf16 a[36:39], a[140:143], a[160:163], 0\n"
    "ds_read_b64_tr_b16 a[136:137], v3 offset:8704\n"
    "v_mul_f32 v46, v54, v54\n"
    "v_mul_f32 v46, v46, v54\n"
    "v_mul_f32 v46, 0xbdd2d3e8, v46\n"
    "v_mfma_f32_16x16x32_bf16 a[44:47], a[140:143], a[168:171], 0\n"
    "ds_read_b64_tr_b16 a[138:139], v3 offset:12800\n"
    "buffer_load_dwordx4 v27, s[44:47], s88 offen offset:2048 lds\n"
    "v_mfma_f32_16x16x32_bf16 a[52:55], a[140:143], a[164:167], 0\n"
    "ds_read_b128 a[180:183], v63 offset:34816\n"
    "v_mul_f32 v54, 0x3e0293ee, v54\n"
    "v_mul_f32 v43, 0xc0135761, v55\n"
    "v_mfma_f32_16x16x32_bf16 a[60:63], a[140:143], a[172:175], 0\n"
    "v_mul_f32 v47, v55, v55\n"
    "v_mul_f32 v47, v47, v55\n"
    "v_mul_f32 v47, 0xbdd2d3e8, v47\n"
    "v_mul_f32 v55, 0x3e0293ee, v55\n"
    "v_mfma_f32_16x16x32_bf16 a[64:67], a[144:147], a[160:163], 0\n"
    "ds_read_b64_tr_b16 a[140:141], v4 offset:8704\n"
    "buffer_load_dwordx4 v28, s[48:51], s88 offen offset:3072 lds\n"
    "v_mfma_f32_16x16x32_bf16 a[72:75], a[144:147], a[168:171], 0\n"
    "ds_read_b64_tr_b16 a[142:143], v4 offset:12800\n"
    "s_add_u32 s88, s88, 0xe000\n"
    "v_mfma_f32_16x16x32_bf16 a[80:83], a[144:147], a[164:167], 0\n"
    "ds_read_b128 a[188:191], v63 offset:35840\n"
    "v_mfma_f32_16x16x32_bf16 a[88:91], a[144:147], a[172:175], 0\n"
    "s_add_u32 m0, s89, 0x1000\n"
    "v_mfma_f32_16x16x32_bf16 a[68:71], a[148:151], a[160:163], 0\n"
    "ds_read_b64_tr_b16 a[144:145], v3 offset:9216\n"
    "buffer_load_dwordx4 v25, s[44:47], s88 offen lds\n"
    "v_mfma_f32_16x16x32_bf16 a[76:79], a[148:151], a[168:171], 0\n"
    "ds_read_b64_tr_b16 a[146:147], v3 offset:13312\n"
    "v_mfma_f32_16x16x32_bf16 a[84:87], a[148:151], a[164:167], 0\n"
    "v_mfma_f32_16x16x32_bf16 a[92:95], a[148:151], a[172:175], 0\n"
    "buffer_load_dwordx4 v26, s[48:51], s88 offen offset:1024 lds\n"
    "v_mfma_f32_16x16x32_bf16 a[96:99], a[152:155], a[160:163], 0\n"
    "ds_read_b64_tr_b16 a[148:149], v4 offset:9216\n"
    "v_mfma_f32_16x16x32_bf16 a[104:107], a[152:155], a[168:171], 0\n"
    "s_waitcnt lgkmcnt(3)\n"
    "ds_read_b64_tr_b16 a[150:151], v4 offset:13312\n"
    "v_mfma_f32_16x16x32_bf16 a[112:115], a[152:155], a[164:167], 0\n"
    "buffer_load_dwordx4 v27, s[44:47], s88 offen offset:2048 lds\n"
    "v_mfma_f32_16x16x32_bf16 a[120:123], a[152:155], a[172:175], 0\n"
    "v_mfma_f32_16x16x32_bf16 a[100:103], a[156:159], a[160:163], 0\n"
    "ds_read_b64_tr_b16 a[152:153], v3 offset:9728\n"
    "v_mfma_f32_16x16x32_bf16 a[108:111], a[156:159], a[168:171], 0\n"
    "ds_read_b64_tr_b16 a[154:155], v3 offset:13824\n"
    "buffer_load_dwordx4 v28, s[48:51], s88 offen offset:3072 lds\n"
    "v_mfma_f32_16x16x32_bf16 a[116:119], a[156:159], a[164:167], 0\n"
    "s_add_u32 s88, s88, 0xe000\n"
    "v_mfma_f32_16x16x32_bf16 a[124:127], a[156:159], a[172:175], 0\n"
    "v_mfma_f32_16x16x32_bf16 a[0:3], a[128:131], a[176:179], a[0:3]\n"
    "ds_read_b64_tr_b16 a[156:157], v4 offset:9728\n"
    "v_mfma_f32_16x16x32_bf16 a[8:11], a[128:131], a[184:187], a[8:11]\n"
    "ds_read_b64_tr_b16 a[158:159], v4 offset:13824\n"
    "v_mfma_f32_16x16x32_bf16 a[16:19], a[128:131], a[180:183], a[16:19]\n"
    "v_mfma_f32_16x16x32_bf16 a[24:27], a[128:131], a[188:191], a[24:27]\n"
    "v_mfma_f32_16x16x32_bf16 a[4:7], a[132:135], a[176:179], a[4:7]\n"
    "s_waitcnt vmcnt(12)\n"
    "ds_read_b64_tr_b16 a[128:129], v5\n"
    "v_mfma_f32_16x16x32_bf16 a[12:15], a[132:135], a[184:187], a[12:15]\n"
    "s_waitcnt vmcnt(8)\n"
    "ds_read_b64_tr_b16 a[130:131], v5 offset:16384\n"
    "v_mfma_f32_16x16x32_bf16 a[20:23], a[132:135], a[180:183], a[20:23]\n"
    "ds_read_b128 a[160:163], v62 offset:4096\n"
    "s_add_u32 m0, s89, 0x2000\n"
    "v_mfma_f32_16x16x32_bf16 a[28:31], a[132:135], a[188:191], a[28:31]\n"
    "s_waitcnt lgkmcnt(4)\n"
    "buffer_load_dwordx4 v25, s[44:47], s88 offen lds\n"
    "s_sub_u32 s64, s64, 0x1000\n"
    "s_subb_u32 s65, s65, 0\n"
    "v_mfma_f32_16x16x32_bf16 a[32:35], a[136:139], a[176:179], a[32:35]\n"
    "ds_read_b64_tr_b16 a[132:133], v6\n"
    "v_mfma_f32_16x16x32_bf16 a[40:43], a[136:139], a[184:187], a[40:43]\n"
    "ds_read_b64_tr_b16 a[134:135], v6 offset:16384\n"
    "s_and_b32 s65, s65, 0xffff\n"
    "v_mfma_f32_16x16x32_bf16 a[48:51], a[136:139], a[180:183], a[48:51]\n"
    "ds_read_b128 a[168:171], v62 offset:5120\n"
    "buffer_load_dwordx4 v26, s[48:51], s88 offen offset:1024 lds\n"
    "v_mfma_f32_16x16x32_bf16 a[56:59], a[136:139], a[188:191], a[56:59]\n"
    "s_mov_b32 s66, 0x401000\n"
    "v_mfma_f32_16x16x32_bf16 a[36:39], a[140:143], a[176:179], a[36:39]\n"
    "ds_read_b64_tr_b16 a[136:137], v5 offset:512\n"
    "v_mfma_f32_16x16x32_bf16 a[44:47], a[140:143], a[184:187], a[44:47]\n"
    "ds_read_b64_tr_b16 a[138:139], v5 offset:16896\n"
    "buffer_load_dwordx4 v27, s[44:47], s88 offen offset:2048 lds\n"
    "v_mfma_f32_16x16x32_bf16 a[52:55], a[140:143], a[180:183], a[52:55]\n"
    "ds_read_b128 a[164:167], v62 offset:36864\n"
    "s_mov_b32 s67, 0x20000\n"
    "v_mfma_f32_16x16x32_bf16 a[60:63], a[140:143], a[188:191], a[60:63]\n"
    "v_mfma_f32_16x16x32_bf16 a[64:67], a[144:147], a[176:179], a[64:67]\n"
    "ds_read_b64_tr_b16 a[140:141], v6 offset:512\n"
    "buffer_load_dwordx4 v28, s[48:51], s88 offen offset:3072 lds\n"
    "v_mfma_f32_16x16x32_bf16 a[72:75], a[144:147], a[184:187], a[72:75]\n"
    "ds_read_b64_tr_b16 a[142:143], v6 offset:16896\n"
    "s_add_u32 s88, s88, 0xe000\n"
    "v_mfma_f32_16x16x32_bf16 a[80:83], a[144:147], a[180:183], a[80:83]\n"
    "ds_read_b128 a[172:175], v62 offset:37888\n"
    "s_sub_u32 s68, s68, 0x1000\n"
    "s_subb_u32 s69, s69, 0\n"
    "v_mfma_f32_16x16x32_bf16 a[88:91], a[144:147], a[188:191], a[88:91]\n"
    "s_add_u32 m0, s89, 0x3000\n"
    "s_and_b32 s69, s69, 0xffff\n"
    "v_mfma_f32_16x16x32_bf16 a[68:71], a[148:151], a[176:179], a[68:71]\n"
    "ds_read_b64_tr_b16 a[144:145], v5 offset:1024\n"
    "s_waitcnt lgkmcnt(3)\n"
    "buffer_load_dwordx4 v25, s[44:47], s88 offen lds\n"
    "v_mfma_f32_16x16x32_bf16 a[76:79], a[148:151], a[184:187], a[76:79]\n"
    "ds_read_b64_tr_b16 a[146:147], v5 offset:17408\n"
    "v_mfma_f32_16x16x32_bf16 a[84:87], a[148:151], a[180:183], a[84:87]\n"
    "s_mov_b32 s70, 0x1c1000\n"
    "v_mfma_f32_16x16x32_bf16 a[92:95], a[148:151], a[188:191], a[92:95]\n"
    "buffer_load_dwordx4 v26, s[48:51], s88 offen offset:1024 lds\n"
    "s_mov_b32 s71, 0x20000\n"
    "v_mfma_f32_16x16x32_bf16 a[96:99], a[152:155], a[176:179], a[96:99]\n"
    "ds_read_b64_tr_b16 a[148:149], v6 offset:1024\n"
    "v_mfma_f32_16x16x32_bf16 a[104:107], a[152:155], a[184:187], a[104:107]\n"
    "ds_read_b64_tr_b16 a[150:151], v6 offset:17408\n"
    "v_mfma_f32_16x16x32_bf16 a[112:115], a[152:155], a[180:183], a[112:115]\n"
    "buffer_load_dwordx4 v27, s[44:47], s88 offen offset:2048 lds\n"
    "s_add_u32 s72, s72, 0x10000\n"
    "s_addc_u32 s73, s73, 0\n"
    "v_mfma_f32_16x16x32_bf16 a[120:123], a[152:155], a[188:191], a[120:123]\n"
    "s_and_b32 s73, s73, 0xffff\n"
    "v_mfma_f32_16x16x32_bf16 a[100:103], a[156:159], a[176:179], a[100:103]\n"
    "ds_read_b64_tr_b16 a[152:153], v5 offset:1536\n"
    "v_mfma_f32_16x16x32_bf16 a[108:111], a[156:159], a[184:187], a[108:111]\n"
    "ds_read_b64_tr_b16 a[154:155], v5 offset:17920\n"
    "buffer_load_dwordx4 v28, s[48:51], s88 offen offset:3072 lds\n"
    "v_mfma_f32_16x16x32_bf16 a[116:119], a[156:159], a[180:183], a[116:119]\n"
    "s_add_u32 s88, s88, 0xe000\n"
    "s_mov_b32 s74, 0x700000\n"
    "v_mfma_f32_16x16x32_bf16 a[124:127], a[156:159], a[188:191], a[124:127]\n"
    "s_mov_b32 s75, 0x20000\n"
    "v_mfma_f32_16x16x32_bf16 a[0:3], a[128:131], a[160:163], a[0:3]\n"
    "ds_read_b64_tr_b16 a[156:157], v6 offset:1536\n"
    "v_mfma_f32_16x16x32_bf16 a[8:11], a[128:131], a[168:171], a[8:11]\n"
    "ds_read_b64_tr_b16 a[158:159], v6 offset:17920\n"
    "s_add_u32 s76, s76, 0x710000\n"
    "s_addc_u32 s77, s77, 0\n"
    "v_mfma_f32_16x16x32_bf16 a[16:19], a[128:131], a[164:167], a[16:19]\n"
    "s_and_b32 s77, s77, 0xffff\n"
    "s_waitcnt lgkmcnt(2)\n"
    "v_mfma_f32_16x16x32_bf16 a[24:27], a[128:131], a[172:175], a[24:27]\n"
    "s_mov_b32 s78, 0x1000000\n"
    "v_mfma_f32_16x16x32_bf16 a[4:7], a[132:135], a[160:163], a[4:7]\n"
    "s_waitcnt vmcnt(12)\n"
    "ds_read_b64_tr_b16 a[128:129], v3\n"
    "v_mfma_f32_16x16x32_bf16 a[12:15], a[132:135], a[168:171], a[12:15]\n"
    "s_waitcnt vmcnt(8)\n"
    "ds_read_b64_tr_b16 a[130:131], v3 offset:4096\n"
    "s_mov_b32 s79, 0x20000\n"
    "v_mfma_f32_16x16x32_bf16 a[20:23], a[132:135], a[164:167], a[20:23]\n"
    "ds_read_b128 a[176:179], v63 offset:6144\n"
    "s_add_u32 m0, s91, 0x10000\n"
    "v_mfma_f32_16x16x32_bf16 a[28:31], a[132:135], a[172:175], a[28:31]\n"
    "s_waitcnt lgkmcnt(3)\n"
    "buffer_load_dwordx4 v25, s[44:47], s88 offen lds\n"
    "s_and_b32 s81, s81, 0xffff\n"
    "v_mfma_f32_16x16x32_bf16 a[32:35], a[136:139], a[160:163], a[32:35]\n"
    "ds_read_b64_tr_b16 a[132:133], v4\n"
    "v_mfma_f32_16x16x32_bf16 a[40:43], a[136:139], a[168:171], a[40:43]\n"
    "ds_read_b64_tr_b16 a[134:135], v4 offset:4096\n"
    "s_mov_b32 s82, 0x200000\n"
    "v_mfma_f32_16x16x32_bf16 a[48:51], a[136:139], a[164:167], a[48:51]\n"
    "ds_read_b128 a[184:187], v63 offset:7168\n"
    "buffer_load_dwordx4 v26, s[48:51], s88 offen offset:1024 lds\n"
    "v_mfma_f32_16x16x32_bf16 a[56:59], a[136:139], a[172:175], a[56:59]\n"
    "s_mov_b32 s83, 0x20000\n"
    "v_mfma_f32_16x16x32_bf16 a[36:39], a[140:143], a[160:163], a[36:39]\n"
    "ds_read_b64_tr_b16 a[136:137], v3 offset:512\n"
    "v_mfma_f32_16x16x32_bf16 a[44:47], a[140:143], a[168:171], a[44:47]\n"
    "ds_read_b64_tr_b16 a[138:139], v3 offset:4608\n"
    "buffer_load_dwordx4 v27, s[44:47], s88 offen offset:2048 lds\n"
    "v_mfma_f32_16x16x32_bf16 a[52:55], a[140:143], a[164:167], a[52:55]\n"
    "ds_read_b128 a[180:183], v63 offset:38912\n"
    "s_lshr_b32 s37, s34, 3\n"
    "v_mfma_f32_16x16x32_bf16 a[60:63], a[140:143], a[172:175], a[60:63]\n"
    "s_lshl_b32 s35, s33, 2\n"
    "v_mfma_f32_16x16x32_bf16 a[64:67], a[144:147], a[160:163], a[64:67]\n"
    "ds_read_b64_tr_b16 a[140:141], v4 offset:512\n"
    "buffer_load_dwordx4 v28, s[48:51], s88 offen offset:3072 lds\n"
    "v_mfma_f32_16x16x32_bf16 a[72:75], a[144:147], a[168:171], a[72:75]\n"
    "ds_read_b64_tr_b16 a[142:143], v4 offset:4608\n"
    "s_add_u32 s88, s88, 0xe000\n"
    "v_mfma_f32_16x16x32_bf16 a[80:83], a[144:147], a[164:167], a[80:83]\n"
    "ds_read_b128 a[188:191], v63 offset:39936\n"
    "s_add_u32 s35, s35, s37\n"
    "v_mfma_f32_16x16x32_bf16 a[88:91], a[144:147], a[172:175], a[88:91]\n"
    "s_add_u32 m0, s91, 0x14000\n"
    "s_and_b32 s36, s34, 7\n"
    "v_mfma_f32_16x16x32_bf16 a[68:71], a[148:151], a[160:163], a[68:71]\n"
    "ds_read_b64_tr_b16 a[144:145], v3 offset:1024\n"
    "buffer_load_dwordx4 v25, s[44:47], s88 offen lds\n"
    "v_mfma_f32_16x16x32_bf16 a[76:79], a[148:151], a[168:171], a[76:79]\n"
    "ds_read_b64_tr_b16 a[146:147], v3 offset:5120\n"
    "v_mfma_f32_16x16x32_bf16 a[84:87], a[148:151], a[164:167], a[84:87]\n"
    "v_add_u32 v7, 0x14000, v9\n"
    "v_mfma_f32_16x16x32_bf16 a[92:95], a[148:151], a[172:175], a[92:95]\n"
    "buffer_load_dwordx4 v26, s[48:51], s88 offen offset:1024 lds\n"
    "v_add_u32 v8, 0x14000, v10\n"
    "v_mfma_f32_16x16x32_bf16 a[96:99], a[152:155], a[160:163], a[96:99]\n"
    "ds_read_b64_tr_b16 a[148:149], v4 offset:1024\n"
    "v_mfma_f32_16x16x32_bf16 a[104:107], a[152:155], a[168:171], a[104:107]\n"
    "s_waitcnt lgkmcnt(3)\n"
    "ds_read_b64_tr_b16 a[150:151], v4 offset:5120\n"
    "v_and_b32 v56, 15, v1\n"
    "v_mfma_f32_16x16x32_bf16 a[112:115], a[152:155], a[164:167], a[112:115]\n"
    "buffer_load_dwordx4 v27, s[44:47], s88 offen offset:2048 lds\n"
    "v_lshrrev_b32 v57, 4, v1\n"
    "v_mfma_f32_16x16x32_bf16 a[120:123], a[152:155], a[172:175], a[120:123]\n"
    "v_xor_b32 v57, v57, v56\n"
    "v_mfma_f32_16x16x32_bf16 a[100:103], a[156:159], a[160:163], a[100:103]\n"
    "ds_read_b64_tr_b16 a[152:153], v3 offset:1536\n"
    "v_mfma_f32_16x16x32_bf16 a[108:111], a[156:159], a[168:171], a[108:111]\n"
    "ds_read_b64_tr_b16 a[154:155], v3 offset:5632\n"
    "buffer_load_dwordx4 v28, s[48:51], s88 offen offset:3072 lds\n"
    "v_mfma_f32_16x16x32_bf16 a[116:119], a[156:159], a[164:167], a[116:119]\n"
    "s_add_u32 s88, s88, 0xe000\n"
    "v_lshlrev_b32 v57, 4, v57\n"
    "v_mfma_f32_16x16x32_bf16 a[124:127], a[156:159], a[172:175], a[124:127]\n"
    "v_lshl_add_u32 v57, v56, 8, v57\n"
    "v_mfma_f32_16x16x32_bf16 a[0:3], a[128:131], a[176:179], a[0:3]\n"
    "ds_read_b64_tr_b16 a[156:157], v4 offset:1536\n"
    "v_add_u32 v11, 0x8000, v57\n"
    "v_mfma_f32_16x16x32_bf16 a[8:11], a[128:131], a[184:187], a[8:11]\n"
    "ds_read_b64_tr_b16 a[158:159], v4 offset:5632\n"
    "v_xor_b32 v12, 64, v11\n"
    "v_mfma_f32_16x16x32_bf16 a[16:19], a[128:131], a[180:183], a[16:19]\n"
    "v_xor_b32 v13, 0x80, v11\n"
    "v_mfma_f32_16x16x32_bf16 a[24:27], a[128:131], a[188:191], a[24:27]\n"
    "v_xor_b32 v14, 0xc0, v11\n"
    "v_mfma_f32_16x16x32_bf16 a[4:7], a[132:135], a[176:179], a[4:7]\n"
    "s_waitcnt vmcnt(12)\n"
    "ds_read_b64_tr_b16 a[128:129], v3 offset:8192\n"
    "v_mfma_f32_16x16x32_bf16 a[12:15], a[132:135], a[184:187], a[12:15]\n"
    "s_waitcnt vmcnt(8)\n"
    "ds_read_b64_tr_b16 a[130:131], v3 offset:12288\n"
    "v_lshrrev_b32 v56, 5, v1\n"
    "v_mfma_f32_16x16x32_bf16 a[20:23], a[132:135], a[180:183], a[20:23]\n"
    "ds_read_b128 a[160:163], v62 offset:8192\n"
    "s_add_u32 m0, s89, 0\n"
    "v_mfma_f32_16x16x32_bf16 a[28:31], a[132:135], a[188:191], a[28:31]\n"
    "s_waitcnt lgkmcnt(3)\n"
    "buffer_load_dwordx4 v25, s[44:47], s88 offen lds\n"
    "v_mfma_f32_16x16x32_bf16 a[32:35], a[136:139], a[176:179], a[32:35]\n"
    "ds_read_b64_tr_b16 a[132:133], v4 offset:8192\n"
    "v_lshlrev_b32 v56, 11, v56\n"
    "v_mfma_f32_16x16x32_bf16 a[40:43], a[136:139], a[184:187], a[40:43]\n"
    "ds_read_b64_tr_b16 a[134:135], v4 offset:12288\n"
    "v_bfe_u32 v57, v1, 2, 3\n"
    "v_mfma_f32_16x16x32_bf16 a[48:51], a[136:139], a[180:183], a[48:51]\n"
    "ds_read_b128 a[168:171], v62 offset:9216\n"
    "buffer_load_dwordx4 v26, s[48:51], s88 offen offset:1024 lds\n"
    "v_mfma_f32_16x16x32_bf16 a[56:59], a[136:139], a[188:191], a[56:59]\n"
    "v_lshl_add_u32 v56, v57, 6, v56\n"
    "v_mfma_f32_16x16x32_bf16 a[36:39], a[140:143], a[176:179], a[36:39]\n"
    "ds_read_b64_tr_b16 a[136:137], v3 offset:8704\n"
    "v_mfma_f32_16x16x32_bf16 a[44:47], a[140:143], a[184:187], a[44:47]\n"
    "ds_read_b64_tr_b16 a[138:139], v3 offset:12800\n"
    "buffer_load_dwordx4 v27, s[44:47], s88 offen offset:2048 lds\n"
    "v_mfma_f32_16x16x32_bf16 a[52:55], a[140:143], a[180:183], a[52:55]\n"
    "ds_read_b128 a[164:167], v62 offset:40960\n"
    "v_lshrrev_b32 v57, 3, v1\n"
    "v_mfma_f32_16x16x32_bf16 a[60:63], a[140:143], a[188:191], a[60:63]\n"
    "v_and_b32 v57, 2, v57\n"
    "v_mfma_f32_16x16x32_bf16 a[64:67], a[144:147], a[176:179], a[64:67]\n"
    "ds_read_b64_tr_b16 a[140:141], v4 offset:8704\n"
    "buffer_load_dwordx4 v28, s[48:51], s88 offen offset:3072 lds\n"
    "v_mfma_f32_16x16x32_bf16 a[72:75], a[144:147], a[184:187], a[72:75]\n"
    "ds_read_b64_tr_b16 a[142:143], v4 offset:12800\n"
    "s_add_u32 s88, s88, 0xe000\n"
    "v_mfma_f32_16x16x32_bf16 a[80:83], a[144:147], a[180:183], a[80:83]\n"
    "ds_read_b128 a[172:175], v62 offset:41984\n"
    "v_mfma_f32_16x16x32_bf16 a[88:91], a[144:147], a[188:191], a[88:91]\n"
    "s_add_u32 m0, s89, 0x1000\n"
    "v_xor_b32 v57, v57, v1\n"
    "v_and_b32 v57, 3, v57\n"
    "v_mfma_f32_16x16x32_bf16 a[68:71], a[148:151], a[176:179], a[68:71]\n"
    "ds_read_b64_tr_b16 a[144:145], v3 offset:9216\n"
    "buffer_load_dwordx4 v25, s[44:47], s88 offen lds\n"
    "v_mfma_f32_16x16x32_bf16 a[76:79], a[148:151], a[184:187], a[76:79]\n"
    "ds_read_b64_tr_b16 a[146:147], v3 offset:13312\n"
    "v_mfma_f32_16x16x32_bf16 a[84:87], a[148:151], a[180:183], a[84:87]\n"
    "v_lshl_add_u32 v56, v57, 4, v56\n"
    "v_mfma_f32_16x16x32_bf16 a[92:95], a[148:151], a[188:191], a[92:95]\n"
    "buffer_load_dwordx4 v26, s[48:51], s88 offen offset:1024 lds\n"
    "v_add_u32 v15, s89, v56\n"
    "v_mfma_f32_16x16x32_bf16 a[96:99], a[152:155], a[176:179], a[96:99]\n"
    "ds_read_b64_tr_b16 a[148:149], v4 offset:9216\n"
    "v_mfma_f32_16x16x32_bf16 a[104:107], a[152:155], a[184:187], a[104:107]\n"
    "s_waitcnt lgkmcnt(3)\n"
    "ds_read_b64_tr_b16 a[150:151], v4 offset:13312\n"
    "v_add_u32 v16, s91, v56\n"
    "v_mfma_f32_16x16x32_bf16 a[112:115], a[152:155], a[180:183], a[112:115]\n"
    "buffer_load_dwordx4 v27, s[44:47], s88 offen offset:2048 lds\n"
    "v_mfma_f32_16x16x32_bf16 a[120:123], a[152:155], a[188:191], a[120:123]\n"
    "v_add_u32 v16, 0x10000, v16\n"
    "v_mfma_f32_16x16x32_bf16 a[100:103], a[156:159], a[176:179], a[100:103]\n"
    "ds_read_b64_tr_b16 a[152:153], v3 offset:9728\n"
    "v_mov_b32 v34, 0x3f803f80\n"
    "v_mfma_f32_16x16x32_bf16 a[108:111], a[156:159], a[184:187], a[108:111]\n"
    "ds_read_b64_tr_b16 a[154:155], v3 offset:13824\n"
    "buffer_load_dwordx4 v28, s[48:51], s88 offen offset:3072 lds\n"
    "v_mfma_f32_16x16x32_bf16 a[116:119], a[156:159], a[180:183], a[116:119]\n"
    "s_add_u32 s88, s88, 0xe000\n"
    "v_mov_b32 v35, 0x3f803f80\n"
    "v_mfma_f32_16x16x32_bf16 a[124:127], a[156:159], a[188:191], a[124:127]\n"
    "v_mov_b32 v36, 0x3f803f80\n"
    "v_mfma_f32_16x16x32_bf16 a[0:3], a[128:131], a[160:163], a[0:3]\n"
    "ds_read_b64_tr_b16 a[156:157], v4 offset:9728\n"
    "v_mov_b32 v37, 0x3f803f80\n"
    "v_mfma_f32_16x16x32_bf16 a[8:11], a[128:131], a[168:171], a[8:11]\n"
    "ds_read_b64_tr_b16 a[158:159], v4 offset:13824\n"
    "v_mfma_f32_16x16x32_bf16 a[16:19], a[128:131], a[164:167], a[16:19]\n"
    "v_lshrrev_b32 v56, 4, v1\n"
    "v_mfma_f32_16x16x32_bf16 a[24:27], a[128:131], a[172:175], a[24:27]\n"
    "v_and_b32 v57, 15, v1\n"
    "v_add_u32 v58, 0, v56\n"
    "v_mfma_f32_16x16x32_bf16 a[4:7], a[132:135], a[160:163], a[4:7]\n"
    "s_waitcnt vmcnt(12)\n"
    "ds_read_b64_tr_b16 a[128:129], v5\n"
    "v_mfma_f32_16x16x32_bf16 a[12:15], a[132:135], a[168:171], a[12:15]\n"
    "s_waitcnt vmcnt(8)\n"
    "ds_read_b64_tr_b16 a[130:131], v5 offset:16384\n"
    "v_xor_b32 v58, v58, v57\n"
    "v_mfma_f32_16x16x32_bf16 a[20:23], a[132:135], a[164:167], a[20:23]\n"
    "ds_read_b128 a[176:179], v63 offset:10240\n"
    "s_add_u32 m0, s89, 0x2000\n"
    "v_mfma_f32_16x16x32_bf16 a[28:31], a[132:135], a[172:175], a[28:31]\n"
    "s_waitcnt lgkmcnt(3)\n"
    "buffer_load_dwordx4 v25, s[44:47], s88 offen lds\n"
    "v_mfma_f32_16x16x32_bf16 a[32:35], a[136:139], a[160:163], a[32:35]\n"
    "ds_read_b64_tr_b16 a[132:133], v6\n"
    "v_lshlrev_b32 v58, 4, v58\n"
    "v_mfma_f32_16x16x32_bf16 a[40:43], a[136:139], a[168:171], a[40:43]\n"
    "ds_read_b64_tr_b16 a[134:135], v6 offset:16384\n"
    "v_mfma_f32_16x16x32_bf16 a[48:51], a[136:139], a[164:167], a[48:51]\n"
    "ds_read_b128 a[184:187], v63 offset:11264\n"
    "buffer_load_dwordx4 v26, s[48:51], s88 offen offset:1024 lds\n"
    "v_mfma_f32_16x16x32_bf16 a[56:59], a[136:139], a[172:175], a[56:59]\n"
    "v_lshl_add_u32 v19, v56, 8, v58\n"
    "v_mfma_f32_16x16x32_bf16 a[36:39], a[140:143], a[160:163], a[36:39]\n"
    "ds_read_b64_tr_b16 a[136:137], v5 offset:512\n"
    "v_add_u32 v58, 4, v56\n"
    "v_mfma_f32_16x16x32_bf16 a[44:47], a[140:143], a[168:171], a[44:47]\n"
    "ds_read_b64_tr_b16 a[138:139], v5 offset:16896\n"
    "buffer_load_dwordx4 v27, s[44:47], s88 offen offset:2048 lds\n"
    "v_mfma_f32_16x16x32_bf16 a[52:55], a[140:143], a[164:167], a[52:55]\n"
    "ds_read_b128 a[180:183], v63 offset:43008\n"
    "v_xor_b32 v58, v58, v57\n"
    "v_mfma_f32_16x16x32_bf16 a[60:63], a[140:143], a[172:175], a[60:63]\n"
    "v_lshlrev_b32 v58, 4, v58\n"
    "v_mfma_f32_16x16x32_bf16 a[64:67], a[144:147], a[160:163], a[64:67]\n"
    "ds_read_b64_tr_b16 a[140:141], v6 offset:512\n"
    "buffer_load_dwordx4 v28, s[48:51], s88 offen offset:3072 lds\n"
    "v_mfma_f32_16x16x32_bf16 a[72:75], a[144:147], a[168:171], a[72:75]\n"
    "ds_read_b64_tr_b16 a[142:143], v6 offset:16896\n"
    "s_add_u32 s88, s88, 0xe000\n"
    "v_mfma_f32_16x16x32_bf16 a[80:83], a[144:147], a[164:167], a[80:83]\n"
    "ds_read_b128 a[188:191], v63 offset:44032\n"
    "v_mfma_f32_16x16x32_bf16 a[88:91], a[144:147], a[172:175], a[88:91]\n"
    "s_add_u32 m0, s89, 0x3000\n"
    "v_lshl_add_u32 v20, v56, 8, v58\n"
    "v_mfma_f32_16x16x32_bf16 a[68:71], a[148:151], a[160:163], a[68:71]\n"
    "ds_read_b64_tr_b16 a[144:145], v5 offset:1024\n"
    "buffer_load_dwordx4 v25, s[44:47], s88 offen lds\n"
    "v_mfma_f32_16x16x32_bf16 a[76:79], a[148:151], a[168:171], a[76:79]\n"
    "ds_read_b64_tr_b16 a[146:147], v5 offset:17408\n"
    "v_add_u32 v58, 8, v56\n"
    "v_mfma_f32_16x16x32_bf16 a[84:87], a[148:151], a[164:167], a[84:87]\n"
    "v_xor_b32 v58, v58, v57\n"
    "v_mfma_f32_16x16x32_bf16 a[92:95], a[148:151], a[172:175], a[92:95]\n"
    "buffer_load_dwordx4 v26, s[48:51], s88 offen offset:1024 lds\n"
    "v_lshlrev_b32 v58, 4, v58\n"
    "v_mfma_f32_16x16x32_bf16 a[96:99], a[152:155], a[160:163], a[96:99]\n"
    "ds_read_b64_tr_b16 a[148:149], v6 offset:1024\n"
    "v_mfma_f32_16x16x32_bf16 a[104:107], a[152:155], a[168:171], a[104:107]\n"
    "s_waitcnt lgkmcnt(3)\n"
    "ds_read_b64_tr_b16 a[150:151], v6 offset:17408\n"
    "v_lshl_add_u32 v21, v56, 8, v58\n"
    "v_mfma_f32_16x16x32_bf16 a[112:115], a[152:155], a[164:167], a[112:115]\n"
    "buffer_load_dwordx4 v27, s[44:47], s88 offen offset:2048 lds\n"
    "v_mfma_f32_16x16x32_bf16 a[120:123], a[152:155], a[172:175], a[120:123]\n"
    "v_add_u32 v58, 12, v56\n"
    "v_mfma_f32_16x16x32_bf16 a[100:103], a[156:159], a[160:163], a[100:103]\n"
    "ds_read_b64_tr_b16 a[152:153], v5 offset:1536\n"
    "v_xor_b32 v58, v58, v57\n"
    "v_mfma_f32_16x16x32_bf16 a[108:111], a[156:159], a[168:171], a[108:111]\n"
    "ds_read_b64_tr_b16 a[154:155], v5 offset:17920\n"
    "buffer_load_dwordx4 v28, s[48:51], s88 offen offset:3072 lds\n"
    "v_mfma_f32_16x16x32_bf16 a[116:119], a[156:159], a[164:167], a[116:119]\n"
    "s_add_u32 s88, s88, 0xe000\n"
    "v_lshlrev_b32 v58, 4, v58\n"
    "v_mfma_f32_16x16x32_bf16 a[124:127], a[156:159], a[172:175], a[124:127]\n"
    "v_lshl_add_u32 v22, v56, 8, v58\n"
    "v_mfma_f32_16x16x32_bf16 a[0:3], a[128:131], a[176:179], a[0:3]\n"
    "ds_read_b64_tr_b16 a[156:157], v6 offset:1536\n"
    "v_bfe_u32 v58, v1, 2, 3\n"
    "v_mfma_f32_16x16x32_bf16 a[8:11], a[128:131], a[184:187], a[8:11]\n"
    "ds_read_b64_tr_b16 a[158:159], v6 offset:17920\n"
    "v_mfma_f32_16x16x32_bf16 a[16:19], a[128:131], a[180:183], a[16:19]\n"
    "v_mul_u32_u24 v57, 0x100, v58\n"
    "v_mfma_f32_16x16x32_bf16 a[24:27], a[128:131], a[188:191], a[24:27]\n"
    "v_lshrrev_b32 v58, 5, v1\n"
    "v_mfma_f32_16x16x32_bf16 a[4:7], a[132:135], a[176:179], a[4:7]\n"
    "s_waitcnt vmcnt(12)\n"
    "ds_read_b64_tr_b16 a[128:129], v3\n"
    "v_lshl_add_u32 v57, v58, 6, v57\n"
    "v_mfma_f32_16x16x32_bf16 a[12:15], a[132:135], a[184:187], a[12:15]\n"
    "s_waitcnt vmcnt(8)\n"
    "ds_read_b64_tr_b16 a[130:131], v3 offset:4096\n"
    "v_lshrrev_b32 v58, 3, v1\n"
    "v_mfma_f32_16x16x32_bf16 a[20:23], a[132:135], a[180:183], a[20:23]\n"
    "ds_read_b128 a[160:163], v62 offset:12288\n"
    "s_add_u32 m0, s91, 0x10000\n"
    "v_mfma_f32_16x16x32_bf16 a[28:31], a[132:135], a[188:191], a[28:31]\n"
    "s_waitcnt lgkmcnt(3)\n"
    "buffer_load_dwordx4 v25, s[44:47], s88 offen lds\n"
    "v_mfma_f32_16x16x32_bf16 a[32:35], a[136:139], a[176:179], a[32:35]\n"
    "ds_read_b64_tr_b16 a[132:133], v4\n"
    "v_and_b32 v58, 2, v58\n"
    "v_mfma_f32_16x16x32_bf16 a[40:43], a[136:139], a[184:187], a[40:43]\n"
    "ds_read_b64_tr_b16 a[134:135], v4 offset:4096\n"
    "v_mfma_f32_16x16x32_bf16 a[48:51], a[136:139], a[180:183], a[48:51]\n"
    "ds_read_b128 a[168:171], v62 offset:13312\n"
    "buffer_load_dwordx4 v26, s[48:51], s88 offen offset:1024 lds\n"
    "v_mfma_f32_16x16x32_bf16 a[56:59], a[136:139], a[188:191], a[56:59]\n"
    "v_xor_b32 v58, v58, v1\n"
    "v_mfma_f32_16x16x32_bf16 a[36:39], a[140:143], a[176:179], a[36:39]\n"
    "ds_read_b64_tr_b16 a[136:137], v3 offset:512\n"
    "v_and_b32 v58, 3, v58\n"
    "v_mfma_f32_16x16x32_bf16 a[44:47], a[140:143], a[184:187], a[44:47]\n"
    "ds_read_b64_tr_b16 a[138:139], v3 offset:4608\n"
    "buffer_load_dwordx4 v27, s[44:47], s88 offen offset:2048 lds\n"
    "v_mfma_f32_16x16x32_bf16 a[52:55], a[140:143], a[180:183], a[52:55]\n"
    "ds_read_b128 a[164:167], v62 offset:45056\n"
    "v_mfma_f32_16x16x32_bf16 a[60:63], a[140:143], a[188:191], a[60:63]\n"
    "v_lshl_add_u32 v57, v58, 4, v57\n"
    "v_add_u32 v23, 0x1000, v57\n"
    "v_mfma_f32_16x16x32_bf16 a[64:67], a[144:147], a[176:179], a[64:67]\n"
    "ds_read_b64_tr_b16 a[140:141], v4 offset:512\n"
    "buffer_load_dwordx4 v28, s[48:51], s88 offen offset:3072 lds\n"
    "v_mfma_f32_16x16x32_bf16 a[72:75], a[144:147], a[184:187], a[72:75]\n"
    "ds_read_b64_tr_b16 a[142:143], v4 offset:4608\n"
    "s_add_u32 s88, s88, 0xe000\n"
    "v_mfma_f32_16x16x32_bf16 a[80:83], a[144:147], a[180:183], a[80:83]\n"
    "ds_read_b128 a[172:175], v62 offset:46080\n"
    "v_mfma_f32_16x16x32_bf16 a[88:91], a[144:147], a[188:191], a[88:91]\n"
    "s_add_u32 m0, s91, 0x14000\n"
    "v_add_u32 v24, 0xc80, v57\n"
    "v_mfma_f32_16x16x32_bf16 a[68:71], a[148:151], a[176:179], a[68:71]\n"
    "ds_read_b64_tr_b16 a[144:145], v3 offset:1024\n"
    "buffer_load_dwordx4 v25, s[44:47], s88 offen lds\n"
    "v_mfma_f32_16x16x32_bf16 a[76:79], a[148:151], a[184:187], a[76:79]\n"
    "ds_read_b64_tr_b16 a[146:147], v3 offset:5120\n"
    "v_bfe_u32 v58, v1, 2, 3\n"
    "v_mfma_f32_16x16x32_bf16 a[84:87], a[148:151], a[180:183], a[84:87]\n"
    "v_mul_u32_u24 v57, 0x2000, v58\n"
    "v_mfma_f32_16x16x32_bf16 a[92:95], a[148:151], a[188:191], a[92:95]\n"
    "buffer_load_dwordx4 v26, s[48:51], s88 offen offset:1024 lds\n"
    "v_mfma_f32_16x16x32_bf16 a[96:99], a[152:155], a[176:179], a[96:99]\n"
    "ds_read_b64_tr_b16 a[148:149], v4 offset:1024\n"
    "v_lshrrev_b32 v58, 5, v1\n"
    "v_mfma_f32_16x16x32_bf16 a[104:107], a[152:155], a[184:187], a[104:107]\n"
    "s_waitcnt lgkmcnt(3)\n"
    "ds_read_b64_tr_b16 a[150:151], v4 offset:5120\n"
    "v_lshl_add_u32 v57, v58, 6, v57\n"
    "v_mfma_f32_16x16x32_bf16 a[112:115], a[152:155], a[180:183], a[112:115]\n"
    "buffer_load_dwordx4 v27, s[44:47], s88 offen offset:2048 lds\n"
    "v_mfma_f32_16x16x32_bf16 a[120:123], a[152:155], a[188:191], a[120:123]\n"
    "v_lshrrev_b32 v58, 3, v1\n"
    "v_mfma_f32_16x16x32_bf16 a[100:103], a[156:159], a[176:179], a[100:103]\n"
    "ds_read_b64_tr_b16 a[152:153], v3 offset:1536\n"
    "v_and_b32 v58, 2, v58\n"
    "v_mfma_f32_16x16x32_bf16 a[108:111], a[156:159], a[184:187], a[108:111]\n"
    "ds_read_b64_tr_b16 a[154:155], v3 offset:5632\n"
    "buffer_load_dwordx4 v28, s[48:51], s88 offen offset:3072 lds\n"
    "v_mfma_f32_16x16x32_bf16 a[116:119], a[156:159], a[180:183], a[116:119]\n"
    "s_add_u32 s88, s88, 0xe000\n"
    "v_xor_b32 v58, v58, v1\n"
    "v_mfma_f32_16x16x32_bf16 a[124:127], a[156:159], a[188:191], a[124:127]\n"
    "v_and_b32 v58, 3, v58\n"
    "v_mfma_f32_16x16x32_bf16 a[0:3], a[128:131], a[160:163], a[0:3]\n"
    "ds_read_b64_tr_b16 a[156:157], v4 offset:1536\n"
    "v_lshl_add_u32 v57, v58, 4, v57\n"
    "v_mfma_f32_16x16x32_bf16 a[8:11], a[128:131], a[168:171], a[8:11]\n"
    "ds_read_b64_tr_b16 a[158:159], v4 offset:5632\n"
    "v_mfma_f32_16x16x32_bf16 a[16:19], a[128:131], a[164:167], a[16:19]\n"
    "v_add_u32 v29, 0x1000, v57\n"
    "v_mfma_f32_16x16x32_bf16 a[24:27], a[128:131], a[172:175], a[24:27]\n"
    "v_add_u32 v30, 0xc80, v57\n"
    "v_mfma_f32_16x16x32_bf16 a[4:7], a[132:135], a[160:163], a[4:7]\n"
    "s_waitcnt vmcnt(12)\n"
    "ds_read_b64_tr_b16 a[128:129], v3 offset:8192\n"
    "v_add_u32 v31, 0x10800, v57\n"
    "v_mfma_f32_16x16x32_bf16 a[12:15], a[132:135], a[168:171], a[12:15]\n"
    "s_waitcnt vmcnt(8)\n"
    "ds_read_b64_tr_b16 a[130:131], v3 offset:12288\n"
    "v_mfma_f32_16x16x32_bf16 a[20:23], a[132:135], a[164:167], a[20:23]\n"
    "ds_read_b128 a[176:179], v63 offset:14336\n"
    "s_add_u32 m0, s89, 0\n"
    "v_mfma_f32_16x16x32_bf16 a[28:31], a[132:135], a[172:175], a[28:31]\n"
    "s_waitcnt lgkmcnt(3)\n"
    "buffer_load_dwordx4 v25, s[44:47], s88 offen lds\n"
    "v_add_u32 v32, 0x10480, v57\n"
    "v_mfma_f32_16x16x32_bf16 a[32:35], a[136:139], a[160:163], a[32:35]\n"
    "ds_read_b64_tr_b16 a[132:133], v4 offset:8192\n"
    "v_mfma_f32_16x16x32_bf16 a[40:43], a[136:139], a[168:171], a[40:43]\n"
    "ds_read_b64_tr_b16 a[134:135], v4 offset:12288\n"
    "v_mfma_f32_16x16x32_bf16 a[48:51], a[136:139], a[164:167], a[48:51]\n"
    "ds_read_b128 a[184:187], v63 offset:15360\n"
    "buffer_load_dwordx4 v26, s[48:51], s88 offen offset:1024 lds\n"
    "v_mfma_f32_16x16x32_bf16 a[56:59], a[136:139], a[172:175], a[56:59]\n"
    "v_mfma_f32_16x16x32_bf16 a[36:39], a[140:143], a[160:163], a[36:39]\n"
    "ds_read_b64_tr_b16 a[136:137], v3 offset:8704\n"
    "v_mfma_f32_16x16x32_bf16 a[44:47], a[140:143], a[168:171], a[44:47]\n"
    "ds_read_b64_tr_b16 a[138:139], v3 offset:12800\n"
    "buffer_load_dwordx4 v27, s[44:47], s88 offen offset:2048 lds\n"
    "v_mfma_f32_16x16x32_bf16 a[52:55], a[140:143], a[164:167], a[52:55]\n"
    "ds_read_b128 a[180:183], v63 offset:47104\n"
    "v_mfma_f32_16x16x32_bf16 a[60:63], a[140:143], a[172:175], a[60:63]\n"
    "v_mfma_f32_16x16x32_bf16 a[64:67], a[144:147], a[160:163], a[64:67]\n"
    "ds_read_b64_tr_b16 a[140:141], v4 offset:8704\n"
    "buffer_load_dwordx4 v28, s[48:51], s88 offen offset:3072 lds\n"
    "v_mfma_f32_16x16x32_bf16 a[72:75], a[144:147], a[168:171], a[72:75]\n"
    "ds_read_b64_tr_b16 a[142:143], v4 offset:12800\n"
    "s_add_u32 s88, s88, 0xe000\n"
    "v_mfma_f32_16x16x32_bf16 a[80:83], a[144:147], a[164:167], a[80:83]\n"
    "ds_read_b128 a[188:191], v63 offset:48128\n"
    "v_mfma_f32_16x16x32_bf16 a[88:91], a[144:147], a[172:175], a[88:91]\n"
    "s_add_u32 m0, s89, 0x1000\n"
    "v_mfma_f32_16x16x32_bf16 a[68:71], a[148:151], a[160:163], a[68:71]\n"
    "ds_read_b64_tr_b16 a[144:145], v3 offset:9216\n"
    "buffer_load_dwordx4 v25, s[44:47], s88 offen lds\n"
    "v_mfma_f32_16x16x32_bf16 a[76:79], a[148:151], a[168:171], a[76:79]\n"
    "ds_read_b64_tr_b16 a[146:147], v3 offset:13312\n"
    "v_mfma_f32_16x16x32_bf16 a[84:87], a[148:151], a[164:167], a[84:87]\n"
    "v_mfma_f32_16x16x32_bf16 a[92:95], a[148:151], a[172:175], a[92:95]\n"
    "buffer_load_dwordx4 v26, s[48:51], s88 offen offset:1024 lds\n"
    "v_mfma_f32_16x16x32_bf16 a[96:99], a[152:155], a[160:163], a[96:99]\n"
    "ds_read_b64_tr_b16 a[148:149], v4 offset:9216\n"
    "v_mfma_f32_16x16x32_bf16 a[104:107], a[152:155], a[168:171], a[104:107]\n"
    "s_waitcnt lgkmcnt(3)\n"
    "ds_read_b64_tr_b16 a[150:151], v4 offset:13312\n"
    "v_mfma_f32_16x16x32_bf16 a[112:115], a[152:155], a[164:167], a[112:115]\n"
    "buffer_load_dwordx4 v27, s[44:47], s88 offen offset:2048 lds\n"
    "v_mfma_f32_16x16x32_bf16 a[120:123], a[152:155], a[172:175], a[120:123]\n"
    "v_mfma_f32_16x16x32_bf16 a[100:103], a[156:159], a[160:163], a[100:103]\n"
    "ds_read_b64_tr_b16 a[152:153], v3 offset:9728\n"
    "v_mfma_f32_16x16x32_bf16 a[108:111], a[156:159], a[168:171], a[108:111]\n"
    "ds_read_b64_tr_b16 a[154:155], v3 offset:13824\n"
    "buffer_load_dwordx4 v28, s[48:51], s88 offen offset:3072 lds\n"
    "v_mfma_f32_16x16x32_bf16 a[116:119], a[156:159], a[164:167], a[116:119]\n"
    "s_add_u32 s88, s88, 0xe000\n"
    "v_mfma_f32_16x16x32_bf16 a[124:127], a[156:159], a[172:175], a[124:127]\n"
    "v_mfma_f32_16x16x32_bf16 a[0:3], a[128:131], a[176:179], a[0:3]\n"
    "ds_read_b64_tr_b16 a[156:157], v4 offset:9728\n"
    "v_mfma_f32_16x16x32_bf16 a[8:11], a[128:131], a[184:187], a[8:11]\n"
    "ds_read_b64_tr_b16 a[158:159], v4 offset:13824\n"
    "v_mfma_f32_16x16x32_bf16 a[16:19], a[128:131], a[180:183], a[16:19]\n"
    "v_mfma_f32_16x16x32_bf16 a[24:27], a[128:131], a[188:191], a[24:27]\n"
    "v_mfma_f32_16x16x32_bf16 a[4:7], a[132:135], a[176:179], a[4:7]\n"
    "s_waitcnt vmcnt(12)\n"
    "ds_read_b64_tr_b16 a[128:129], v5\n"
    "v_mfma_f32_16x16x32_bf16 a[12:15], a[132:135], a[184:187], a[12:15]\n"
    "s_waitcnt vmcnt(8)\n"
    "ds_read_b64_tr_b16 a[130:131], v5 offset:16384\n"
    "v_mfma_f32_16x16x32_bf16 a[20:23], a[132:135], a[180:183], a[20:23]\n"
    "ds_read_b128 a[160:163], v62 offset:16384\n"
    "s_add_u32 m0, s89, 0x2000\n"
    "v_mfma_f32_16x16x32_bf16 a[28:31], a[132:135], a[188:191], a[28:31]\n"
    "s_waitcnt lgkmcnt(4)\n"
    "buffer_load_dwordx4 v25, s[44:47], s88 offen lds\n"
    "v_mfma_f32_16x16x32_bf16 a[32:35], a[136:139], a[176:179], a[32:35]\n"
    "ds_read_b64_tr_b16 a[132:133], v6\n"
    "v_mfma_f32_16x16x32_bf16 a[40:43], a[136:139], a[184:187], a[40:43]\n"
    "ds_read_b64_tr_b16 a[134:135], v6 offset:16384\n"
    "v_mfma_f32_16x16x32_bf16 a[48:51], a[136:139], a[180:183], a[48:51]\n"
    "ds_read_b128 a[168:171], v62 offset:17408\n"
    "buffer_load_dwordx4 v26, s[48:51], s88 offen offset:1024 lds\n"
    "v_mfma_f32_16x16x32_bf16 a[56:59], a[136:139], a[188:191], a[56:59]\n"
    "v_mfma_f32_16x16x32_bf16 a[36:39], a[140:143], a[176:179], a[36:39]\n"
    "ds_read_b64_tr_b16 a[136:137], v5 offset:512\n"
    "v_mfma_f32_16x16x32_bf16 a[44:47], a[140:143], a[184:187], a[44:47]\n"
    "ds_read_b64_tr_b16 a[138:139], v5 offset:16896\n"
    "buffer_load_dwordx4 v27, s[44:47], s88 offen offset:2048 lds\n"
    "v_mfma_f32_16x16x32_bf16 a[52:55], a[140:143], a[180:183], a[52:55]\n"
    "ds_read_b128 a[164:167], v62 offset:49152\n"
    "v_mfma_f32_16x16x32_bf16 a[60:63], a[140:143], a[188:191], a[60:63]\n"
    "v_mfma_f32_16x16x32_bf16 a[64:67], a[144:147], a[176:179], a[64:67]\n"
    "ds_read_b64_tr_b16 a[140:141], v6 offset:512\n"
    "buffer_load_dwordx4 v28, s[48:51], s88 offen offset:3072 lds\n"
    "v_mfma_f32_16x16x32_bf16 a[72:75], a[144:147], a[184:187], a[72:75]\n"
    "ds_read_b64_tr_b16 a[142:143], v6 offset:16896\n"
    "s_add_u32 s88, s88, 0xe000\n"
    "v_mfma_f32_16x16x32_bf16 a[80:83], a[144:147], a[180:183], a[80:83]\n"
    "ds_read_b128 a[172:175], v62 offset:50176\n"
    "v_mfma_f32_16x16x32_bf16 a[88:91], a[144:147], a[188:191], a[88:91]\n"
    "s_add_u32 m0, s89, 0x3000\n"
    "v_mfma_f32_16x16x32_bf16 a[68:71], a[148:151], a[176:179], a[68:71]\n"
    "ds_read_b64_tr_b16 a[144:145], v5 offset:1024\n"
    "s_waitcnt lgkmcnt(4)\n"
    "buffer_load_dwordx4 v25, s[44:47], s88 offen lds\n"
    "v_mfma_f32_16x16x32_bf16 a[76:79], a[148:151], a[184:187], a[76:79]\n"
    "ds_read_b64_tr_b16 a[146:147], v5 offset:17408\n"
    "v_mfma_f32_16x16x32_bf16 a[84:87], a[148:151], a[180:183], a[84:87]\n"
    "v_mfma_f32_16x16x32_bf16 a[92:95], a[148:151], a[188:191], a[92:95]\n"
    "buffer_load_dwordx4 v26, s[48:51], s88 offen offset:1024 lds\n"
    "v_mfma_f32_16x16x32_bf16 a[96:99], a[152:155], a[176:179], a[96:99]\n"
    "ds_read_b64_tr_b16 a[148:149], v6 offset:1024\n"
    "v_mfma_f32_16x16x32_bf16 a[104:107], a[152:155], a[184:187], a[104:107]\n"
    "ds_read_b64_tr_b16 a[150:151], v6 offset:17408\n"
    "v_mfma_f32_16x16x32_bf16 a[112:115], a[152:155], a[180:183], a[112:115]\n"
    "buffer_load_dwordx4 v27, s[44:47], s88 offen offset:2048 lds\n"
    "v_mfma_f32_16x16x32_bf16 a[120:123], a[152:155], a[188:191], a[120:123]\n"
    "v_mfma_f32_16x16x32_bf16 a[100:103], a[156:159], a[176:179], a[100:103]\n"
    "ds_read_b64_tr_b16 a[152:153], v5 offset:1536\n"
    "v_mfma_f32_16x16x32_bf16 a[108:111], a[156:159], a[184:187], a[108:111]\n"
    "ds_read_b64_tr_b16 a[154:155], v5 offset:17920\n"
    "buffer_load_dwordx4 v28, s[48:51], s88 offen offset:3072 lds\n"
    "v_mfma_f32_16x16x32_bf16 a[116:119], a[156:159], a[180:183], a[116:119]\n"
    "s_add_u32 s88, s88, 0xe000\n"
    "v_mfma_f32_16x16x32_bf16 a[124:127], a[156:159], a[188:191], a[124:127]\n"
    "v_mfma_f32_16x16x32_bf16 a[0:3], a[128:131], a[160:163], a[0:3]\n"
    "ds_read_b64_tr_b16 a[156:157], v6 offset:1536\n"
    "v_mfma_f32_16x16x32_bf16 a[8:11], a[128:131], a[168:171], a[8:11]\n"
    "ds_read_b64_tr_b16 a[158:159], v6 offset:17920\n"
    "v_mfma_f32_16x16x32_bf16 a[16:19], a[128:131], a[164:167], a[16:19]\n"
    "s_waitcnt lgkmcnt(4)\n"
    "v_mfma_f32_16x16x32_bf16 a[24:27], a[128:131], a[172:175], a[24:27]\n"
    "v_mfma_f32_16x16x32_bf16 a[4:7], a[132:135], a[160:163], a[4:7]\n"
    "s_waitcnt vmcnt(12)\n"
    "ds_read_b64_tr_b16 a[128:129], v3\n"
    "v_mfma_f32_16x16x32_bf16 a[12:15], a[132:135], a[168:171], a[12:15]\n"
    "s_waitcnt vmcnt(8)\n"
    "ds_read_b64_tr_b16 a[130:131], v3 offset:4096\n"
    "v_mfma_f32_16x16x32_bf16 a[20:23], a[132:135], a[164:167], a[20:23]\n"
    "ds_read_b128 a[176:179], v63 offset:18432\n"
    "s_add_u32 m0, s91, 0x10000\n"
    "v_mfma_f32_16x16x32_bf16 a[28:31], a[132:135], a[172:175], a[28:31]\n"
    "s_waitcnt lgkmcnt(3)\n"
    "buffer_load_dwordx4 v25, s[44:47], s88 offen lds\n"
    "v_mfma_f32_16x16x32_bf16 a[32:35], a[136:139], a[160:163], a[32:35]\n"
    "ds_read_b64_tr_b16 a[132:133], v4\n"
    "v_mfma_f32_16x16x32_bf16 a[40:43], a[136:139], a[168:171], a[40:43]\n"
    "ds_read_b64_tr_b16 a[134:135], v4 offset:4096\n"
    "v_mfma_f32_16x16x32_bf16 a[48:51], a[136:139], a[164:167], a[48:51]\n"
    "ds_read_b128 a[184:187], v63 offset:19456\n"
    "buffer_load_dwordx4 v26, s[48:51], s88 offen offset:1024 lds\n"
    "v_mfma_f32_16x16x32_bf16 a[56:59], a[136:139], a[172:175], a[56:59]\n"
    "v_mfma_f32_16x16x32_bf16 a[36:39], a[140:143], a[160:163], a[36:39]\n"
    "ds_read_b64_tr_b16 a[136:137], v3 offset:512\n"
    "v_mfma_f32_16x16x32_bf16 a[44:47], a[140:143], a[168:171], a[44:47]\n"
    "ds_read_b64_tr_b16 a[138:139], v3 offset:4608\n"
    "buffer_load_dwordx4 v27, s[44:47], s88 offen offset:2048 lds\n"
    "v_mfma_f32_16x16x32_bf16 a[52:55], a[140:143], a[164:167], a[52:55]\n"
    "ds_read_b128 a[180:183], v63 offset:51200\n"
    "v_mfma_f32_16x16x32_bf16 a[60:63], a[140:143], a[172:175], a[60:63]\n"
    "v_mfma_f32_16x16x32_bf16 a[64:67], a[144:147], a[160:163], a[64:67]\n"
    "ds_read_b64_tr_b16 a[140:141], v4 offset:512\n"
    "buffer_load_dwordx4 v28, s[48:51], s88 offen offset:3072 lds\n"
    "v_mfma_f32_16x16x32_bf16 a[72:75], a[144:147], a[168:171], a[72:75]\n"
    "ds_read_b64_tr_b16 a[142:143], v4 offset:4608\n"
    "s_add_u32 s88, s88, 0xe000\n"
    "v_mfma_f32_16x16x32_bf16 a[80:83], a[144:147], a[164:167], a[80:83]\n"
    "ds_read_b128 a[188:191], v63 offset:52224\n"
    "v_mfma_f32_16x16x32_bf16 a[88:91], a[144:147], a[172:175], a[88:91]\n"
    "s_add_u32 m0, s91, 0x14000\n"
    "v_mfma_f32_16x16x32_bf16 a[68:71], a[148:151], a[160:163], a[68:71]\n"
    "ds_read_b64_tr_b16 a[144:145], v3 offset:1024\n"
    "buffer_load_dwordx4 v25, s[44:47], s88 offen lds\n"
    "v_mfma_f32_16x16x32_bf16 a[76:79], a[148:151], a[168:171], a[76:79]\n"
    "ds_read_b64_tr_b16 a[146:147], v3 offset:5120\n"
    "v_mfma_f32_16x16x32_bf16 a[84:87], a[148:151], a[164:167], a[84:87]\n"
    "v_mfma_f32_16x16x32_bf16 a[92:95], a[148:151], a[172:175], a[92:95]\n"
    "buffer_load_dwordx4 v26, s[48:51], s88 offen offset:1024 lds\n"
    "v_mfma_f32_16x16x32_bf16 a[96:99], a[152:155], a[160:163], a[96:99]\n"
    "ds_read_b64_tr_b16 a[148:149], v4 offset:1024\n"
    "v_mfma_f32_16x16x32_bf16 a[104:107], a[152:155], a[168:171], a[104:107]\n"
    "s_waitcnt lgkmcnt(3)\n"
    "ds_read_b64_tr_b16 a[150:151], v4 offset:5120\n"
    "v_mfma_f32_16x16x32_bf16 a[112:115], a[152:155], a[164:167], a[112:115]\n"
    "buffer_load_dwordx4 v27, s[44:47], s88 offen offset:2048 lds\n"
    "v_mfma_f32_16x16x32_bf16 a[120:123], a[152:155], a[172:175], a[120:123]\n"
    "v_mfma_f32_16x16x32_bf16 a[100:103], a[156:159], a[160:163], a[100:103]\n"
    "ds_read_b64_tr_b16 a[152:153], v3 offset:1536\n"
    "v_mfma_f32_16x16x32_bf16 a[108:111], a[156:159], a[168:171], a[108:111]\n"
    "ds_read_b64_tr_b16 a[154:155], v3 offset:5632\n"
    "buffer_load_dwordx4 v28, s[48:51], s88 offen offset:3072 lds\n"
    "v_mfma_f32_16x16x32_bf16 a[116:119], a[156:159], a[164:167], a[116:119]\n"
    "s_add_u32 s88, s88, 0xe000\n"
    "v_mfma_f32_16x16x32_bf16 a[124:127], a[156:159], a[172:175], a[124:127]\n"
    "v_mfma_f32_16x16x32_bf16 a[0:3], a[128:131], a[176:179], a[0:3]\n"
    "ds_read_b64_tr_b16 a[156:157], v4 offset:1536\n"
    "v_mfma_f32_16x16x32_bf16 a[8:11], a[128:131], a[184:187], a[8:11]\n"
    "ds_read_b64_tr_b16 a[158:159], v4 offset:5632\n"
    "v_mfma_f32_16x16x32_bf16 a[16:19], a[128:131], a[180:183], a[16:19]\n"
    "v_mfma_f32_16x16x32_bf16 a[24:27], a[128:131], a[188:191], a[24:27]\n"
    "v_mfma_f32_16x16x32_bf16 a[4:7], a[132:135], a[176:179], a[4:7]\n"
    "s_waitcnt vmcnt(12)\n"
    "ds_read_b64_tr_b16 a[128:129], v3 offset:8192\n"
    "v_mfma_f32_16x16x32_bf16 a[12:15], a[132:135], a[184:187], a[12:15]\n"
    "s_waitcnt vmcnt(8)\n"
    "ds_read_b64_tr_b16 a[130:131], v3 offset:12288\n"
    "v_mfma_f32_16x16x32_bf16 a[20:23], a[132:135], a[180:183], a[20:23]\n"
    "ds_read_b128 a[160:163], v62 offset:20480\n"
    "s_add_u32 m0, s89, 0\n"
    "v_mfma_f32_16x16x32_bf16 a[28:31], a[132:135], a[188:191], a[28:31]\n"
    "s_waitcnt lgkmcnt(4)\n"
    "buffer_load_dwordx4 v25, s[44:47], s88 offen lds\n"
    "v_mfma_f32_16x16x32_bf16 a[32:35], a[136:139], a[176:179], a[32:35]\n"
    "ds_read_b64_tr_b16 a[132:133], v4 offset:8192\n"
    "v_mfma_f32_16x16x32_bf16 a[40:43], a[136:139], a[184:187], a[40:43]\n"
    "ds_read_b64_tr_b16 a[134:135], v4 offset:12288\n"
    "v_mfma_f32_16x16x32_bf16 a[48:51], a[136:139], a[180:183], a[48:51]\n"
    "ds_read_b128 a[168:171], v62 offset:21504\n"
    "buffer_load_dwordx4 v26, s[48:51], s88 offen offset:1024 lds\n"
    "v_mfma_f32_16x16x32_bf16 a[56:59], a[136:139], a[188:191], a[56:59]\n"
    "v_mfma_f32_16x16x32_bf16 a[36:39], a[140:143], a[176:179], a[36:39]\n"
    "ds_read_b64_tr_b16 a[136:137], v3 offset:8704\n"
    "v_mfma_f32_16x16x32_bf16 a[44:47], a[140:143], a[184:187], a[44:47]\n"
    "ds_read_b64_tr_b16 a[138:139], v3 offset:12800\n"
    "buffer_load_dwordx4 v27, s[44:47], s88 offen offset:2048 lds\n"
    "v_mfma_f32_16x16x32_bf16 a[52:55], a[140:143], a[180:183], a[52:55]\n"
    "ds_read_b128 a[164:167], v62 offset:53248\n"
    "v_mfma_f32_16x16x32_bf16 a[60:63], a[140:143], a[188:191], a[60:63]\n"
    "v_mfma_f32_16x16x32_bf16 a[64:67], a[144:147], a[176:179], a[64:67]\n"
    "ds_read_b64_tr_b16 a[140:141], v4 offset:8704\n"
    "buffer_load_dwordx4 v28, s[48:51], s88 offen offset:3072 lds\n"
    "v_mfma_f32_16x16x32_bf16 a[72:75], a[144:147], a[184:187], a[72:75]\n"
    "ds_read_b64_tr_b16 a[142:143], v4 offset:12800\n"
    "s_add_u32 s88, s88, 0xe000\n"
    "v_mfma_f32_16x16x32_bf16 a[80:83], a[144:147], a[180:183], a[80:83]\n"
    "ds_read_b128 a[172:175], v62 offset:54272\n"
    "v_mfma_f32_16x16x32_bf16 a[88:91], a[144:147], a[188:191], a[88:91]\n"
    "s_add_u32 m0, s89, 0x1000\n"
    "v_mfma_f32_16x16x32_bf16 a[68:71], a[148:151], a[176:179], a[68:71]\n"
    "ds_read_b64_tr_b16 a[144:145], v3 offset:9216\n"
    "s_waitcnt lgkmcnt(4)\n"
    "buffer_load_dwordx4 v25, s[44:47], s88 offen lds\n"
    "v_mfma_f32_16x16x32_bf16 a[76:79], a[148:151], a[184:187], a[76:79]\n"
    "ds_read_b64_tr_b16 a[146:147], v3 offset:13312\n"
    "v_mfma_f32_16x16x32_bf16 a[84:87], a[148:151], a[180:183], a[84:87]\n"
    "v_mfma_f32_16x16x32_bf16 a[92:95], a[148:151], a[188:191], a[92:95]\n"
    "buffer_load_dwordx4 v26, s[48:51], s88 offen offset:1024 lds\n"
    "v_mfma_f32_16x16x32_bf16 a[96:99], a[152:155], a[176:179], a[96:99]\n"
    "ds_read_b64_tr_b16 a[148:149], v4 offset:9216\n"
    "v_mfma_f32_16x16x32_bf16 a[104:107], a[152:155], a[184:187], a[104:107]\n"
    "ds_read_b64_tr_b16 a[150:151], v4 offset:13312\n"
    "v_mfma_f32_16x16x32_bf16 a[112:115], a[152:155], a[180:183], a[112:115]\n"
    "buffer_load_dwordx4 v27, s[44:47], s88 offen offset:2048 lds\n"
    "v_mfma_f32_16x16x32_bf16 a[120:123], a[152:155], a[188:191], a[120:123]\n"
    "v_mfma_f32_16x16x32_bf16 a[100:103], a[156:159], a[176:179], a[100:103]\n"
    "ds_read_b64_tr_b16 a[152:153], v3 offset:9728\n"
    "v_mfma_f32_16x16x32_bf16 a[108:111], a[156:159], a[184:187], a[108:111]\n"
    "ds_read_b64_tr_b16 a[154:155], v3 offset:13824\n"
    "buffer_load_dwordx4 v28, s[48:51], s88 offen offset:3072 lds\n"
    "v_mfma_f32_16x16x32_bf16 a[116:119], a[156:159], a[180:183], a[116:119]\n"
    "s_add_u32 s88, s88, 0xe000\n"
    "v_mfma_f32_16x16x32_bf16 a[124:127], a[156:159], a[188:191], a[124:127]\n"
    "v_mfma_f32_16x16x32_bf16 a[0:3], a[128:131], a[160:163], a[0:3]\n"
    "ds_read_b64_tr_b16 a[156:157], v4 offset:9728\n"
    "v_mfma_f32_16x16x32_bf16 a[8:11], a[128:131], a[168:171], a[8:11]\n"
    "ds_read_b64_tr_b16 a[158:159], v4 offset:13824\n"
    "v_mfma_f32_16x16x32_bf16 a[16:19], a[128:131], a[164:167], a[16:19]\n"
    "s_waitcnt lgkmcnt(4)\n"
    "v_mfma_f32_16x16x32_bf16 a[24:27], a[128:131], a[172:175], a[24:27]\n"
    "v_mfma_f32_16x16x32_bf16 a[4:7], a[132:135], a[160:163], a[4:7]\n"
    "s_waitcnt vmcnt(12)\n"
    "ds_read_b64_tr_b16 a[128:129], v5\n"
    "v_mfma_f32_16x16x32_bf16 a[12:15], a[132:135], a[168:171], a[12:15]\n"
    "s_waitcnt vmcnt(8)\n"
    "ds_read_b64_tr_b16 a[130:131], v5 offset:16384\n"
    "v_mfma_f32_16x16x32_bf16 a[20:23], a[132:135], a[164:167], a[20:23]\n"
    "ds_read_b128 a[176:179], v63 offset:22528\n"
    "s_add_u32 m0, s89, 0x2000\n"
    "v_mfma_f32_16x16x32_bf16 a[28:31], a[132:135], a[172:175], a[28:31]\n"
    "s_waitcnt lgkmcnt(3)\n"
    "buffer_load_dwordx4 v25, s[44:47], s88 offen lds\n"
    "v_mfma_f32_16x16x32_bf16 a[32:35], a[136:139], a[160:163], a[32:35]\n"
    "ds_read_b64_tr_b16 a[132:133], v6\n"
    "v_mfma_f32_16x16x32_bf16 a[40:43], a[136:139], a[168:171], a[40:43]\n"
    "ds_read_b64_tr_b16 a[134:135], v6 offset:16384\n"
    "v_mfma_f32_16x16x32_bf16 a[48:51], a[136:139], a[164:167], a[48:51]\n"
    "ds_read_b128 a[184:187], v63 offset:23552\n"
    "buffer_load_dwordx4 v26, s[48:51], s88 offen offset:1024 lds\n"
    "v_mfma_f32_16x16x32_bf16 a[56:59], a[136:139], a[172:175], a[56:59]\n"
    "v_mfma_f32_16x16x32_bf16 a[36:39], a[140:143], a[160:163], a[36:39]\n"
    "ds_read_b64_tr_b16 a[136:137], v5 offset:512\n"
    "v_mfma_f32_16x16x32_bf16 a[44:47], a[140:143], a[168:171], a[44:47]\n"
    "ds_read_b64_tr_b16 a[138:139], v5 offset:16896\n"
    "buffer_load_dwordx4 v27, s[44:47], s88 offen offset:2048 lds\n"
    "v_mfma_f32_16x16x32_bf16 a[52:55], a[140:143], a[164:167], a[52:55]\n"
    "ds_read_b128 a[180:183], v63 offset:55296\n"
    "v_mfma_f32_16x16x32_bf16 a[60:63], a[140:143], a[172:175], a[60:63]\n"
    "v_mfma_f32_16x16x32_bf16 a[64:67], a[144:147], a[160:163], a[64:67]\n"
    "ds_read_b64_tr_b16 a[140:141], v6 offset:512\n"
    "buffer_load_dwordx4 v28, s[48:51], s88 offen offset:3072 lds\n"
    "v_mfma_f32_16x16x32_bf16 a[72:75], a[144:147], a[168:171], a[72:75]\n"
    "ds_read_b64_tr_b16 a[142:143], v6 offset:16896\n"
    "s_add_u32 s88, s88, 0xe000\n"
    "v_mfma_f32_16x16x32_bf16 a[80:83], a[144:147], a[164:167], a[80:83]\n"
    "ds_read_b128 a[188:191], v63 offset:56320\n"
    "v_mfma_f32_16x16x32_bf16 a[88:91], a[144:147], a[172:175], a[88:91]\n"
    "s_add_u32 m0, s89, 0x3000\n"
    "v_mfma_f32_16x16x32_bf16 a[68:71], a[148:151], a[160:163], a[68:71]\n"
    "ds_read_b64_tr_b16 a[144:145], v5 offset:1024\n"
    "buffer_load_dwordx4 v25, s[44:47], s88 offen lds\n"
    "v_mfma_f32_16x16x32_bf16 a[76:79], a[148:151], a[168:171], a[76:79]\n"
    "ds_read_b64_tr_b16 a[146:147], v5 offset:17408\n"
    "v_mfma_f32_16x16x32_bf16 a[84:87], a[148:151], a[164:167], a[84:87]\n"
    "v_mfma_f32_16x16x32_bf16 a[92:95], a[148:151], a[172:175], a[92:95]\n"
    "buffer_load_dwordx4 v26, s[48:51], s88 offen offset:1024 lds\n"
    "v_mfma_f32_16x16x32_bf16 a[96:99], a[152:155], a[160:163], a[96:99]\n"
    "ds_read_b64_tr_b16 a[148:149], v6 offset:1024\n"
    "v_mfma_f32_16x16x32_bf16 a[104:107], a[152:155], a[168:171], a[104:107]\n"
    "s_waitcnt lgkmcnt(3)\n"
    "ds_read_b64_tr_b16 a[150:151], v6 offset:17408\n"
    "v_mfma_f32_16x16x32_bf16 a[112:115], a[152:155], a[164:167], a[112:115]\n"
    "buffer_load_dwordx4 v27, s[44:47], s88 offen offset:2048 lds\n"
    "v_mfma_f32_16x16x32_bf16 a[120:123], a[152:155], a[172:175], a[120:123]\n"
    "v_mfma_f32_16x16x32_bf16 a[100:103], a[156:159], a[160:163], a[100:103]\n"
    "ds_read_b64_tr_b16 a[152:153], v5 offset:1536\n"
    "v_mfma_f32_16x16x32_bf16 a[108:111], a[156:159], a[168:171], a[108:111]\n"
    "ds_read_b64_tr_b16 a[154:155], v5 offset:17920\n"
    "buffer_load_dwordx4 v28, s[48:51], s88 offen offset:3072 lds\n"
    "v_mfma_f32_16x16x32_bf16 a[116:119], a[156:159], a[164:167], a[116:119]\n"
    "s_add_u32 s88, s88, 0xe000\n"
    "v_mfma_f32_16x16x32_bf16 a[124:127], a[156:159], a[172:175], a[124:127]\n"
    "v_mfma_f32_16x16x32_bf16 a[0:3], a[128:131], a[176:179], a[0:3]\n"
    "ds_read_b64_tr_b16 a[156:157], v6 offset:1536\n"
    "v_mfma_f32_16x16x32_bf16 a[8:11], a[128:131], a[184:187], a[8:11]\n"
    "ds_read_b64_tr_b16 a[158:159], v6 offset:17920\n"
    "v_mfma_f32_16x16x32_bf16 a[16:19], a[128:131], a[180:183], a[16:19]\n"
    "v_mfma_f32_16x16x32_bf16 a[24:27], a[128:131], a[188:191], a[24:27]\n"
    "v_mfma_f32_16x16x32_bf16 a[4:7], a[132:135], a[176:179], a[4:7]\n"
    "s_waitcnt vmcnt(12)\n"
    "ds_read_b64_tr_b16 a[128:129], v3\n"
    "v_mfma_f32_16x16x32_bf16 a[12:15], a[132:135], a[184:187], a[12:15]\n"
    "s_waitcnt vmcnt(8)\n"
    "ds_read_b64_tr_b16 a[130:131], v3 offset:4096\n"
    "v_mfma_f32_16x16x32_bf16 a[20:23], a[132:135], a[180:183], a[20:23]\n"
    "ds_read_b128 a[160:163], v62 offset:24576\n"
    "s_add_u32 m0, s91, 0x10000\n"
    "v_mfma_f32_16x16x32_bf16 a[28:31], a[132:135], a[188:191], a[28:31]\n"
    "s_waitcnt lgkmcnt(4)\n"
    "buffer_load_dwordx4 v25, s[44:47], s88 offen lds\n"
    "v_mfma_f32_16x16x32_bf16 a[32:35], a[136:139], a[176:179], a[32:35]\n"
    "ds_read_b64_tr_b16 a[132:133], v4\n"
    "v_mfma_f32_16x16x32_bf16 a[40:43], a[136:139], a[184:187], a[40:43]\n"
    "ds_read_b64_tr_b16 a[134:135], v4 offset:4096\n"
    "v_mfma_f32_16x16x32_bf16 a[48:51], a[136:139], a[180:183], a[48:51]\n"
    "ds_read_b128 a[168:171], v62 offset:25600\n"
    "buffer_load_dwordx4 v26, s[48:51], s88 offen offset:1024 lds\n"
    "v_mfma_f32_16x16x32_bf16 a[56:59], a[136:139], a[188:191], a[56:59]\n"
    "v_mfma_f32_16x16x32_bf16 a[36:39], a[140:143], a[176:179], a[36:39]\n"
    "ds_read_b64_tr_b16 a[136:137], v3 offset:512\n"
    "v_mfma_f32_16x16x32_bf16 a[44:47], a[140:143], a[184:187], a[44:47]\n"
    "ds_read_b64_tr_b16 a[138:139], v3 offset:4608\n"
    "buffer_load_dwordx4 v27, s[44:47], s88 offen offset:2048 lds\n"
    "v_mfma_f32_16x16x32_bf16 a[52:55], a[140:143], a[180:183], a[52:55]\n"
    "ds_read_b128 a[164:167], v62 offset:57344\n"
    "v_mfma_f32_16x16x32_bf16 a[60:63], a[140:143], a[188:191], a[60:63]\n"
    "v_mfma_f32_16x16x32_bf16 a[64:67], a[144:147], a[176:179], a[64:67]\n"
    "ds_read_b64_tr_b16 a[140:141], v4 offset:512\n"
    "buffer_load_dwordx4 v28, s[48:51], s88 offen offset:3072 lds\n"
    "v_mfma_f32_16x16x32_bf16 a[72:75], a[144:147], a[184:187], a[72:75]\n"
    "ds_read_b64_tr_b16 a[142:143], v4 offset:4608\n"
    "s_add_u32 s88, s88, 0xe000\n"
    "v_mfma_f32_16x16x32_bf16 a[80:83], a[144:147], a[180:183], a[80:83]\n"
    "ds_read_b128 a[172:175], v62 offset:58368\n"
    "v_mfma_f32_16x16x32_bf16 a[88:91], a[144:147], a[188:191], a[88:91]\n"
    "s_add_u32 m0, s91, 0x14000\n"
    "v_mfma_f32_16x16x32_bf16 a[68:71], a[148:151], a[176:179], a[68:71]\n"
    "ds_read_b64_tr_b16 a[144:145], v3 offset:1024\n"
    "s_waitcnt lgkmcnt(4)\n"
    "buffer_load_dwordx4 v25, s[44:47], s88 offen lds\n"
    "v_mfma_f32_16x16x32_bf16 a[76:79], a[148:151], a[184:187], a[76:79]\n"
    "ds_read_b64_tr_b16 a[146:147], v3 offset:5120\n"
    "v_mfma_f32_16x16x32_bf16 a[84:87], a[148:151], a[180:183], a[84:87]\n"
    "v_mfma_f32_16x16x32_bf16 a[92:95], a[148:151], a[188:191], a[92:95]\n"
    "buffer_load_dwordx4 v26, s[48:51], s88 offen offset:1024 lds\n"
    "v_mfma_f32_16x16x32_bf16 a[96:99], a[152:155], a[176:179], a[96:99]\n"
    "ds_read_b64_tr_b16 a[148:149], v4 offset:1024\n"
    "v_mfma_f32_16x16x32_bf16 a[104:107], a[152:155], a[184:187], a[104:107]\n"
    "ds_read_b64_tr_b16 a[150:151], v4 offset:5120\n"
    "v_mfma_f32_16x16x32_bf16 a[112:115], a[152:155], a[180:183], a[112:115]\n"
    "buffer_load_dwordx4 v27, s[44:47], s88 offen offset:2048 lds\n"
    "v_mfma_f32_16x16x32_bf16 a[120:123], a[152:155], a[188:191], a[120:123]\n"
    "v_mfma_f32_16x16x32_bf16 a[100:103], a[156:159], a[176:179], a[100:103]\n"
    "ds_read_b64_tr_b16 a[152:153], v3 offset:1536\n"
    "v_mfma_f32_16x16x32_bf16 a[108:111], a[156:159], a[184:187], a[108:111]\n"
    "ds_read_b64_tr_b16 a[154:155], v3 offset:5632\n"
    "buffer_load_dwordx4 v28, s[48:51], s88 offen offset:3072 lds\n"
    "v_mfma_f32_16x16x32_bf16 a[116:119], a[156:159], a[180:183], a[116:119]\n"
    "s_add_u32 s88, s88, 0xe000\n"
    "v_mfma_f32_16x16x32_bf16 a[124:127], a[156:159], a[188:191], a[124:127]\n"
    "v_mfma_f32_16x16x32_bf16 a[0:3], a[128:131], a[160:163], a[0:3]\n"
    "ds_read_b64_tr_b16 a[156:157], v4 offset:1536\n"
    "v_mfma_f32_16x16x32_bf16 a[8:11], a[128:131], a[168:171], a[8:11]\n"
    "ds_read_b64_tr_b16 a[158:159], v4 offset:5632\n"
    "v_mfma_f32_16x16x32_bf16 a[16:19], a[128:131], a[164:167], a[16:19]\n"
    "s_waitcnt lgkmcnt(4)\n"
    "v_mfma_f32_16x16x32_bf16 a[24:27], a[128:131], a[172:175], a[24:27]\n"
    "v_mfma_f32_16x16x32_bf16 a[4:7], a[132:135], a[160:163], a[4:7]\n"
    "s_waitcnt vmcnt(12)\n"
    "ds_read_b64_tr_b16 a[128:129], v3 offset:8192\n"
    "v_mfma_f32_16x16x32_bf16 a[12:15], a[132:135], a[168:171], a[12:15]\n"
    "s_waitcnt vmcnt(8)\n"
    "ds_read_b64_tr_b16 a[130:131], v3 offset:12288\n"
    "v_mfma_f32_16x16x32_bf16 a[20:23], a[132:135], a[164:167], a[20:23]\n"
    "ds_read_b128 a[176:179], v63 offset:26624\n"
    "s_add_u32 m0, s89, 0\n"
    "v_mfma_f32_16x16x32_bf16 a[28:31], a[132:135], a[172:175], a[28:31]\n"
    "s_waitcnt lgkmcnt(3)\n"
    "buffer_load_dwordx4 v25, s[44:47], s88 offen lds\n"
    "v_mfma_f32_16x16x32_bf16 a[32:35], a[136:139], a[160:163], a[32:35]\n"
    "ds_read_b64_tr_b16 a[132:133], v4 offset:8192\n"
    "v_mfma_f32_16x16x32_bf16 a[40:43], a[136:139], a[168:171], a[40:43]\n"
    "ds_read_b64_tr_b16 a[134:135], v4 offset:12288\n"
    "v_mfma_f32_16x16x32_bf16 a[48:51], a[136:139], a[164:167], a[48:51]\n"
    "ds_read_b128 a[184:187], v63 offset:27648\n"
    "buffer_load_dwordx4 v26, s[48:51], s88 offen offset:1024 lds\n"
    "v_mfma_f32_16x16x32_bf16 a[56:59], a[136:139], a[172:175], a[56:59]\n"
    "v_mfma_f32_16x16x32_bf16 a[36:39], a[140:143], a[160:163], a[36:39]\n"
    "ds_read_b64_tr_b16 a[136:137], v3 offset:8704\n"
    "v_mfma_f32_16x16x32_bf16 a[44:47], a[140:143], a[168:171], a[44:47]\n"
    "ds_read_b64_tr_b16 a[138:139], v3 offset:12800\n"
    "buffer_load_dwordx4 v27, s[44:47], s88 offen offset:2048 lds\n"
    "v_mfma_f32_16x16x32_bf16 a[52:55], a[140:143], a[164:167], a[52:55]\n"
    "ds_read_b128 a[180:183], v63 offset:59392\n"
    "v_mfma_f32_16x16x32_bf16 a[60:63], a[140:143], a[172:175], a[60:63]\n"
    "v_mfma_f32_16x16x32_bf16 a[64:67], a[144:147], a[160:163], a[64:67]\n"
    "ds_read_b64_tr_b16 a[140:141], v4 offset:8704\n"
    "buffer_load_dwordx4 v28, s[48:51], s88 offen offset:3072 lds\n"
    "v_mfma_f32_16x16x32_bf16 a[72:75], a[144:147], a[168:171], a[72:75]\n"
    "ds_read_b64_tr_b16 a[142:143], v4 offset:12800\n"
    "s_add_u32 s88, s88, 0xe000\n"
    "v_mfma_f32_16x16x32_bf16 a[80:83], a[144:147], a[164:167], a[80:83]\n"
    "ds_read_b128 a[188:191], v63 offset:60416\n"
    "v_mfma_f32_16x16x32_bf16 a[88:91], a[144:147], a[172:175], a[88:91]\n"
    "s_add_u32 m0, s89, 0x1000\n"
    "v_mfma_f32_16x16x32_bf16 a[68:71], a[148:151], a[160:163], a[68:71]\n"
    "ds_read_b64_tr_b16 a[144:145], v3 offset:9216\n"
    "buffer_load_dwordx4 v25, s[44:47], s88 offen lds\n"
    "v_mfma_f32_16x16x32_bf16 a[76:79], a[148:151], a[168:171], a[76:79]\n"
    "ds_read_b64_tr_b16 a[146:147], v3 offset:13312\n"
    "v_mfma_f32_16x16x32_bf16 a[84:87], a[148:151], a[164:167], a[84:87]\n"
    "v_mfma_f32_16x16x32_bf16 a[92:95], a[148:151], a[172:175], a[92:95]\n"
    "buffer_load_dwordx4 v26, s[48:51], s88 offen offset:1024 lds\n"
    "v_mfma_f32_16x16x32_bf16 a[96:99], a[152:155], a[160:163], a[96:99]\n"
    "ds_read_b64_tr_b16 a[148:149], v4 offset:9216\n"
    "v_mfma_f32_16x16x32_bf16 a[104:107], a[152:155], a[168:171], a[104:107]\n"
    "s_waitcnt lgkmcnt(3)\n"
    "ds_read_b64_tr_b16 a[150:151], v4 offset:13312\n"
    "v_mfma_f32_16x16x32_bf16 a[112:115], a[152:155], a[164:167], a[112:115]\n"
    "buffer_load_dwordx4 v27, s[44:47], s88 offen offset:2048 lds\n"
    "v_mfma_f32_16x16x32_bf16 a[120:123], a[152:155], a[172:175], a[120:123]\n"
    "v_mfma_f32_16x16x32_bf16 a[100:103], a[156:159], a[160:163], a[100:103]\n"
    "ds_read_b64_tr_b16 a[152:153], v3 offset:9728\n"
    "v_mfma_f32_16x16x32_bf16 a[108:111], a[156:159], a[168:171], a[108:111]\n"
    "ds_read_b64_tr_b16 a[154:155], v3 offset:13824\n"
    "buffer_load_dwordx4 v28, s[48:51], s88 offen offset:3072 lds\n"
    "v_mfma_f32_16x16x32_bf16 a[116:119], a[156:159], a[164:167], a[116:119]\n"
    "s_add_u32 s88, s88, 0xe000\n"
    "v_mfma_f32_16x16x32_bf16 a[124:127], a[156:159], a[172:175], a[124:127]\n"
    "v_mfma_f32_16x16x32_bf16 a[0:3], a[128:131], a[176:179], a[0:3]\n"
    "ds_read_b64_tr_b16 a[156:157], v4 offset:9728\n"
    "v_mfma_f32_16x16x32_bf16 a[8:11], a[128:131], a[184:187], a[8:11]\n"
    "ds_read_b64_tr_b16 a[158:159], v4 offset:13824\n"
    "v_mfma_f32_16x16x32_bf16 a[16:19], a[128:131], a[180:183], a[16:19]\n"
    "v_mfma_f32_16x16x32_bf16 a[24:27], a[128:131], a[188:191], a[24:27]\n"
    "v_mfma_f32_16x16x32_bf16 a[4:7], a[132:135], a[176:179], a[4:7]\n"
    "s_waitcnt vmcnt(12)\n"
    "ds_read_b64_tr_b16 a[128:129], v5\n"
    "v_mfma_f32_16x16x32_bf16 a[12:15], a[132:135], a[184:187], a[12:15]\n"
    "s_waitcnt vmcnt(8)\n"
    "ds_read_b64_tr_b16 a[130:131], v5 offset:16384\n"
    "v_mfma_f32_16x16x32_bf16 a[20:23], a[132:135], a[180:183], a[20:23]\n"
    "ds_read_b128 a[160:163], v62 offset:28672\n"
    "s_lshl_b32 s37, s33, 2\n"
    "s_add_u32 s37, s37, s32\n"
    "s_lshl_b32 s88, s37, 8\n"
    "s_add_u32 m0, s89, 0x2000\n"
    "v_mfma_f32_16x16x32_bf16 a[28:31], a[132:135], a[188:191], a[28:31]\n"
    "s_waitcnt lgkmcnt(3)\n"
    "buffer_load_dwordx4 v29, s[52:55], s88 offen lds\n"
    "v_mfma_f32_16x16x32_bf16 a[32:35], a[136:139], a[176:179], a[32:35]\n"
    "ds_read_b64_tr_b16 a[132:133], v6\n"
    "v_mfma_f32_16x16x32_bf16 a[40:43], a[136:139], a[184:187], a[40:43]\n"
    "ds_read_b64_tr_b16 a[134:135], v6 offset:16384\n"
    "v_mfma_f32_16x16x32_bf16 a[48:51], a[136:139], a[180:183], a[48:51]\n"
    "ds_read_b128 a[168:171], v62 offset:29696\n"
    "buffer_load_dwordx4 v30, s[52:55], s88 offen offset:1024 lds\n"
    "v_mfma_f32_16x16x32_bf16 a[56:59], a[136:139], a[188:191], a[56:59]\n"
    "v_mfma_f32_16x16x32_bf16 a[36:39], a[140:143], a[176:179], a[36:39]\n"
    "ds_read_b64_tr_b16 a[136:137], v5 offset:512\n"
    "v_mfma_f32_16x16x32_bf16 a[44:47], a[140:143], a[184:187], a[44:47]\n"
    "ds_read_b64_tr_b16 a[138:139], v5 offset:16896\n"
    "buffer_load_dwordx4 v31, s[52:55], s88 offen offset:2048 lds\n"
    "v_mfma_f32_16x16x32_bf16 a[52:55], a[140:143], a[180:183], a[52:55]\n"
    "ds_read_b128 a[164:167], v62 offset:61440\n"
    "v_mfma_f32_16x16x32_bf16 a[60:63], a[140:143], a[188:191], a[60:63]\n"
    "v_mfma_f32_16x16x32_bf16 a[64:67], a[144:147], a[176:179], a[64:67]\n"
    "ds_read_b64_tr_b16 a[140:141], v6 offset:512\n"
    "buffer_load_dwordx4 v32, s[52:55], s88 offen offset:3072 lds\n"
    "v_mfma_f32_16x16x32_bf16 a[72:75], a[144:147], a[184:187], a[72:75]\n"
    "ds_read_b64_tr_b16 a[142:143], v6 offset:16896\n"
    "s_add_u32 s88, s88, 0x20000\n"
    "v_mfma_f32_16x16x32_bf16 a[80:83], a[144:147], a[180:183], a[80:83]\n"
    "ds_read_b128 a[172:175], v62 offset:62464\n"
    "v_mfma_f32_16x16x32_bf16 a[88:91], a[144:147], a[188:191], a[88:91]\n"
    "s_add_u32 m0, s89, 0x3000\n"
    "v_mfma_f32_16x16x32_bf16 a[68:71], a[148:151], a[176:179], a[68:71]\n"
    "ds_read_b64_tr_b16 a[144:145], v5 offset:1024\n"
    "buffer_load_dwordx4 v29, s[52:55], s88 offen lds\n"
    "v_mfma_f32_16x16x32_bf16 a[76:79], a[148:151], a[184:187], a[76:79]\n"
    "ds_read_b64_tr_b16 a[146:147], v5 offset:17408\n"
    "v_mfma_f32_16x16x32_bf16 a[84:87], a[148:151], a[180:183], a[84:87]\n"
    "v_mfma_f32_16x16x32_bf16 a[92:95], a[148:151], a[188:191], a[92:95]\n"
    "buffer_load_dwordx4 v30, s[52:55], s88 offen offset:1024 lds\n"
    "v_mfma_f32_16x16x32_bf16 a[96:99], a[152:155], a[176:179], a[96:99]\n"
    "ds_read_b64_tr_b16 a[148:149], v6 offset:1024\n"
    "v_mfma_f32_16x16x32_bf16 a[104:107], a[152:155], a[184:187], a[104:107]\n"
    "s_waitcnt lgkmcnt(3)\n"
    "ds_read_b64_tr_b16 a[150:151], v6 offset:17408\n"
    "v_mfma_f32_16x16x32_bf16 a[112:115], a[152:155], a[180:183], a[112:115]\n"
    "buffer_load_dwordx4 v31, s[52:55], s88 offen offset:2048 lds\n"
    "v_mfma_f32_16x16x32_bf16 a[120:123], a[152:155], a[188:191], a[120:123]\n"
    "v_mfma_f32_16x16x32_bf16 a[100:103], a[156:159], a[176:179], a[100:103]\n"
    "ds_read_b64_tr_b16 a[152:153], v5 offset:1536\n"
    "v_mfma_f32_16x16x32_bf16 a[108:111], a[156:159], a[184:187], a[108:111]\n"
    "ds_read_b64_tr_b16 a[154:155], v5 offset:17920\n"
    "buffer_load_dwordx4 v32, s[52:55], s88 offen offset:3072 lds\n"
    "v_mfma_f32_16x16x32_bf16 a[116:119], a[156:159], a[180:183], a[116:119]\n"
    "s_add_u32 s88, s88, 0x20000\n"
    "v_mfma_f32_16x16x32_bf16 a[124:127], a[156:159], a[188:191], a[124:127]\n"
    "v_mfma_f32_16x16x32_bf16 a[0:3], a[128:131], a[160:163], a[0:3]\n"
    "ds_read_b64_tr_b16 a[156:157], v6 offset:1536\n"
    "v_mfma_f32_16x16x32_bf16 a[8:11], a[128:131], a[168:171], a[8:11]\n"
    "ds_read_b64_tr_b16 a[158:159], v6 offset:17920\n"
    "v_mfma_f32_16x16x32_bf16 a[16:19], a[128:131], a[164:167], a[16:19]\n"
    "v_mfma_f32_16x16x32_bf16 a[24:27], a[128:131], a[172:175], a[24:27]\n"
    "v_mfma_f32_16x16x32_bf16 a[4:7], a[132:135], a[160:163], a[4:7]\n"
    "s_waitcnt vmcnt(12)\n"
    "ds_read_b64_tr_b16 a[128:129], v3\n"
    "v_mfma_f32_16x16x32_bf16 a[12:15], a[132:135], a[168:171], a[12:15]\n"
    "s_waitcnt vmcnt(8)\n"
    "ds_read_b64_tr_b16 a[130:131], v3 offset:4096\n"
    "v_mfma_f32_16x16x32_bf16 a[20:23], a[132:135], a[164:167], a[20:23]\n"
    "ds_read_b128 a[176:179], v63 offset:30720\n"
    "s_add_u32 m0, s91, 0x10000\n"
    "v_mfma_f32_16x16x32_bf16 a[28:31], a[132:135], a[172:175], a[28:31]\n"
    "s_waitcnt lgkmcnt(4)\n"
    "buffer_load_dwordx4 v29, s[52:55], s88 offen lds\n"
    "v_mfma_f32_16x16x32_bf16 a[32:35], a[136:139], a[160:163], a[32:35]\n"
    "ds_read_b64_tr_b16 a[132:133], v4\n"
    "v_mfma_f32_16x16x32_bf16 a[40:43], a[136:139], a[168:171], a[40:43]\n"
    "ds_read_b64_tr_b16 a[134:135], v4 offset:4096\n"
    "v_mfma_f32_16x16x32_bf16 a[48:51], a[136:139], a[164:167], a[48:51]\n"
    "ds_read_b128 a[184:187], v63 offset:31744\n"
    "buffer_load_dwordx4 v30, s[52:55], s88 offen offset:1024 lds\n"
    "v_mfma_f32_16x16x32_bf16 a[56:59], a[136:139], a[172:175], a[56:59]\n"
    "v_mfma_f32_16x16x32_bf16 a[36:39], a[140:143], a[160:163], a[36:39]\n"
    "ds_read_b64_tr_b16 a[136:137], v3 offset:512\n"
    "v_mfma_f32_16x16x32_bf16 a[44:47], a[140:143], a[168:171], a[44:47]\n"
    "ds_read_b64_tr_b16 a[138:139], v3 offset:4608\n"
    "buffer_load_dwordx4 v31, s[52:55], s88 offen offset:2048 lds\n"
    "v_mfma_f32_16x16x32_bf16 a[52:55], a[140:143], a[164:167], a[52:55]\n"
    "ds_read_b128 a[180:183], v63 offset:63488\n"
    "v_mfma_f32_16x16x32_bf16 a[60:63], a[140:143], a[172:175], a[60:63]\n"
    "v_mfma_f32_16x16x32_bf16 a[64:67], a[144:147], a[160:163], a[64:67]\n"
    "ds_read_b64_tr_b16 a[140:141], v4 offset:512\n"
    "buffer_load_dwordx4 v32, s[52:55], s88 offen offset:3072 lds\n"
    "v_mfma_f32_16x16x32_bf16 a[72:75], a[144:147], a[168:171], a[72:75]\n"
    "ds_read_b64_tr_b16 a[142:143], v4 offset:4608\n"
    "s_add_u32 s88, s88, 0x20000\n"
    "v_mfma_f32_16x16x32_bf16 a[80:83], a[144:147], a[164:167], a[80:83]\n"
    "ds_read_b128 a[188:191], v63 offset:64512\n"
    "v_mfma_f32_16x16x32_bf16 a[88:91], a[144:147], a[172:175], a[88:91]\n"
    "s_add_u32 m0, s91, 0x14000\n"
    "v_mfma_f32_16x16x32_bf16 a[68:71], a[148:151], a[160:163], a[68:71]\n"
    "ds_read_b64_tr_b16 a[144:145], v3 offset:1024\n"
    "s_waitcnt lgkmcnt(4)\n"
    "buffer_load_dwordx4 v29, s[52:55], s88 offen lds\n"
    "v_mfma_f32_16x16x32_bf16 a[76:79], a[148:151], a[168:171], a[76:79]\n"
    "ds_read_b64_tr_b16 a[146:147], v3 offset:5120\n"
    "v_mfma_f32_16x16x32_bf16 a[84:87], a[148:151], a[164:167], a[84:87]\n"
    "v_mfma_f32_16x16x32_bf16 a[92:95], a[148:151], a[172:175], a[92:95]\n"
    "buffer_load_dwordx4 v30, s[52:55], s88 offen offset:1024 lds\n"
    "v_mfma_f32_16x16x32_bf16 a[96:99], a[152:155], a[160:163], a[96:99]\n"
    "ds_read_b64_tr_b16 a[148:149], v4 offset:1024\n"
    "v_mfma_f32_16x16x32_bf16 a[104:107], a[152:155], a[168:171], a[104:107]\n"
    "ds_read_b64_tr_b16 a[150:151], v4 offset:5120\n"
    "v_mfma_f32_16x16x32_bf16 a[112:115], a[152:155], a[164:167], a[112:115]\n"
    "buffer_load_dwordx4 v31, s[52:55], s88 offen offset:2048 lds\n"
    "v_mfma_f32_16x16x32_bf16 a[120:123], a[152:155], a[172:175], a[120:123]\n"
    "v_mfma_f32_16x16x32_bf16 a[100:103], a[156:159], a[160:163], a[100:103]\n"
    "ds_read_b64_tr_b16 a[152:153], v3 offset:1536\n"
    "v_mfma_f32_16x16x32_bf16 a[108:111], a[156:159], a[168:171], a[108:111]\n"
    "ds_read_b64_tr_b16 a[154:155], v3 offset:5632\n"
    "buffer_load_dwordx4 v32, s[52:55], s88 offen offset:3072 lds\n"
    "v_mfma_f32_16x16x32_bf16 a[116:119], a[156:159], a[164:167], a[116:119]\n"
    "s_add_u32 s88, s88, 0x20000\n"
    "v_mfma_f32_16x16x32_bf16 a[124:127], a[156:159], a[172:175], a[124:127]\n"
    "v_mfma_f32_16x16x32_bf16 a[0:3], a[128:131], a[176:179], a[0:3]\n"
    "ds_read_b64_tr_b16 a[156:157], v4 offset:1536\n"
    "v_mfma_f32_16x16x32_bf16 a[8:11], a[128:131], a[184:187], a[8:11]\n"
    "ds_read_b64_tr_b16 a[158:159], v4 offset:5632\n"
    "v_mfma_f32_16x16x32_bf16 a[16:19], a[128:131], a[180:183], a[16:19]\n"
    "s_waitcnt lgkmcnt(4)\n"
    "v_mfma_f32_16x16x32_bf16 a[24:27], a[128:131], a[188:191], a[24:27]\n"
    "v_mfma_f32_16x16x32_bf16 a[4:7], a[132:135], a[176:179], a[4:7]\n"
    "s_waitcnt vmcnt(12)\n"
    "ds_read_b64_tr_b16 a[128:129], v15 offset:8192\n"
    "v_mfma_f32_16x16x32_bf16 a[12:15], a[132:135], a[184:187], a[12:15]\n"
    "s_waitcnt vmcnt(8)\n"
    "ds_read_b64_tr_b16 a[130:131], v15 offset:12288\n"
    "v_mfma_f32_16x16x32_bf16 a[20:23], a[132:135], a[180:183], a[20:23]\n"
    "ds_read_b128 a[160:163], v62\n"
    "s_add_u32 m0, s89, 0\n"
    "v_mfma_f32_16x16x32_bf16 a[28:31], a[132:135], a[188:191], a[28:31]\n"
    "s_waitcnt lgkmcnt(3)\n"
    "buffer_load_dwordx4 v29, s[52:55], s88 offen lds\n"
    "v_mfma_f32_16x16x32_bf16 a[32:35], a[136:139], a[176:179], a[32:35]\n"
    "ds_read_b64_tr_b16 a[132:133], v15 offset:8200\n"
    "v_mfma_f32_16x16x32_bf16 a[40:43], a[136:139], a[184:187], a[40:43]\n"
    "ds_read_b64_tr_b16 a[134:135], v15 offset:12296\n"
    "v_mfma_f32_16x16x32_bf16 a[48:51], a[136:139], a[180:183], a[48:51]\n"
    "ds_read_b128 a[168:171], v62 offset:1024\n"
    "buffer_load_dwordx4 v30, s[52:55], s88 offen offset:1024 lds\n"
    "v_mfma_f32_16x16x32_bf16 a[56:59], a[136:139], a[188:191], a[56:59]\n"
    "v_mfma_f32_16x16x32_bf16 a[36:39], a[140:143], a[176:179], a[36:39]\n"
    "ds_read_b64_tr_b16 a[136:137], v15 offset:8704\n"
    "v_mfma_f32_16x16x32_bf16 a[44:47], a[140:143], a[184:187], a[44:47]\n"
    "ds_read_b64_tr_b16 a[138:139], v15 offset:12800\n"
    "buffer_load_dwordx4 v31, s[52:55], s88 offen offset:2048 lds\n"
    "v_mfma_f32_16x16x32_bf16 a[52:55], a[140:143], a[180:183], a[52:55]\n"
    "ds_read_b128 a[164:167], v62 offset:32768\n"
    "v_mfma_f32_16x16x32_bf16 a[60:63], a[140:143], a[188:191], a[60:63]\n"
    "v_mfma_f32_16x16x32_bf16 a[64:67], a[144:147], a[176:179], a[64:67]\n"
    "ds_read_b64_tr_b16 a[140:141], v15 offset:8712\n"
    "buffer_load_dwordx4 v32, s[52:55], s88 offen offset:3072 lds\n"
    "v_mfma_f32_16x16x32_bf16 a[72:75], a[144:147], a[184:187], a[72:75]\n"
    "ds_read_b64_tr_b16 a[142:143], v15 offset:12808\n"
    "s_add_u32 s88, s88, 0x20000\n"
    "v_mfma_f32_16x16x32_bf16 a[80:83], a[144:147], a[180:183], a[80:83]\n"
    "ds_read_b128 a[172:175], v62 offset:33792\n"
    "v_mfma_f32_16x16x32_bf16 a[88:91], a[144:147], a[188:191], a[88:91]\n"
    "s_add_u32 m0, s89, 0x1000\n"
    "v_mfma_f32_16x16x32_bf16 a[68:71], a[148:151], a[176:179], a[68:71]\n"
    "ds_read_b64_tr_b16 a[144:145], v15 offset:9216\n"
    "buffer_load_dwordx4 v29, s[52:55], s88 offen lds\n"
    "v_mfma_f32_16x16x32_bf16 a[76:79], a[148:151], a[184:187], a[76:79]\n"
    "ds_read_b64_tr_b16 a[146:147], v15 offset:13312\n"
    "v_mfma_f32_16x16x32_bf16 a[84:87], a[148:151], a[180:183], a[84:87]\n"
    "v_mfma_f32_16x16x32_bf16 a[92:95], a[148:151], a[188:191], a[92:95]\n"
    "buffer_load_dwordx4 v30, s[52:55], s88 offen offset:1024 lds\n"
    "v_mfma_f32_16x16x32_bf16 a[96:99], a[152:155], a[176:179], a[96:99]\n"
    "ds_read_b64_tr_b16 a[148:149], v15 offset:9224\n"
    "v_mfma_f32_16x16x32_bf16 a[104:107], a[152:155], a[184:187], a[104:107]\n"
    "s_waitcnt lgkmcnt(3)\n"
    "ds_read_b64_tr_b16 a[150:151], v15 offset:13320\n"
    "v_mfma_f32_16x16x32_bf16 a[112:115], a[152:155], a[180:183], a[112:115]\n"
    "buffer_load_dwordx4 v31, s[52:55], s88 offen offset:2048 lds\n"
    "v_mfma_f32_16x16x32_bf16 a[120:123], a[152:155], a[188:191], a[120:123]\n"
    "v_mfma_f32_16x16x32_bf16 a[100:103], a[156:159], a[176:179], a[100:103]\n"
    "ds_read_b64_tr_b16 a[152:153], v15 offset:9728\n"
    "v_mfma_f32_16x16x32_bf16 a[108:111], a[156:159], a[184:187], a[108:111]\n"
    "ds_read_b64_tr_b16 a[154:155], v15 offset:13824\n"
    "buffer_load_dwordx4 v32, s[52:55], s88 offen offset:3072 lds\n"
    "v_mfma_f32_16x16x32_bf16 a[116:119], a[156:159], a[180:183], a[116:119]\n"
    "s_add_u32 s88, s88, 0x20000\n"
    "v_mfma_f32_16x16x32_bf16 a[124:127], a[156:159], a[188:191], a[124:127]\n"
    "; PHASE T2\n"
    "v_mfma_f32_16x16x32_bf16 v[128:131], a[128:131], a[160:163], 0\n"
    "ds_read_b64_tr_b16 a[156:157], v15 offset:9736\n"
    "v_mfma_f32_16x16x32_bf16 v[136:139], a[128:131], a[168:171], 0\n"
    "ds_read_b64_tr_b16 a[158:159], v15 offset:13832\n"
    "v_mfma_f32_16x16x32_bf16 v[144:147], a[128:131], a[164:167], 0\n"
    "v_mfma_f32_16x16x32_bf16 v[152:155], a[128:131], a[172:175], 0\n"
    "v_mfma_f32_16x16x32_bf16 v[132:135], a[132:135], a[160:163], 0\n"
    "s_waitcnt vmcnt(12)\n"
    "ds_read_b64_tr_b16 a[128:129], v16\n"
    "s_and_b32 s39, s33, 1\n"
    "v_mfma_f32_16x16x32_bf16 v[140:143], a[132:135], a[168:171], 0\n"
    "s_waitcnt vmcnt(8)\n"
    "ds_read_b64_tr_b16 a[130:131], v16 offset:16384\n"
    "s_lshl_b32 s93, s32, 1\n"
    "v_accvgpr_read_b32 v64, a0\n"
    "v_mfma_f32_16x16x32_bf16 v[148:151], a[132:135], a[164:167], 0\n"
    "ds_read_b128 a[176:179], v63 offset:2048\n"
    "s_add_u32 m0, s89, 0x2000\n"
    "v_accvgpr_read_b32 v65, a1\n"
    "v_mfma_f32_16x16x32_bf16 v[156:159], a[132:135], a[172:175], 0\n"
    "s_waitcnt lgkmcnt(3)\n"
    "buffer_load_dwordx4 v29, s[52:55], s88 offen lds\n"
    "s_add_u32 s93, s93, s39\n"
    "v_accvgpr_read_b32 v66, a2\n"
    "v_mfma_f32_16x16x32_bf16 v[160:163], a[136:139], a[160:163], 0\n"
    "ds_read_b64_tr_b16 a[132:133], v16 offset:8\n"
    "s_cmp_eq_u32 s39, 1\n"
    "s_cselect_b32 s39, 0, 6\n"
    "v_accvgpr_read_b32 v67, a3\n"
    "v_mfma_f32_16x16x32_bf16 v[168:171], a[136:139], a[168:171], 0\n"
    "ds_read_b64_tr_b16 a[134:135], v16 offset:16392\n"
    "v_accvgpr_read_b32 v68, a4\n"
    "v_mfma_f32_16x16x32_bf16 v[176:179], a[136:139], a[164:167], 0\n"
    "ds_read_b128 a[184:187], v63 offset:3072\n"
    "buffer_load_dwordx4 v30, s[52:55], s88 offen offset:1024 lds\n"
    "v_accvgpr_read_b32 v69, a5\n"
    "v_mfma_f32_16x16x32_bf16 v[184:187], a[136:139], a[172:175], 0\n"
    "s_cmp_eq_u32 s32, 3\n"
    "s_cselect_b32 s93, s39, s93\n"
    "s_mul_i32 s39, s33, 7\n"
    "v_accvgpr_read_b32 v70, a6\n"
    "v_mfma_f32_16x16x32_bf16 v[164:167], a[140:143], a[160:163], 0\n"
    "ds_read_b64_tr_b16 a[136:137], v16 offset:512\n"
    "s_add_u32 s93, s93, s39\n"
    "v_accvgpr_read_b32 v71, a7\n"
    "v_mfma_f32_16x16x32_bf16 v[172:175], a[140:143], a[168:171], 0\n"
    "ds_read_b64_tr_b16 a[138:139], v16 offset:16896\n"
    "buffer_load_dwordx4 v31, s[52:55], s88 offen offset:2048 lds\n"
    "v_mul_f32 v80, v64, v64\n"
    "v_mfma_f32_16x16x32_bf16 v[180:183], a[140:143], a[164:167], 0\n"
    "ds_read_b128 a[180:183], v63 offset:34816\n"
    "s_lshl_b32 s93, s93, 12\n"
    "v_mfma_f32_16x16x32_bf16 v[188:191], a[140:143], a[172:175], 0\n"
    "s_mul_i32 s95, s34, 0x38000\n"
    "s_add_u32 s95, s95, s93\n"
    "v_mul_f32 v81, v65, v65\n"
    "v_mfma_f32_16x16x32_bf16 v[192:195], a[144:147], a[160:163], 0\n"
    "ds_read_b64_tr_b16 a[140:141], v16 offset:520\n"
    "buffer_load_dwordx4 v32, s[52:55], s88 offen offset:3072 lds\n"
    "v_mul_f32 v82, v66, v66\n"
    "v_mfma_f32_16x16x32_bf16 v[200:203], a[144:147], a[168:171], 0\n"
    "ds_read_b64_tr_b16 a[142:143], v16 offset:16904\n"
    "s_add_u32 s88, s88, 0x20000\n"
    "v_mul_f32 v83, v67, v67\n"
    "v_mfma_f32_16x16x32_bf16 v[208:211], a[144:147], a[164:167], 0\n"
    "ds_read_b128 a[188:191], v63 offset:35840\n"
    "s_cmp_eq_u32 s32, 3\n"
    "s_cselect_b32 s39, 0x40000000, 0\n"
    "v_mul_f32 v84, v68, v68\n"
    "v_mfma_f32_16x16x32_bf16 v[216:219], a[144:147], a[172:175], 0\n"
    "s_add_u32 m0, s89, 0x3000\n"
    "s_add_u32 s93, s95, s39\n"
    "s_add_u32 s93, s93, 0x1000\n"
    "v_mfma_f32_16x16x32_bf16 v[196:199], a[148:151], a[160:163], 0\n"
    "ds_read_b64_tr_b16 a[144:145], v16 offset:1024\n"
    "buffer_load_dwordx4 v29, s[52:55], s88 offen lds\n"
    "v_mul_f32 v85, v69, v69\n"
    "v_mfma_f32_16x16x32_bf16 v[204:207], a[148:151], a[168:171], 0\n"
    "ds_read_b64_tr_b16 a[146:147], v16 offset:17408\n"
    "v_mul_f32 v86, v70, v70\n"
    "v_mul_f32 v87, v71, v71\n"
    "v_mfma_f32_16x16x32_bf16 v[212:215], a[148:151], a[164:167], 0\n"
    "v_fma_f32 v80, v80, v44, v40\n"
    "v_fma_f32 v81, v81, v44, v40\n"
    "v_fma_f32 v82, v82, v44, v40\n"
    "v_mfma_f32_16x16x32_bf16 v[220:223], a[148:151], a[172:175], 0\n"
    "buffer_load_dwordx4 v30, s[52:55], s88 offen offset:1024 lds\n"
    "v_fma_f32 v83, v83, v44, v40\n"
    "v_fma_f32 v84, v84, v44, v40\n"
    "v_mfma_f32_16x16x32_bf16 v[224:227], a[152:155], a[160:163], 0\n"
    "ds_read_b64_tr_b16 a[148:149], v16 offset:1032\n"
    "v_fma_f32 v85, v85, v44, v40\n"
    "v_mfma_f32_16x16x32_bf16 v[232:235], a[152:155], a[168:171], 0\n"
    "s_waitcnt lgkmcnt(1)\n"
    "ds_read_b64_tr_b16 a[150:151], v16 offset:17416\n"
    "v_fma_f32 v86, v86, v44, v40\n"
    "v_fma_f32 v87, v87, v44, v40\n"
    "v_mfma_f32_16x16x32_bf16 v[240:243], a[152:155], a[164:167], 0\n"
    "buffer_load_dwordx4 v31, s[52:55], s88 offen offset:2048 lds\n"
    "v_mul_f32 v80, v80, v64\n"
    "v_mul_f32 v81, v81, v65\n"
    "v_mfma_f32_16x16x32_bf16 v[248:251], a[152:155], a[172:175], 0\n"
    "v_mul_f32 v82, v82, v66\n"
    "v_mul_f32 v83, v83, v67\n"
    "v_mul_f32 v84, v84, v68\n"
    "v_mfma_f32_16x16x32_bf16 v[228:231], a[156:159], a[160:163], 0\n"
    "ds_read_b64_tr_b16 a[152:153], v16 offset:1536\n"
    "v_mul_f32 v85, v85, v69\n"
    "v_mul_f32 v86, v86, v70\n"
    "v_mfma_f32_16x16x32_bf16 v[236:239], a[156:159], a[168:171], 0\n"
    "ds_read_b64_tr_b16 a[154:155], v16 offset:17920\n"
    "buffer_load_dwordx4 v32, s[52:55], s88 offen offset:3072 lds\n"
    "v_mfma_f32_16x16x32_bf16 v[244:247], a[156:159], a[164:167], 0\n"
    "s_add_u32 s88, s88, 0x20000\n"
    "v_mul_f32 v87, v87, v71\n"
    "v_exp_f32 v80, v80\n"
    "v_mfma_f32_16x16x32_bf16 v[252:255], a[156:159], a[172:175], 0\n"
    "v_exp_f32 v81, v81\n"
    "v_accvgpr_read_b32 v72, a64\n"
    "v_mfma_f32_16x16x32_bf16 v[128:131], a[128:131], a[176:179], v[128:131]\n"
    "ds_read_b64_tr_b16 a[156:157], v16 offset:1544\n"
    "v_exp_f32 v82, v82\n"
    "v_mfma_f32_16x16x32_bf16 v[136:139], a[128:131], a[184:187], v[136:139]\n"
    "ds_read_b64_tr_b16 a[158:159], v16 offset:17928\n"
    "v_exp_f32 v83, v83\n"
    "v_mfma_f32_16x16x32_bf16 v[144:147], a[128:131], a[180:183], v[144:147]\n"
    "v_exp_f32 v84, v84\n"
    "v_mfma_f32_16x16x32_bf16 v[152:155], a[128:131], a[188:191], v[152:155]\n"
    "v_exp_f32 v85, v85\n"
    "v_accvgpr_read_b32 v73, a65\n"
    "v_mfma_f32_16x16x32_bf16 v[132:135], a[132:135], a[176:179], v[132:135]\n"
    "s_waitcnt vmcnt(12)\n"
    "ds_read_b64_tr_b16 a[128:129], v15\n"
    "v_exp_f32 v86, v86\n"
    "v_mfma_f32_16x16x32_bf16 v[140:143], a[132:135], a[184:187], v[140:143]\n"
    "s_waitcnt vmcnt(8)\n"
    "ds_read_b64_tr_b16 a[130:131], v15 offset:4096\n"
    "v_exp_f32 v87, v87\n"
    "v_mfma_f32_16x16x32_bf16 v[148:151], a[132:135], a[180:183], v[148:151]\n"
    "ds_read_b128 a[160:163], v62 offset:4096\n"
    "s_add_u32 m0, s91, 0x10000\n"
    "v_accvgpr_read_b32 v74, a66\n"
    "v_mfma_f32_16x16x32_bf16 v[156:159], a[132:135], a[188:191], v[156:159]\n"
    "s_waitcnt lgkmcnt(3)\n"
    "buffer_load_dwordx4 v29, s[52:55], s88 offen lds\n"
    "v_accvgpr_read_b32 v75, a67\n"
    "v_mfma_f32_16x16x32_bf16 v[160:163], a[136:139], a[176:179], v[160:163]\n"
    "ds_read_b64_tr_b16 a[132:133], v15 offset:8\n"
    "v_accvgpr_read_b32 v76, a68\n"
    "v_accvgpr_read_b32 v77, a69\n"
    "v_mfma_f32_16x16x32_bf16 v[168:171], a[136:139], a[184:187], v[168:171]\n"
    "ds_read_b64_tr_b16 a[134:135], v15 offset:4104\n"
    "v_accvgpr_read_b32 v78, a70\n"
    "v_accvgpr_read_b32 v79, a71\n"
    "v_mfma_f32_16x16x32_bf16 v[176:179], a[136:139], a[180:183], v[176:179]\n"
    "ds_read_b128 a[168:171], v62 offset:5120\n"
    "buffer_load_dwordx4 v30, s[52:55], s88 offen offset:1024 lds\n"
    "v_fma_f32 v80, v80, v48, v48\n"
    "v_mfma_f32_16x16x32_bf16 v[184:187], a[136:139], a[188:191], v[184:187]\n"
    "v_fma_f32 v81, v81, v48, v48\n"
    "v_fma_f32 v82, v82, v48, v48\n"
    "v_fma_f32 v83, v83, v48, v48\n"
    "v_mfma_f32_16x16x32_bf16 v[164:167], a[140:143], a[176:179], v[164:167]\n"
    "ds_read_b64_tr_b16 a[136:137], v15 offset:512\n"
    "v_fma_f32 v84, v84, v48, v48\n"
    "v_mfma_f32_16x16x32_bf16 v[172:175], a[140:143], a[184:187], v[172:175]\n"
    "ds_read_b64_tr_b16 a[138:139], v15 offset:4608\n"
    "buffer_load_dwordx4 v31, s[52:55], s88 offen offset:2048 lds\n"
    "v_fma_f32 v85, v85, v48, v48\n"
    "v_mfma_f32_16x16x32_bf16 v[180:183], a[140:143], a[180:183], v[180:183]\n"
    "ds_read_b128 a[164:167], v62 offset:36864\n"
    "v_fma_f32 v86, v86, v48, v48\n"
    "v_fma_f32 v87, v87, v48, v48\n"
    "v_mfma_f32_16x16x32_bf16 v[188:191], a[140:143], a[188:191], v[188:191]\n"
    "v_rcp_f32 v80, v80\n"
    "v_mul_f32 v64, v64, v72\n"
    "v_mfma_f32_16x16x32_bf16 v[192:195], a[144:147], a[176:179], v[192:195]\n"
    "ds_read_b64_tr_b16 a[140:141], v15 offset:520\n"
    "buffer_load_dwordx4 v32, s[52:55], s88 offen offset:3072 lds\n"
    "v_mul_f32 v65, v65, v73\n"
    "v_mfma_f32_16x16x32_bf16 v[200:203], a[144:147], a[184:187], v[200:203]\n"
    "ds_read_b64_tr_b16 a[142:143], v15 offset:4616\n"
    "s_add_u32 s88, s88, 0x20000\n"
    "v_mfma_f32_16x16x32_bf16 v[208:211], a[144:147], a[180:183], v[208:211]\n"
    "ds_read_b128 a[172:175], v62 offset:37888\n"
    "v_rcp_f32 v81, v81\n"
    "v_mfma_f32_16x16x32_bf16 v[216:219], a[144:147], a[188:191], v[216:219]\n"
    "s_add_u32 m0, s91, 0x14000\n"
    "v_rcp_f32 v82, v82\n"
    "v_mul_f32 v66, v66, v74\n"
    "v_mfma_f32_16x16x32_bf16 v[196:199], a[148:151], a[176:179], v[196:199]\n"
    "ds_read_b64_tr_b16 a[144:145], v15 offset:1024\n"
    "buffer_load_dwordx4 v29, s[52:55], s88 offen lds\n"
    "v_mul_f32 v67, v67, v75\n"
    "v_mfma_f32_16x16x32_bf16 v[204:207], a[148:151], a[184:187], v[204:207]\n"
    "ds_read_b64_tr_b16 a[146:147], v15 offset:5120\n"
    "v_rcp_f32 v83, v83\n"
    "v_mfma_f32_16x16x32_bf16 v[212:215], a[148:151], a[180:183], v[212:215]\n"
    "v_rcp_f32 v84, v84\n"
    "v_mfma_f32_16x16x32_bf16 v[220:223], a[148:151], a[188:191], v[220:223]\n"
    "buffer_load_dwordx4 v30, s[52:55], s88 offen offset:1024 lds\n"
    "v_rcp_f32 v85, v85\n"
    "v_mfma_f32_16x16x32_bf16 v[224:227], a[152:155], a[176:179], v[224:227]\n"
    "ds_read_b64_tr_b16 a[148:149], v15 offset:1032\n"
    "v_rcp_f32 v86, v86\n"
    "v_mfma_f32_16x16x32_bf16 v[232:235], a[152:155], a[184:187], v[232:235]\n"
    "s_waitcnt lgkmcnt(2)\n"
    "ds_read_b64_tr_b16 a[150:151], v15 offset:5128\n"
    "v_rcp_f32 v87, v87\n"
    "v_mfma_f32_16x16x32_bf16 v[240:243], a[152:155], a[180:183], v[240:243]\n"
    "buffer_load_dwordx4 v31, s[52:55], s88 offen offset:2048 lds\n"
    "v_mul_f32 v68, v68, v76\n"
    "v_mul_f32 v69, v69, v77\n"
    "v_mfma_f32_16x16x32_bf16 v[248:251], a[152:155], a[188:191], v[248:251]\n"
    "v_mul_f32 v70, v70, v78\n"
    "v_mul_f32 v71, v71, v79\n"
    "v_mfma_f32_16x16x32_bf16 v[228:231], a[156:159], a[176:179], v[228:231]\n"
    "ds_read_b64_tr_b16 a[152:153], v15 offset:1536\n"
    "v_mul_f32 v64, v64, v80\n"
    "v_mul_f32 v65, v65, v81\n"
    "v_mfma_f32_16x16x32_bf16 v[236:239], a[156:159], a[184:187], v[236:239]\n"
    "ds_read_b64_tr_b16 a[154:155], v15 offset:5632\n"
    "buffer_load_dwordx4 v32, s[52:55], s88 offen offset:3072 lds\n"
    "v_mul_f32 v66, v66, v82\n"
    "v_mfma_f32_16x16x32_bf16 v[244:247], a[156:159], a[180:183], v[244:247]\n"
    "s_add_u32 s88, s88, 0x20000\n"
    "v_mul_f32 v67, v67, v83\n"
    "v_mul_f32 v68, v68, v84\n"
    "v_mul_f32 v69, v69, v85\n"
    "v_mfma_f32_16x16x32_bf16 v[252:255], a[156:159], a[188:191], v[252:255]\n"
    "v_mul_f32 v70, v70, v86\n"
    "v_mul_f32 v71, v71, v87\n"
    "v_cvt_pk_bf16_f32 v88, v64, v65\n"
    "v_mfma_f32_16x16x32_bf16 v[128:131], a[128:131], a[160:163], v[128:131]\n"
    "ds_read_b64_tr_b16 a[156:157], v15 offset:1544\n"
    "v_cvt_pk_bf16_f32 v89, v66, v67\n"
    "v_mfma_f32_16x16x32_bf16 v[136:139], a[128:131], a[168:171], v[136:139]\n"
    "ds_read_b64_tr_b16 a[158:159], v15 offset:5640\n"
    "v_cvt_pk_bf16_f32 v90, v68, v69\n"
    "v_cvt_pk_bf16_f32 v91, v70, v71\n"
    "v_mfma_f32_16x16x32_bf16 v[144:147], a[128:131], a[164:167], v[144:147]\n"
    "v_accvgpr_read_b32 v64, a16\n"
    "v_accvgpr_read_b32 v65, a17\n"
    "v_accvgpr_read_b32 v66, a18\n"
    "v_mfma_f32_16x16x32_bf16 v[152:155], a[128:131], a[172:175], v[152:155]\n"
    "v_accvgpr_read_b32 v67, a19\n"
    "v_accvgpr_read_b32 v68, a20\n"
    "v_accvgpr_read_b32 v69, a21\n"
    "v_mfma_f32_16x16x32_bf16 v[132:135], a[132:135], a[160:163], v[132:135]\n"
    "s_waitcnt vmcnt(12)\n"
    "ds_read_b64_tr_b16 a[128:129], v15 offset:8192\n"
    "v_accvgpr_read_b32 v70, a22\n"
    "v_accvgpr_read_b32 v71, a23\n"
    "v_mfma_f32_16x16x32_bf16 v[140:143], a[132:135], a[168:171], v[140:143]\n"
    "s_waitcnt vmcnt(8)\n"
    "ds_read_b64_tr_b16 a[130:131], v15 offset:12288\n"
    "v_mul_f32 v80, v64, v64\n"
    "v_mfma_f32_16x16x32_bf16 v[148:151], a[132:135], a[164:167], v[148:151]\n"
    "ds_read_b128 a[176:179], v63 offset:6144\n"
    "s_add_u32 m0, s89, 0\n"
    "v_mul_f32 v81, v65, v65\n"
    "v_mfma_f32_16x16x32_bf16 v[156:159], a[132:135], a[172:175], v[156:159]\n"
    "s_waitcnt lgkmcnt(3)\n"
    "buffer_load_dwordx4 v29, s[52:55], s88 offen lds\n"
    "v_mul_f32 v82, v66, v66\n"
    "v_mul_f32 v83, v67, v67\n"
    "v_mfma_f32_16x16x32_bf16 v[160:163], a[136:139], a[160:163], v[160:163]\n"
    "ds_read_b64_tr_b16 a[132:133], v15 offset:8200\n"
    "v_mul_f32 v84, v68, v68\n"
    "v_mul_f32 v85, v69, v69\n"
    "v_mfma_f32_16x16x32_bf16 v[168:171], a[136:139], a[168:171], v[168:171]\n"
    "ds_read_b64_tr_b16 a[134:135], v15 offset:12296\n"
    "v_mul_f32 v86, v70, v70\n"
    "v_mul_f32 v87, v71, v71\n"
    "v_mfma_f32_16x16x32_bf16 v[176:179], a[136:139], a[164:167], v[176:179]\n"
    "ds_read_b128 a[184:187], v63 offset:7168\n"
    "buffer_load_dwordx4 v30, s[52:55], s88 offen offset:1024 lds\n"
    "v_mfma_f32_16x16x32_bf16 v[184:187], a[136:139], a[172:175], v[184:187]\n"
    "v_fma_f32 v80, v80, v46, v42\n"
    "v_fma_f32 v81, v81, v46, v42\n"
    "v_fma_f32 v82, v82, v46, v42\n"
    "v_mfma_f32_16x16x32_bf16 v[164:167], a[140:143], a[160:163], v[164:167]\n"
    "ds_read_b64_tr_b16 a[136:137], v15 offset:8704\n"
    "v_fma_f32 v83, v83, v46, v42\n"
    "v_fma_f32 v84, v84, v46, v42\n"
    "v_mfma_f32_16x16x32_bf16 v[172:175], a[140:143], a[168:171], v[172:175]\n"
    "ds_read_b64_tr_b16 a[138:139], v15 offset:12800\n"
    "buffer_load_dwordx4 v31, s[52:55], s88 offen offset:2048 lds\n"
    "v_fma_f32 v85, v85, v46, v42\n"
    "v_mfma_f32_16x16x32_bf16 v[180:183], a[140:143], a[164:167], v[180:183]\n"
    "ds_read_b128 a[180:183], v63 offset:38912\n"
    "v_fma_f32 v86, v86, v46, v42\n"
    "v_fma_f32 v87, v87, v46, v42\n"
    "v_mfma_f32_16x16x32_bf16 v[188:191], a[140:143], a[172:175], v[188:191]\n"
    "v_mul_f32 v80, v80, v64\n"
    "v_mul_f32 v81, v81, v65\n"
    "v_mfma_f32_16x16x32_bf16 v[192:195], a[144:147], a[160:163], v[192:195]\n"
    "ds_read_b64_tr_b16 a[140:141], v15 offset:8712\n"
    "buffer_load_dwordx4 v32, s[52:55], s88 offen offset:3072 lds\n"
    "v_mul_f32 v82, v82, v66\n"
    "v_mfma_f32_16x16x32_bf16 v[200:203], a[144:147], a[168:171], v[200:203]\n"
    "ds_read_b64_tr_b16 a[142:143], v15 offset:12808\n"
    "s_add_u32 s88, s88, 0x20000\n"
    "v_mul_f32 v83, v83, v67\n"
    "v_mfma_f32_16x16x32_bf16 v[208:211], a[144:147], a[164:167], v[208:211]\n"
    "ds_read_b128 a[188:191], v63 offset:39936\n"
    "v_mul_f32 v84, v84, v68\n"
    "v_mul_f32 v85, v85, v69\n"
    "v_mfma_f32_16x16x32_bf16 v[216:219], a[144:147], a[172:175], v[216:219]\n"
    "s_add_u32 m0, s89, 0x1000\n"
    "v_mul_f32 v86, v86, v70\n"
    "v_mul_f32 v87, v87, v71\n"
    "v_accvgpr_read_b32 v72, a80\n"
    "v_mfma_f32_16x16x32_bf16 v[196:199], a[148:151], a[160:163], v[196:199]\n"
    "ds_read_b64_tr_b16 a[144:145], v15 offset:9216\n"
    "buffer_load_dwordx4 v29, s[52:55], s88 offen lds\n"
    "v_mfma_f32_16x16x32_bf16 v[204:207], a[148:151], a[168:171], v[204:207]\n"
    "ds_read_b64_tr_b16 a[146:147], v15 offset:13312\n"
    "v_exp_f32 v80, v80\n"
    "v_mfma_f32_16x16x32_bf16 v[212:215], a[148:151], a[164:167], v[212:215]\n"
    "v_exp_f32 v81, v81\n"
    "v_accvgpr_read_b32 v73, a81\n"
    "v_mfma_f32_16x16x32_bf16 v[220:223], a[148:151], a[172:175], v[220:223]\n"
    "buffer_load_dwordx4 v30, s[52:55], s88 offen offset:1024 lds\n"
    "v_exp_f32 v82, v82\n"
    "v_mfma_f32_16x16x32_bf16 v[224:227], a[152:155], a[160:163], v[224:227]\n"
    "ds_read_b64_tr_b16 a[148:149], v15 offset:9224\n"
    "v_exp_f32 v83, v83\n"
    "v_mfma_f32_16x16x32_bf16 v[232:235], a[152:155], a[168:171], v[232:235]\n"
    "s_waitcnt lgkmcnt(2)\n"
    "ds_read_b64_tr_b16 a[150:151], v15 offset:13320\n"
    "v_accvgpr_read_b32 v74, a82\n"
    "v_mfma_f32_16x16x32_bf16 v[240:243], a[152:155], a[164:167], v[240:243]\n"
    "buffer_load_dwordx4 v31, s[52:55], s88 offen offset:2048 lds\n"
    "v_exp_f32 v84, v84\n"
    "v_mfma_f32_16x16x32_bf16 v[248:251], a[152:155], a[172:175], v[248:251]\n"
    "v_exp_f32 v85, v85\n"
    "v_accvgpr_read_b32 v75, a83\n"
    "v_mfma_f32_16x16x32_bf16 v[228:231], a[156:159], a[160:163], v[228:231]\n"
    "ds_read_b64_tr_b16 a[152:153], v15 offset:9728\n"
    "v_exp_f32 v86, v86\n"
    "v_mfma_f32_16x16x32_bf16 v[236:239], a[156:159], a[168:171], v[236:239]\n"
    "ds_read_b64_tr_b16 a[154:155], v15 offset:13824\n"
    "buffer_load_dwordx4 v32, s[52:55], s88 offen offset:3072 lds\n"
    "v_accvgpr_read_b32 v76, a84\n"
    "v_mfma_f32_16x16x32_bf16 v[244:247], a[156:159], a[164:167], v[244:247]\n"
    "s_add_u32 s88, s88, 0x20000\n"
    "v_exp_f32 v87, v87\n"
    "v_mfma_f32_16x16x32_bf16 v[252:255], a[156:159], a[172:175], v[252:255]\n"
    "v_accvgpr_read_b32 v77, a85\n"
    "v_accvgpr_read_b32 v78, a86\n"
    "v_accvgpr_read_b32 v79, a87\n"
    "v_mfma_f32_16x16x32_bf16 v[128:131], a[128:131], a[176:179], v[128:131]\n"
    "ds_read_b64_tr_b16 a[156:157], v15 offset:9736\n"
    "v_fma_f32 v80, v80, v50, v50\n"
    "v_fma_f32 v81, v81, v50, v50\n"
    "v_mfma_f32_16x16x32_bf16 v[136:139], a[128:131], a[184:187], v[136:139]\n"
    "ds_read_b64_tr_b16 a[158:159], v15 offset:13832\n"
    "v_fma_f32 v82, v82, v50, v50\n"
    "v_fma_f32 v83, v83, v50, v50\n"
    "v_mfma_f32_16x16x32_bf16 v[144:147], a[128:131], a[180:183], v[144:147]\n"
    "v_fma_f32 v84, v84, v50, v50\n"
    "v_fma_f32 v85, v85, v50, v50\n"
    "v_fma_f32 v86, v86, v50, v50\n"
    "v_mfma_f32_16x16x32_bf16 v[152:155], a[128:131], a[188:191], v[152:155]\n"
    "v_fma_f32 v87, v87, v50, v50\n"
    "v_mul_f32 v64, v64, v72\n"
    "v_mfma_f32_16x16x32_bf16 v[132:135], a[132:135], a[176:179], v[132:135]\n"
    "s_waitcnt vmcnt(12)\n"
    "ds_read_b64_tr_b16 a[128:129], v16\n"
    "v_rcp_f32 v80, v80\n"
    "v_mfma_f32_16x16x32_bf16 v[140:143], a[132:135], a[184:187], v[140:143]\n"
    "s_waitcnt vmcnt(8)\n"
    "ds_read_b64_tr_b16 a[130:131], v16 offset:16384\n"
    "v_rcp_f32 v81, v81\n"
    "v_mfma_f32_16x16x32_bf16 v[148:151], a[132:135], a[180:183], v[148:151]\n"
    "ds_read_b128 a[160:163], v62 offset:8192\n"
    "s_add_u32 m0, s89, 0x2000\n"
    "v_mul_f32 v65, v65, v73\n"
    "v_mfma_f32_16x16x32_bf16 v[156:159], a[132:135], a[188:191], v[156:159]\n"
    "s_waitcnt lgkmcnt(3)\n"
    "buffer_load_dwordx4 v29, s[52:55], s88 offen lds\n"
    "v_rcp_f32 v82, v82\n"
    "v_mfma_f32_16x16x32_bf16 v[160:163], a[136:139], a[176:179], v[160:163]\n"
    "ds_read_b64_tr_b16 a[132:133], v16 offset:8\n"
    "v_mul_f32 v66, v66, v74\n"
    "v_mfma_f32_16x16x32_bf16 v[168:171], a[136:139], a[184:187], v[168:171]\n"
    "ds_read_b64_tr_b16 a[134:135], v16 offset:16392\n"
    "v_rcp_f32 v83, v83\n"
    "v_mfma_f32_16x16x32_bf16 v[176:179], a[136:139], a[180:183], v[176:179]\n"
    "ds_read_b128 a[168:171], v62 offset:9216\n"
    "buffer_load_dwordx4 v30, s[52:55], s88 offen offset:1024 lds\n"
    "v_mul_f32 v67, v67, v75\n"
    "v_mfma_f32_16x16x32_bf16 v[184:187], a[136:139], a[188:191], v[184:187]\n"
    "v_rcp_f32 v84, v84\n"
    "v_mul_f32 v68, v68, v76\n"
    "v_mfma_f32_16x16x32_bf16 v[164:167], a[140:143], a[176:179], v[164:167]\n"
    "ds_read_b64_tr_b16 a[136:137], v16 offset:512\n"
    "v_rcp_f32 v85, v85\n"
    "v_mfma_f32_16x16x32_bf16 v[172:175], a[140:143], a[184:187], v[172:175]\n"
    "ds_read_b64_tr_b16 a[138:139], v16 offset:16896\n"
    "buffer_load_dwordx4 v31, s[52:55], s88 offen offset:2048 lds\n"
    "v_mfma_f32_16x16x32_bf16 v[180:183], a[140:143], a[180:183], v[180:183]\n"
    "ds_read_b128 a[164:167], v62 offset:40960\n"
    "v_rcp_f32 v86, v86\n"
    "v_mfma_f32_16x16x32_bf16 v[188:191], a[140:143], a[188:191], v[188:191]\n"
    "v_rcp_f32 v87, v87\n"
    "v_mul_f32 v69, v69, v77\n"
    "v_mfma_f32_16x16x32_bf16 v[192:195], a[144:147], a[176:179], v[192:195]\n"
    "ds_read_b64_tr_b16 a[140:141], v16 offset:520\n"
    "buffer_load_dwordx4 v32, s[52:55], s88 offen offset:3072 lds\n"
    "v_mul_f32 v70, v70, v78\n"
    "v_mfma_f32_16x16x32_bf16 v[200:203], a[144:147], a[184:187], v[200:203]\n"
    "ds_read_b64_tr_b16 a[142:143], v16 offset:16904\n"
    "s_add_u32 s88, s88, 0x20000\n"
    "v_mul_f32 v71, v71, v79\n"
    "v_mfma_f32_16x16x32_bf16 v[208:211], a[144:147], a[180:183], v[208:211]\n"
    "ds_read_b128 a[172:175], v62 offset:41984\n"
    "v_mul_f32 v64, v64, v80\n"
    "v_mfma_f32_16x16x32_bf16 v[216:219], a[144:147], a[188:191], v[216:219]\n"
    "s_add_u32 m0, s89, 0x3000\n"
    "v_mul_f32 v65, v65, v81\n"
    "v_mul_f32 v66, v66, v82\n"
    "v_mul_f32 v67, v67, v83\n"
    "v_mfma_f32_16x16x32_bf16 v[196:199], a[148:151], a[176:179], v[196:199]\n"
    "ds_read_b64_tr_b16 a[144:145], v16 offset:1024\n"
    "buffer_load_dwordx4 v29, s[52:55], s88 offen lds\n"
    "v_mul_f32 v68, v68, v84\n"
    "v_mfma_f32_16x16x32_bf16 v[204:207], a[148:151], a[184:187], v[204:207]\n"
    "ds_read_b64_tr_b16 a[146:147], v16 offset:17408\n"
    "v_mul_f32 v69, v69, v85\n"
    "v_mul_f32 v70, v70, v86\n"
    "v_mfma_f32_16x16x32_bf16 v[212:215], a[148:151], a[180:183], v[212:215]\n"
    "v_mul_f32 v71, v71, v87\n"
    "v_cvt_pk_bf16_f32 v92, v64, v65\n"
    "v_cvt_pk_bf16_f32 v93, v66, v67\n"
    "v_mfma_f32_16x16x32_bf16 v[220:223], a[148:151], a[188:191], v[220:223]\n"
    "buffer_load_dwordx4 v30, s[52:55], s88 offen offset:1024 lds\n"
    "v_cvt_pk_bf16_f32 v94, v68, v69\n"
    "v_mfma_f32_16x16x32_bf16 v[224:227], a[152:155], a[176:179], v[224:227]\n"
    "ds_read_b64_tr_b16 a[148:149], v16 offset:1032\n"
    "v_cvt_pk_bf16_f32 v95, v70, v71\n"
    "v_accvgpr_read_b32 v64, a8\n"
    "v_mfma_f32_16x16x32_bf16 v[232:235], a[152:155], a[184:187], v[232:235]\n"
    "s_waitcnt lgkmcnt(1)\n"
    "ds_read_b64_tr_b16 a[150:151], v16 offset:17416\n"
    "v_accvgpr_read_b32 v65, a9\n"
    "v_accvgpr_read_b32 v66, a10\n"
    "v_mfma_f32_16x16x32_bf16 v[240:243], a[152:155], a[180:183], v[240:243]\n"
    "buffer_load_dwordx4 v31, s[52:55], s88 offen offset:2048 lds\n"
    "v_accvgpr_read_b32 v67, a11\n"
    "v_accvgpr_read_b32 v68, a12\n"
    "v_mfma_f32_16x16x32_bf16 v[248:251], a[152:155], a[188:191], v[248:251]\n"
    "v_accvgpr_read_b32 v69, a13\n"
    "v_accvgpr_read_b32 v70, a14\n"
    "v_accvgpr_read_b32 v71, a15\n"
    "v_mfma_f32_16x16x32_bf16 v[228:231], a[156:159], a[176:179], v[228:231]\n"
    "ds_read_b64_tr_b16 a[152:153], v16 offset:1536\n"
    "v_mul_f32 v80, v64, v64\n"
    "v_mfma_f32_16x16x32_bf16 v[236:239], a[156:159], a[184:187], v[236:239]\n"
    "ds_read_b64_tr_b16 a[154:155], v16 offset:17920\n"
    "buffer_load_dwordx4 v32, s[52:55], s88 offen offset:3072 lds\n"
    "v_mul_f32 v81, v65, v65\n"
    "v_mfma_f32_16x16x32_bf16 v[244:247], a[156:159], a[180:183], v[244:247]\n"
    "s_add_u32 s88, s88, 0x20000\n"
    "v_mul_f32 v82, v66, v66\n"
    "v_mul_f32 v83, v67, v67\n"
    "v_mul_f32 v84, v68, v68\n"
    "v_mfma_f32_16x16x32_bf16 v[252:255], a[156:159], a[188:191], v[252:255]\n"
    "v_mul_f32 v85, v69, v69\n"
    "v_mul_f32 v86, v70, v70\n"
    "v_mul_f32 v87, v71, v71\n"
    "v_mfma_f32_16x16x32_bf16 v[128:131], a[128:131], a[160:163], v[128:131]\n"
    "ds_read_b64_tr_b16 a[156:157], v16 offset:1544\n"
    "v_fma_f32 v80, v80, v45, v41\n"
    "v_fma_f32 v81, v81, v45, v41\n"
    "v_mfma_f32_16x16x32_bf16 v[136:139], a[128:131], a[168:171], v[136:139]\n"
    "ds_read_b64_tr_b16 a[158:159], v16 offset:17928\n"
    "v_fma_f32 v82, v82, v45, v41\n"
    "v_mfma_f32_16x16x32_bf16 v[144:147], a[128:131], a[164:167], v[144:147]\n"
    "v_fma_f32 v83, v83, v45, v41\n"
    "v_fma_f32 v84, v84, v45, v41\n"
    "v_fma_f32 v85, v85, v45, v41\n"
    "v_mfma_f32_16x16x32_bf16 v[152:155], a[128:131], a[172:175], v[152:155]\n"
    "v_fma_f32 v86, v86, v45, v41\n"
    "v_fma_f32 v87, v87, v45, v41\n"
    "v_mul_f32 v80, v80, v64\n"
    "v_mfma_f32_16x16x32_bf16 v[132:135], a[132:135], a[160:163], v[132:135]\n"
    "s_waitcnt vmcnt(12)\n"
    "ds_read_b64_tr_b16 a[128:129], v15\n"
    "v_mul_f32 v81, v81, v65\n"
    "v_mul_f32 v82, v82, v66\n"
    "v_mfma_f32_16x16x32_bf16 v[140:143], a[132:135], a[168:171], v[140:143]\n"
    "s_waitcnt vmcnt(8)\n"
    "ds_read_b64_tr_b16 a[130:131], v15 offset:4096\n"
    "v_mul_f32 v83, v83, v67\n"
    "v_mul_f32 v84, v84, v68\n"
    "v_mfma_f32_16x16x32_bf16 v[148:151], a[132:135], a[164:167], v[148:151]\n"
    "ds_read_b128 a[176:179], v63 offset:10240\n"
    "s_add_u32 m0, s91, 0x10000\n"
    "v_mfma_f32_16x16x32_bf16 v[156:159], a[132:135], a[172:175], v[156:159]\n"
    "s_waitcnt lgkmcnt(3)\n"
    "buffer_load_dwordx4 v29, s[52:55], s88 offen lds\n"
    "v_mul_f32 v85, v85, v69\n"
    "v_mul_f32 v86, v86, v70\n"
    "v_mfma_f32_16x16x32_bf16 v[160:163], a[136:139], a[160:163], v[160:163]\n"
    "ds_read_b64_tr_b16 a[132:133], v15 offset:8\n"
    "v_mul_f32 v87, v87, v71\n"
    "v_accvgpr_read_b32 v72, a72\n"
    "v_mfma_f32_16x16x32_bf16 v[168:171], a[136:139], a[168:171], v[168:171]\n"
    "ds_read_b64_tr_b16 a[134:135], v15 offset:4104\n"
    "v_exp_f32 v80, v80\n"
    "v_mfma_f32_16x16x32_bf16 v[176:179], a[136:139], a[164:167], v[176:179]\n"
    "ds_read_b128 a[184:187], v63 offset:11264\n"
    "buffer_load_dwordx4 v30, s[52:55], s88 offen offset:1024 lds\n"
    "v_accvgpr_read_b32 v73, a73\n"
    "v_mfma_f32_16x16x32_bf16 v[184:187], a[136:139], a[172:175], v[184:187]\n"
    "v_exp_f32 v81, v81\n"
    "v_mfma_f32_16x16x32_bf16 v[164:167], a[140:143], a[160:163], v[164:167]\n"
    "ds_read_b64_tr_b16 a[136:137], v15 offset:512\n"
    "v_exp_f32 v82, v82\n"
    "v_mfma_f32_16x16x32_bf16 v[172:175], a[140:143], a[168:171], v[172:175]\n"
    "ds_read_b64_tr_b16 a[138:139], v15 offset:4608\n"
    "buffer_load_dwordx4 v31, s[52:55], s88 offen offset:2048 lds\n"
    "v_accvgpr_read_b32 v74, a74\n"
    "v_mfma_f32_16x16x32_bf16 v[180:183], a[140:143], a[164:167], v[180:183]\n"
    "ds_read_b128 a[180:183], v63 offset:43008\n"
    "v_exp_f32 v83, v83\n"
    "v_mfma_f32_16x16x32_bf16 v[188:191], a[140:143], a[172:175], v[188:191]\n"
    "v_exp_f32 v84, v84\n"
    "v_accvgpr_read_b32 v75, a75\n"
    "v_mfma_f32_16x16x32_bf16 v[192:195], a[144:147], a[160:163], v[192:195]\n"
    "ds_read_b64_tr_b16 a[140:141], v15 offset:520\n"
    "buffer_load_dwordx4 v32, s[52:55], s88 offen offset:3072 lds\n"
    "v_mfma_f32_16x16x32_bf16 v[200:203], a[144:147], a[168:171], v[200:203]\n"
    "ds_read_b64_tr_b16 a[142:143], v15 offset:4616\n"
    "s_add_u32 s88, s88, 0x20000\n"
    "v_accvgpr_read_b32 v76, a76\n"
    "v_mfma_f32_16x16x32_bf16 v[208:211], a[144:147], a[164:167], v[208:211]\n"
    "ds_read_b128 a[188:191], v63 offset:44032\n"
    "v_exp_f32 v85, v85\n"
    "v_mfma_f32_16x16x32_bf16 v[216:219], a[144:147], a[172:175], v[216:219]\n"
    "s_add_u32 m0, s91, 0x14000\n"
    "v_exp_f32 v86, v86\n"
    "v_accvgpr_read_b32 v77, a77\n"
    "v_mfma_f32_16x16x32_bf16 v[196:199], a[148:151], a[160:163], v[196:199]\n"
    "ds_read_b64_tr_b16 a[144:145], v15 offset:1024\n"
    "buffer_load_dwordx4 v29, s[52:55], s88 offen lds\n"
    "v_accvgpr_read_b32 v78, a78\n"
    "v_mfma_f32_16x16x32_bf16 v[204:207], a[148:151], a[168:171], v[204:207]\n"
    "ds_read_b64_tr_b16 a[146:147], v15 offset:5120\n"
    "v_accvgpr_read_b32 v79, a79\n"
    "v_mfma_f32_16x16x32_bf16 v[212:215], a[148:151], a[164:167], v[212:215]\n"
    "v_exp_f32 v87, v87\n"
    "v_fma_f32 v80, v80, v49, v49\n"
    "v_mfma_f32_16x16x32_bf16 v[220:223], a[148:151], a[172:175], v[220:223]\n"
    "buffer_load_dwordx4 v30, s[52:55], s88 offen offset:1024 lds\n"
    "v_fma_f32 v81, v81, v49, v49\n"
    "v_fma_f32 v82, v82, v49, v49\n"
    "v_mfma_f32_16x16x32_bf16 v[224:227], a[152:155], a[160:163], v[224:227]\n"
    "ds_read_b64_tr_b16 a[148:149], v15 offset:1032\n"
    "v_fma_f32 v83, v83, v49, v49\n"
    "v_fma_f32 v84, v84, v49, v49\n"
    "v_mfma_f32_16x16x32_bf16 v[232:235], a[152:155], a[168:171], v[232:235]\n"
    "s_waitcnt lgkmcnt(1)\n"
    "ds_read_b64_tr_b16 a[150:151], v15 offset:5128\n"
    "v_fma_f32 v85, v85, v49, v49\n"
    "v_fma_f32 v86, v86, v49, v49\n"
    "v_mfma_f32_16x16x32_bf16 v[240:243], a[152:155], a[164:167], v[240:243]\n"
    "buffer_load_dwordx4 v31, s[52:55], s88 offen offset:2048 lds\n"
    "v_fma_f32 v87, v87, v49, v49\n"
    "v_mfma_f32_16x16x32_bf16 v[248:251], a[152:155], a[172:175], v[248:251]\n"
    "v_rcp_f32 v80, v80\n"
    "v_mul_f32 v64, v64, v72\n"
    "v_mfma_f32_16x16x32_bf16 v[228:231], a[156:159], a[160:163], v[228:231]\n"
    "ds_read_b64_tr_b16 a[152:153], v15 offset:1536\n"
    "v_rcp_f32 v81, v81\n"
    "v_mfma_f32_16x16x32_bf16 v[236:239], a[156:159], a[168:171], v[236:239]\n"
    "ds_read_b64_tr_b16 a[154:155], v15 offset:5632\n"
    "buffer_load_dwordx4 v32, s[52:55], s88 offen offset:3072 lds\n"
    "v_mul_f32 v65, v65, v73\n"
    "v_mfma_f32_16x16x32_bf16 v[244:247], a[156:159], a[164:167], v[244:247]\n"
    "s_add_u32 s88, s88, 0x20000\n"
    "v_rcp_f32 v82, v82\n"
    "v_mul_f32 v66, v66, v74\n"
    "v_mfma_f32_16x16x32_bf16 v[252:255], a[156:159], a[172:175], v[252:255]\n"
    "v_rcp_f32 v83, v83\n"
    "v_mfma_f32_16x16x32_bf16 v[128:131], a[128:131], a[176:179], v[128:131]\n"
    "ds_read_b64_tr_b16 a[156:157], v15 offset:1544\n"
    "v_rcp_f32 v84, v84\n"
    "v_mfma_f32_16x16x32_bf16 v[136:139], a[128:131], a[184:187], v[136:139]\n"
    "ds_read_b64_tr_b16 a[158:159], v15 offset:5640\n"
    "v_rcp_f32 v85, v85\n"
    "v_mfma_f32_16x16x32_bf16 v[144:147], a[128:131], a[180:183], v[144:147]\n"
    "v_rcp_f32 v86, v86\n"
    "v_mul_f32 v67, v67, v75\n"
    "v_mfma_f32_16x16x32_bf16 v[152:155], a[128:131], a[188:191], v[152:155]\n"
    "v_rcp_f32 v87, v87\n"
    "v_mul_f32 v68, v68, v76\n"
    "v_mfma_f32_16x16x32_bf16 v[132:135], a[132:135], a[176:179], v[132:135]\n"
    "s_waitcnt vmcnt(12)\n"
    "ds_read_b64_tr_b16 a[128:129], v15 offset:8192\n"
    "v_mul_f32 v69, v69, v77\n"
    "v_mfma_f32_16x16x32_bf16 v[140:143], a[132:135], a[184:187], v[140:143]\n"
    "s_waitcnt vmcnt(8)\n"
    "ds_read_b64_tr_b16 a[130:131], v15 offset:12288\n"
    "v_mul_f32 v70, v70, v78\n"
    "v_mul_f32 v71, v71, v79\n"
    "v_mfma_f32_16x16x32_bf16 v[148:151], a[132:135], a[180:183], v[148:151]\n"
    "ds_read_b128 a[160:163], v62 offset:12288\n"
    "s_add_u32 m0, s89, 0\n"
    "v_mul_f32 v64, v64, v80\n"
    "v_mfma_f32_16x16x32_bf16 v[156:159], a[132:135], a[188:191], v[156:159]\n"
    "s_waitcnt lgkmcnt(3)\n"
    "buffer_load_dwordx4 v29, s[52:55], s88 offen lds\n"
    "v_mul_f32 v65, v65, v81\n"
    "v_mul_f32 v66, v66, v82\n"
    "v_mfma_f32_16x16x32_bf16 v[160:163], a[136:139], a[176:179], v[160:163]\n"
    "ds_read_b64_tr_b16 a[132:133], v15 offset:8200\n"
    "v_mul_f32 v67, v67, v83\n"
    "v_mul_f32 v68, v68, v84\n"
    "v_mfma_f32_16x16x32_bf16 v[168:171], a[136:139], a[184:187], v[168:171]\n"
    "ds_read_b64_tr_b16 a[134:135], v15 offset:12296\n"
    "v_mul_f32 v69, v69, v85\n"
    "v_mfma_f32_16x16x32_bf16 v[176:179], a[136:139], a[180:183], v[176:179]\n"
    "ds_read_b128 a[168:171], v62 offset:13312\n"
    "buffer_load_dwordx4 v30, s[52:55], s88 offen offset:1024 lds\n"
    "v_mul_f32 v70, v70, v86\n"
    "v_mfma_f32_16x16x32_bf16 v[184:187], a[136:139], a[188:191], v[184:187]\n"
    "v_mul_f32 v71, v71, v87\n"
    "v_cvt_pk_bf16_f32 v96, v64, v65\n"
    "v_cvt_pk_bf16_f32 v97, v66, v67\n"
    "v_mfma_f32_16x16x32_bf16 v[164:167], a[140:143], a[176:179], v[164:167]\n"
    "ds_read_b64_tr_b16 a[136:137], v15 offset:8704\n"
    "v_cvt_pk_bf16_f32 v98, v68, v69\n"
    "v_cvt_pk_bf16_f32 v99, v70, v71\n"
    "v_mfma_f32_16x16x32_bf16 v[172:175], a[140:143], a[184:187], v[172:175]\n"
    "ds_read_b64_tr_b16 a[138:139], v15 offset:12800\n"
    "buffer_load_dwordx4 v31, s[52:55], s88 offen offset:2048 lds\n"
    "v_accvgpr_read_b32 v64, a24\n"
    "v_mfma_f32_16x16x32_bf16 v[180:183], a[140:143], a[180:183], v[180:183]\n"
    "ds_read_b128 a[164:167], v62 offset:45056\n"
    "v_accvgpr_read_b32 v65, a25\n"
    "v_mfma_f32_16x16x32_bf16 v[188:191], a[140:143], a[188:191], v[188:191]\n"
    "v_accvgpr_read_b32 v66, a26\n"
    "v_accvgpr_read_b32 v67, a27\n"
    "v_accvgpr_read_b32 v68, a28\n"
    "v_mfma_f32_16x16x32_bf16 v[192:195], a[144:147], a[176:179], v[192:195]\n"
    "ds_read_b64_tr_b16 a[140:141], v15 offset:8712\n"
    "buffer_load_dwordx4 v32, s[52:55], s88 offen offset:3072 lds\n"
    "v_accvgpr_read_b32 v69, a29\n"
    "v_mfma_f32_16x16x32_bf16 v[200:203], a[144:147], a[184:187], v[200:203]\n"
    "ds_read_b64_tr_b16 a[142:143], v15 offset:12808\n"
    "s_add_u32 s88, s88, 0x20000\n"
    "v_accvgpr_read_b32 v70, a30\n"
    "v_mfma_f32_16x16x32_bf16 v[208:211], a[144:147], a[180:183], v[208:211]\n"
    "ds_read_b128 a[172:175], v62 offset:46080\n"
    "v_accvgpr_read_b32 v71, a31\n"
    "v_mul_f32 v80, v64, v64\n"
    "v_mfma_f32_16x16x32_bf16 v[216:219], a[144:147], a[188:191], v[216:219]\n"
    "s_add_u32 m0, s89, 0x1000\n"
    "v_mul_f32 v81, v65, v65\n"
    "v_mul_f32 v82, v66, v66\n"
    "v_mfma_f32_16x16x32_bf16 v[196:199], a[148:151], a[176:179], v[196:199]\n"
    "ds_read_b64_tr_b16 a[144:145], v15 offset:9216\n"
    "buffer_load_dwordx4 v29, s[52:55], s88 offen lds\n"
    "v_mul_f32 v83, v67, v67\n"
    "v_mfma_f32_16x16x32_bf16 v[204:207], a[148:151], a[184:187], v[204:207]\n"
    "ds_read_b64_tr_b16 a[146:147], v15 offset:13312\n"
    "v_mul_f32 v84, v68, v68\n"
    "v_mul_f32 v85, v69, v69\n"
    "v_mfma_f32_16x16x32_bf16 v[212:215], a[148:151], a[180:183], v[212:215]\n"
    "v_mul_f32 v86, v70, v70\n"
    "v_mul_f32 v87, v71, v71\n"
    "v_fma_f32 v80, v80, v47, v43\n"
    "v_mfma_f32_16x16x32_bf16 v[220:223], a[148:151], a[188:191], v[220:223]\n"
    "buffer_load_dwordx4 v30, s[52:55], s88 offen offset:1024 lds\n"
    "v_fma_f32 v81, v81, v47, v43\n"
    "v_fma_f32 v82, v82, v47, v43\n"
    "v_mfma_f32_16x16x32_bf16 v[224:227], a[152:155], a[176:179], v[224:227]\n"
    "ds_read_b64_tr_b16 a[148:149], v15 offset:9224\n"
    "v_fma_f32 v83, v83, v47, v43\n"
    "v_mfma_f32_16x16x32_bf16 v[232:235], a[152:155], a[184:187], v[232:235]\n"
    "s_waitcnt lgkmcnt(1)\n"
    "ds_read_b64_tr_b16 a[150:151], v15 offset:13320\n"
    "v_fma_f32 v84, v84, v47, v43\n"
    "v_fma_f32 v85, v85, v47, v43\n"
    "v_mfma_f32_16x16x32_bf16 v[240:243], a[152:155], a[180:183], v[240:243]\n"
    "buffer_load_dwordx4 v31, s[52:55], s88 offen offset:2048 lds\n"
    "v_fma_f32 v86, v86, v47, v43\n"
    "v_fma_f32 v87, v87, v47, v43\n"
    "v_mfma_f32_16x16x32_bf16 v[248:251], a[152:155], a[188:191], v[248:251]\n"
    "v_mul_f32 v80, v80, v64\n"
    "v_mul_f32 v81, v81, v65\n"
    "v_mul_f32 v82, v82, v66\n"
    "v_mfma_f32_16x16x32_bf16 v[228:231], a[156:159], a[176:179], v[228:231]\n"
    "ds_read_b64_tr_b16 a[152:153], v15 offset:9728\n"
    "v_mul_f32 v83, v83, v67\n"
    "v_mul_f32 v84, v84, v68\n"
    "v_mfma_f32_16x16x32_bf16 v[236:239], a[156:159], a[184:187], v[236:239]\n"
    "ds_read_b64_tr_b16 a[154:155], v15 offset:13824\n"
    "buffer_load_dwordx4 v32, s[52:55], s88 offen offset:3072 lds\n"
    "v_mfma_f32_16x16x32_bf16 v[244:247], a[156:159], a[180:183], v[244:247]\n"
    "s_add_u32 s88, s88, 0x20000\n"
    "v_mul_f32 v85, v85, v69\n"
    "v_mul_f32 v86, v86, v70\n"
    "v_mul_f32 v87, v87, v71\n"
    "v_mfma_f32_16x16x32_bf16 v[252:255], a[156:159], a[188:191], v[252:255]\n"
    "v_exp_f32 v80, v80\n"
    "v_accvgpr_read_b32 v72, a88\n"
    "v_mfma_f32_16x16x32_bf16 v[128:131], a[128:131], a[160:163], v[128:131]\n"
    "ds_read_b64_tr_b16 a[156:157], v15 offset:9736\n"
    "v_exp_f32 v81, v81\n"
    "v_mfma_f32_16x16x32_bf16 v[136:139], a[128:131], a[168:171], v[136:139]\n"
    "ds_read_b64_tr_b16 a[158:159], v15 offset:13832\n"
    "v_exp_f32 v82, v82\n"
    "v_mfma_f32_16x16x32_bf16 v[144:147], a[128:131], a[164:167], v[144:147]\n"
    "v_exp_f32 v83, v83\n"
    "v_mfma_f32_16x16x32_bf16 v[152:155], a[128:131], a[172:175], v[152:155]\n"
    "v_exp_f32 v84, v84\n"
    "v_accvgpr_read_b32 v73, a89\n"
    "v_mfma_f32_16x16x32_bf16 v[132:135], a[132:135], a[160:163], v[132:135]\n"
    "s_waitcnt vmcnt(12)\n"
    "ds_read_b64_tr_b16 a[128:129], v16\n"
    "v_exp_f32 v85, v85\n"
    "v_mfma_f32_16x16x32_bf16 v[140:143], a[132:135], a[168:171], v[140:143]\n"
    "s_waitcnt vmcnt(8)\n"
    "ds_read_b64_tr_b16 a[130:131], v16 offset:16384\n"
    "v_exp_f32 v86, v86\n"
    "v_mfma_f32_16x16x32_bf16 v[148:151], a[132:135], a[164:167], v[148:151]\n"
    "ds_read_b128 a[176:179], v63 offset:14336\n"
    "s_add_u32 m0, s89, 0x2000\n"
    "v_accvgpr_read_b32 v74, a90\n"
    "v_mfma_f32_16x16x32_bf16 v[156:159], a[132:135], a[172:175], v[156:159]\n"
    "s_waitcnt lgkmcnt(3)\n"
    "buffer_load_dwordx4 v29, s[52:55], s88 offen lds\n"
    "v_accvgpr_read_b32 v75, a91\n"
    "v_mfma_f32_16x16x32_bf16 v[160:163], a[136:139], a[160:163], v[160:163]\n"
    "ds_read_b64_tr_b16 a[132:133], v16 offset:8\n"
    "v_exp_f32 v87, v87\n"
    "v_mfma_f32_16x16x32_bf16 v[168:171], a[136:139], a[168:171], v[168:171]\n"
    "ds_read_b64_tr_b16 a[134:135], v16 offset:16392\n"
    "v_accvgpr_read_b32 v76, a92\n"
    "v_accvgpr_read_b32 v77, a93\n"
    "v_mfma_f32_16x16x32_bf16 v[176:179], a[136:139], a[164:167], v[176:179]\n"
    "ds_read_b128 a[184:187], v63 offset:15360\n"
    "buffer_load_dwordx4 v30, s[52:55], s88 offen offset:1024 lds\n"
    "v_accvgpr_read_b32 v78, a94\n"
    "v_mfma_f32_16x16x32_bf16 v[184:187], a[136:139], a[172:175], v[184:187]\n"
    "v_accvgpr_read_b32 v79, a95\n"
    "v_fma_f32 v80, v80, v51, v51\n"
    "v_fma_f32 v81, v81, v51, v51\n"
    "v_mfma_f32_16x16x32_bf16 v[164:167], a[140:143], a[160:163], v[164:167]\n"
    "ds_read_b64_tr_b16 a[136:137], v16 offset:512\n"
    "v_fma_f32 v82, v82, v51, v51\n"
    "v_mfma_f32_16x16x32_bf16 v[172:175], a[140:143], a[168:171], v[172:175]\n"
    "ds_read_b64_tr_b16 a[138:139], v16 offset:16896\n"
    "buffer_load_dwordx4 v31, s[52:55], s88 offen offset:2048 lds\n"
    "v_fma_f32 v83, v83, v51, v51\n"
    "v_mfma_f32_16x16x32_bf16 v[180:183], a[140:143], a[164:167], v[180:183]\n"
    "ds_read_b128 a[180:183], v63 offset:47104\n"
    "v_fma_f32 v84, v84, v51, v51\n"
    "v_fma_f32 v85, v85, v51, v51\n"
    "v_mfma_f32_16x16x32_bf16 v[188:191], a[140:143], a[172:175], v[188:191]\n"
    "v_fma_f32 v86, v86, v51, v51\n"
    "v_fma_f32 v87, v87, v51, v51\n"
    "v_mul_f32 v64, v64, v72\n"
    "v_mfma_f32_16x16x32_bf16 v[192:195], a[144:147], a[160:163], v[192:195]\n"
    "ds_read_b64_tr_b16 a[140:141], v16 offset:520\n"
    "buffer_load_dwordx4 v32, s[52:55], s88 offen offset:3072 lds\n"
    "v_mul_f32 v65, v65, v73\n"
    "v_mfma_f32_16x16x32_bf16 v[200:203], a[144:147], a[168:171], v[200:203]\n"
    "ds_read_b64_tr_b16 a[142:143], v16 offset:16904\n"
    "s_add_u32 s88, s88, 0x20000\n"
    "v_mfma_f32_16x16x32_bf16 v[208:211], a[144:147], a[164:167], v[208:211]\n"
    "ds_read_b128 a[188:191], v63 offset:48128\n"
    "v_rcp_f32 v80, v80\n"
    "v_mfma_f32_16x16x32_bf16 v[216:219], a[144:147], a[172:175], v[216:219]\n"
    "s_add_u32 m0, s89, 0x3000\n"
    "v_rcp_f32 v81, v81\n"
    "v_mul_f32 v66, v66, v74\n"
    "v_mfma_f32_16x16x32_bf16 v[196:199], a[148:151], a[160:163], v[196:199]\n"
    "ds_read_b64_tr_b16 a[144:145], v16 offset:1024\n"
    "buffer_load_dwordx4 v29, s[52:55], s88 offen lds\n"
    "v_mul_f32 v67, v67, v75\n"
    "v_mfma_f32_16x16x32_bf16 v[204:207], a[148:151], a[168:171], v[204:207]\n"
    "ds_read_b64_tr_b16 a[146:147], v16 offset:17408\n"
    "v_rcp_f32 v82, v82\n"
    "v_mfma_f32_16x16x32_bf16 v[212:215], a[148:151], a[164:167], v[212:215]\n"
    "v_rcp_f32 v83, v83\n"
    "v_mfma_f32_16x16x32_bf16 v[220:223], a[148:151], a[172:175], v[220:223]\n"
    "buffer_load_dwordx4 v30, s[52:55], s88 offen offset:1024 lds\n"
    "v_rcp_f32 v84, v84\n"
    "v_mfma_f32_16x16x32_bf16 v[224:227], a[152:155], a[160:163], v[224:227]\n"
    "ds_read_b64_tr_b16 a[148:149], v16 offset:1032\n"
    "v_rcp_f32 v85, v85\n"
    "v_mfma_f32_16x16x32_bf16 v[232:235], a[152:155], a[168:171], v[232:235]\n"
    "s_waitcnt lgkmcnt(2)\n"
    "ds_read_b64_tr_b16 a[150:151], v16 offset:17416\n"
    "v_rcp_f32 v86, v86\n"
    "v_mfma_f32_16x16x32_bf16 v[240:243], a[152:155], a[164:167], v[240:243]\n"
    "buffer_load_dwordx4 v31, s[52:55], s88 offen offset:2048 lds\n"
    "v_rcp_f32 v87, v87\n"
    "v_mfma_f32_16x16x32_bf16 v[248:251], a[152:155], a[172:175], v[248:251]\n"
    "v_mul_f32 v68, v68, v76\n"
    "v_mul_f32 v69, v69, v77\n"
    "v_mfma_f32_16x16x32_bf16 v[228:231], a[156:159], a[160:163], v[228:231]\n"
    "ds_read_b64_tr_b16 a[152:153], v16 offset:1536\n"
    "v_mul_f32 v70, v70, v78\n"
    "v_mul_f32 v71, v71, v79\n"
    "v_mfma_f32_16x16x32_bf16 v[236:239], a[156:159], a[168:171], v[236:239]\n"
    "ds_read_b64_tr_b16 a[154:155], v16 offset:17920\n"
    "buffer_load_dwordx4 v32, s[52:55], s88 offen offset:3072 lds\n"
    "v_mul_f32 v64, v64, v80\n"
    "v_mfma_f32_16x16x32_bf16 v[244:247], a[156:159], a[164:167], v[244:247]\n"
    "s_add_u32 s88, s88, 0x20000\n"
    "v_mul_f32 v65, v65, v81\n"
    "v_mul_f32 v66, v66, v82\n"
    "v_mul_f32 v67, v67, v83\n"
    "v_mfma_f32_16x16x32_bf16 v[252:255], a[156:159], a[172:175], v[252:255]\n"
    "v_mul_f32 v68, v68, v84\n"
    "v_mul_f32 v69, v69, v85\n"
    "v_mul_f32 v70, v70, v86\n"
    "v_mfma_f32_16x16x32_bf16 v[128:131], a[128:131], a[176:179], v[128:131]\n"
    "ds_read_b64_tr_b16 a[156:157], v16 offset:1544\n"
    "v_mul_f32 v71, v71, v87\n"
    "v_mfma_f32_16x16x32_bf16 v[136:139], a[128:131], a[184:187], v[136:139]\n"
    "ds_read_b64_tr_b16 a[158:159], v16 offset:17928\n"
    "v_cvt_pk_bf16_f32 v100, v64, v65\n"
    "v_cvt_pk_bf16_f32 v101, v66, v67\n"
    "v_mfma_f32_16x16x32_bf16 v[144:147], a[128:131], a[180:183], v[144:147]\n"
    "v_cvt_pk_bf16_f32 v102, v68, v69\n"
    "v_cvt_pk_bf16_f32 v103, v70, v71\n"
    "v_accvgpr_read_b32 v64, a32\n"
    "v_mfma_f32_16x16x32_bf16 v[152:155], a[128:131], a[188:191], v[152:155]\n"
    "v_accvgpr_read_b32 v65, a33\n"
    "v_accvgpr_read_b32 v66, a34\n"
    "v_accvgpr_read_b32 v67, a35\n"
    "v_mfma_f32_16x16x32_bf16 v[132:135], a[132:135], a[176:179], v[132:135]\n"
    "s_waitcnt vmcnt(12)\n"
    "ds_read_b64_tr_b16 a[128:129], v15\n"
    "v_accvgpr_read_b32 v68, a36\n"
    "v_accvgpr_read_b32 v69, a37\n"
    "v_mfma_f32_16x16x32_bf16 v[140:143], a[132:135], a[184:187], v[140:143]\n"
    "s_waitcnt vmcnt(8)\n"
    "ds_read_b64_tr_b16 a[130:131], v15 offset:4096\n"
    "v_accvgpr_read_b32 v70, a38\n"
    "v_mfma_f32_16x16x32_bf16 v[148:151], a[132:135], a[180:183], v[148:151]\n"
    "ds_read_b128 a[160:163], v62 offset:16384\n"
    "s_add_u32 m0, s91, 0x10000\n"
    "v_accvgpr_read_b32 v71, a39\n"
    "v_mfma_f32_16x16x32_bf16 v[156:159], a[132:135], a[188:191], v[156:159]\n"
    "s_waitcnt lgkmcnt(3)\n"
    "buffer_load_dwordx4 v29, s[52:55], s88 offen lds\n"
    "v_mul_f32 v80, v64, v64\n"
    "v_mul_f32 v81, v65, v65\n"
    "v_mfma_f32_16x16x32_bf16 v[160:163], a[136:139], a[176:179], v[160:163]\n"
    "ds_read_b64_tr_b16 a[132:133], v15 offset:8\n"
    "v_mul_f32 v82, v66, v66\n"
    "v_mul_f32 v83, v67, v67\n"
    "v_mfma_f32_16x16x32_bf16 v[168:171], a[136:139], a[184:187], v[168:171]\n"
    "ds_read_b64_tr_b16 a[134:135], v15 offset:4104\n"
    "v_mul_f32 v84, v68, v68\n"
    "v_mul_f32 v85, v69, v69\n"
    "v_mfma_f32_16x16x32_bf16 v[176:179], a[136:139], a[180:183], v[176:179]\n"
    "ds_read_b128 a[168:171], v62 offset:17408\n"
    "buffer_load_dwordx4 v30, s[52:55], s88 offen offset:1024 lds\n"
    "v_mfma_f32_16x16x32_bf16 v[184:187], a[136:139], a[188:191], v[184:187]\n"
    "v_mul_f32 v86, v70, v70\n"
    "v_mul_f32 v87, v71, v71\n"
    "v_fma_f32 v80, v80, v44, v40\n"
    "v_mfma_f32_16x16x32_bf16 v[164:167], a[140:143], a[176:179], v[164:167]\n"
    "ds_read_b64_tr_b16 a[136:137], v15 offset:512\n"
    "v_fma_f32 v81, v81, v44, v40\n"
    "v_fma_f32 v82, v82, v44, v40\n"
    "v_mfma_f32_16x16x32_bf16 v[172:175], a[140:143], a[184:187], v[172:175]\n"
    "ds_read_b64_tr_b16 a[138:139], v15 offset:4608\n"
    "buffer_load_dwordx4 v31, s[52:55], s88 offen offset:2048 lds\n"
    "v_fma_f32 v83, v83, v44, v40\n"
    "v_mfma_f32_16x16x32_bf16 v[180:183], a[140:143], a[180:183], v[180:183]\n"
    "ds_read_b128 a[164:167], v62 offset:49152\n"
    "v_fma_f32 v84, v84, v44, v40\n"
    "v_fma_f32 v85, v85, v44, v40\n"
    "v_mfma_f32_16x16x32_bf16 v[188:191], a[140:143], a[188:191], v[188:191]\n"
    "v_fma_f32 v86, v86, v44, v40\n"
    "v_fma_f32 v87, v87, v44, v40\n"
    "v_mfma_f32_16x16x32_bf16 v[192:195], a[144:147], a[176:179], v[192:195]\n"
    "ds_read_b64_tr_b16 a[140:141], v15 offset:520\n"
    "buffer_load_dwordx4 v32, s[52:55], s88 offen offset:3072 lds\n"
    "v_mul_f32 v80, v80, v64\n"
    "v_mfma_f32_16x16x32_bf16 v[200:203], a[144:147], a[184:187], v[200:203]\n"
    "ds_read_b64_tr_b16 a[142:143], v15 offset:4616\n"
    "s_add_u32 s88, s88, 0x20000\n"
    "v_mul_f32 v81, v81, v65\n"
    "v_mfma_f32_16x16x32_bf16 v[208:211], a[144:147], a[180:183], v[208:211]\n"
    "ds_read_b128 a[172:175], v62 offset:50176\n"
    "v_mul_f32 v82, v82, v66\n"
    "v_mul_f32 v83, v83, v67\n"
    "v_mfma_f32_16x16x32_bf16 v[216:219], a[144:147], a[188:191], v[216:219]\n"
    "s_add_u32 m0, s91, 0x14000\n"
    "v_mul_f32 v84, v84, v68\n"
    "v_mul_f32 v85, v85, v69\n"
    "v_mul_f32 v86, v86, v70\n"
    "v_mfma_f32_16x16x32_bf16 v[196:199], a[148:151], a[176:179], v[196:199]\n"
    "ds_read_b64_tr_b16 a[144:145], v15 offset:1024\n"
    "buffer_load_dwordx4 v29, s[52:55], s88 offen lds\n"
    "v_mfma_f32_16x16x32_bf16 v[204:207], a[148:151], a[184:187], v[204:207]\n"
    "ds_read_b64_tr_b16 a[146:147], v15 offset:5120\n"
    "v_mul_f32 v87, v87, v71\n"
    "v_accvgpr_read_b32 v72, a96\n"
    "v_mfma_f32_16x16x32_bf16 v[212:215], a[148:151], a[180:183], v[212:215]\n"
    "v_exp_f32 v80, v80\n"
    "v_accvgpr_read_b32 v73, a97\n"
    "v_mfma_f32_16x16x32_bf16 v[220:223], a[148:151], a[188:191], v[220:223]\n"
    "buffer_load_dwordx4 v30, s[52:55], s88 offen offset:1024 lds\n"
    "v_exp_f32 v81, v81\n"
    "v_mfma_f32_16x16x32_bf16 v[224:227], a[152:155], a[176:179], v[224:227]\n"
    "ds_read_b64_tr_b16 a[148:149], v15 offset:1032\n"
    "v_exp_f32 v82, v82\n"
    "v_mfma_f32_16x16x32_bf16 v[232:235], a[152:155], a[184:187], v[232:235]\n"
    "s_waitcnt lgkmcnt(2)\n"
    "ds_read_b64_tr_b16 a[150:151], v15 offset:5128\n"
    "v_accvgpr_read_b32 v74, a98\n"
    "v_mfma_f32_16x16x32_bf16 v[240:243], a[152:155], a[180:183], v[240:243]\n"
    "buffer_load_dwordx4 v31, s[52:55], s88 offen offset:2048 lds\n"
    "v_exp_f32 v83, v83\n"
    "v_mfma_f32_16x16x32_bf16 v[248:251], a[152:155], a[188:191], v[248:251]\n"
    "v_exp_f32 v84, v84\n"
    "v_accvgpr_read_b32 v75, a99\n"
    "v_mfma_f32_16x16x32_bf16 v[228:231], a[156:159], a[176:179], v[228:231]\n"
    "ds_read_b64_tr_b16 a[152:153], v15 offset:1536\n"
    "v_exp_f32 v85, v85\n"
    "v_mfma_f32_16x16x32_bf16 v[236:239], a[156:159], a[184:187], v[236:239]\n"
    "ds_read_b64_tr_b16 a[154:155], v15 offset:5632\n"
    "buffer_load_dwordx4 v32, s[52:55], s88 offen offset:3072 lds\n"
    "v_accvgpr_read_b32 v76, a100\n"
    "v_mfma_f32_16x16x32_bf16 v[244:247], a[156:159], a[180:183], v[244:247]\n"
    "s_add_u32 s88, s88, 0x20000\n"
    "v_exp_f32 v86, v86\n"
    "v_mfma_f32_16x16x32_bf16 v[252:255], a[156:159], a[188:191], v[252:255]\n"
    "v_exp_f32 v87, v87\n"
    "v_accvgpr_read_b32 v77, a101\n"
    "v_mfma_f32_16x16x32_bf16 v[128:131], a[128:131], a[160:163], v[128:131]\n"
    "ds_read_b64_tr_b16 a[156:157], v15 offset:1544\n"
    "v_accvgpr_read_b32 v78, a102\n"
    "v_accvgpr_read_b32 v79, a103\n"
    "v_mfma_f32_16x16x32_bf16 v[136:139], a[128:131], a[168:171], v[136:139]\n"
    "ds_read_b64_tr_b16 a[158:159], v15 offset:5640\n"
    "v_fma_f32 v80, v80, v48, v48\n"
    "v_fma_f32 v81, v81, v48, v48\n"
    "v_mfma_f32_16x16x32_bf16 v[144:147], a[128:131], a[164:167], v[144:147]\n"
    "v_fma_f32 v82, v82, v48, v48\n"
    "v_fma_f32 v83, v83, v48, v48\n"
    "v_fma_f32 v84, v84, v48, v48\n"
    "v_mfma_f32_16x16x32_bf16 v[152:155], a[128:131], a[172:175], v[152:155]\n"
    "v_fma_f32 v85, v85, v48, v48\n"
    "v_fma_f32 v86, v86, v48, v48\n"
    "v_mfma_f32_16x16x32_bf16 v[132:135], a[132:135], a[160:163], v[132:135]\n"
    "s_waitcnt vmcnt(12)\n"
    "ds_read_b64_tr_b16 a[128:129], v15 offset:8192\n"
    "v_fma_f32 v87, v87, v48, v48\n"
    "v_mul_f32 v64, v64, v72\n"
    "v_mfma_f32_16x16x32_bf16 v[140:143], a[132:135], a[168:171], v[140:143]\n"
    "s_waitcnt vmcnt(8)\n"
    "ds_read_b64_tr_b16 a[130:131], v15 offset:12288\n"
    "v_rcp_f32 v80, v80\n"
    "v_mfma_f32_16x16x32_bf16 v[148:151], a[132:135], a[164:167], v[148:151]\n"
    "ds_read_b128 a[176:179], v63 offset:18432\n"
    "s_add_u32 m0, s89, 0\n"
    "v_mul_f32 v65, v65, v73\n"
    "v_mfma_f32_16x16x32_bf16 v[156:159], a[132:135], a[172:175], v[156:159]\n"
    "s_waitcnt lgkmcnt(3)\n"
    "buffer_load_dwordx4 v29, s[52:55], s88 offen lds\n"
    "v_rcp_f32 v81, v81\n"
    "v_mfma_f32_16x16x32_bf16 v[160:163], a[136:139], a[160:163], v[160:163]\n"
    "ds_read_b64_tr_b16 a[132:133], v15 offset:8200\n"
    "v_mul_f32 v66, v66, v74\n"
    "v_mfma_f32_16x16x32_bf16 v[168:171], a[136:139], a[168:171], v[168:171]\n"
    "ds_read_b64_tr_b16 a[134:135], v15 offset:12296\n"
    "v_rcp_f32 v82, v82\n"
    "v_mfma_f32_16x16x32_bf16 v[176:179], a[136:139], a[164:167], v[176:179]\n"
    "ds_read_b128 a[184:187], v63 offset:19456\n"
    "buffer_load_dwordx4 v30, s[52:55], s88 offen offset:1024 lds\n"
    "v_mul_f32 v67, v67, v75\n"
    "v_mfma_f32_16x16x32_bf16 v[184:187], a[136:139], a[172:175], v[184:187]\n"
    "v_rcp_f32 v83, v83\n"
    "v_mul_f32 v68, v68, v76\n"
    "v_mfma_f32_16x16x32_bf16 v[164:167], a[140:143], a[160:163], v[164:167]\n"
    "ds_read_b64_tr_b16 a[136:137], v15 offset:8704\n"
    "v_rcp_f32 v84, v84\n"
    "v_mfma_f32_16x16x32_bf16 v[172:175], a[140:143], a[168:171], v[172:175]\n"
    "ds_read_b64_tr_b16 a[138:139], v15 offset:12800\n"
    "buffer_load_dwordx4 v31, s[52:55], s88 offen offset:2048 lds\n"
    "v_mfma_f32_16x16x32_bf16 v[180:183], a[140:143], a[164:167], v[180:183]\n"
    "ds_read_b128 a[180:183], v63 offset:51200\n"
    "v_rcp_f32 v85, v85\n"
    "v_mfma_f32_16x16x32_bf16 v[188:191], a[140:143], a[172:175], v[188:191]\n"
    "v_rcp_f32 v86, v86\n"
    "v_mul_f32 v69, v69, v77\n"
    "v_mfma_f32_16x16x32_bf16 v[192:195], a[144:147], a[160:163], v[192:195]\n"
    "ds_read_b64_tr_b16 a[140:141], v15 offset:8712\n"
    "buffer_load_dwordx4 v32, s[52:55], s88 offen offset:3072 lds\n"
    "v_mul_f32 v70, v70, v78\n"
    "v_mfma_f32_16x16x32_bf16 v[200:203], a[144:147], a[168:171], v[200:203]\n"
    "ds_read_b64_tr_b16 a[142:143], v15 offset:12808\n"
    "s_add_u32 s88, s88, 0x20000\n"
    "v_mul_f32 v71, v71, v79\n"
    "v_mfma_f32_16x16x32_bf16 v[208:211], a[144:147], a[164:167], v[208:211]\n"
    "ds_read_b128 a[188:191], v63 offset:52224\n"
    "v_mul_f32 v64, v64, v80\n"
    "v_mfma_f32_16x16x32_bf16 v[216:219], a[144:147], a[172:175], v[216:219]\n"
    "s_add_u32 m0, s89, 0x1000\n"
    "v_rcp_f32 v87, v87\n"
    "v_mul_f32 v65, v65, v81\n"
    "v_mfma_f32_16x16x32_bf16 v[196:199], a[148:151], a[160:163], v[196:199]\n"
    "ds_read_b64_tr_b16 a[144:145], v15 offset:9216\n"
    "buffer_load_dwordx4 v29, s[52:55], s88 offen lds\n"
    "v_mul_f32 v66, v66, v82\n"
    "v_mfma_f32_16x16x32_bf16 v[204:207], a[148:151], a[168:171], v[204:207]\n"
    "ds_read_b64_tr_b16 a[146:147], v15 offset:13312\n"
    "v_mul_f32 v67, v67, v83\n"
    "v_mul_f32 v68, v68, v84\n"
    "v_mfma_f32_16x16x32_bf16 v[212:215], a[148:151], a[164:167], v[212:215]\n"
    "v_mul_f32 v69, v69, v85\n"
    "v_mul_f32 v70, v70, v86\n"
    "v_mul_f32 v71, v71, v87\n"
    "v_mfma_f32_16x16x32_bf16 v[220:223], a[148:151], a[172:175], v[220:223]\n"
    "buffer_load_dwordx4 v30, s[52:55], s88 offen offset:1024 lds\n"
    "v_cvt_pk_bf16_f32 v104, v64, v65\n"
    "v_mfma_f32_16x16x32_bf16 v[224:227], a[152:155], a[160:163], v[224:227]\n"
    "ds_read_b64_tr_b16 a[148:149], v15 offset:9224\n"
    "v_cvt_pk_bf16_f32 v105, v66, v67\n"
    "v_cvt_pk_bf16_f32 v106, v68, v69\n"
    "v_mfma_f32_16x16x32_bf16 v[232:235], a[152:155], a[168:171], v[232:235]\n"
    "s_waitcnt lgkmcnt(1)\n"
    "ds_read_b64_tr_b16 a[150:151], v15 offset:13320\n"
    "v_cvt_pk_bf16_f32 v107, v70, v71\n"
    "v_accvgpr_read_b32 v64, a48\n"
    "v_mfma_f32_16x16x32_bf16 v[240:243], a[152:155], a[164:167], v[240:243]\n"
    "buffer_load_dwordx4 v31, s[52:55], s88 offen offset:2048 lds\n"
    "v_accvgpr_read_b32 v65, a49\n"
    "v_accvgpr_read_b32 v66, a50\n"
    "v_mfma_f32_16x16x32_bf16 v[248:251], a[152:155], a[172:175], v[248:251]\n"
    "v_accvgpr_read_b32 v67, a51\n"
    "v_accvgpr_read_b32 v68, a52\n"
    "v_accvgpr_read_b32 v69, a53\n"
    "v_mfma_f32_16x16x32_bf16 v[228:231], a[156:159], a[160:163], v[228:231]\n"
    "ds_read_b64_tr_b16 a[152:153], v15 offset:9728\n"
    "v_accvgpr_read_b32 v70, a54\n"
    "v_mfma_f32_16x16x32_bf16 v[236:239], a[156:159], a[168:171], v[236:239]\n"
    "ds_read_b64_tr_b16 a[154:155], v15 offset:13824\n"
    "buffer_load_dwordx4 v32, s[52:55], s88 offen offset:3072 lds\n"
    "v_accvgpr_read_b32 v71, a55\n"
    "v_mfma_f32_16x16x32_bf16 v[244:247], a[156:159], a[164:167], v[244:247]\n"
    "s_add_u32 s88, s88, 0x20000\n"
    "v_mul_f32 v80, v64, v64\n"
    "v_mul_f32 v81, v65, v65\n"
    "v_mul_f32 v82, v66, v66\n"
    "v_mfma_f32_16x16x32_bf16 v[252:255], a[156:159], a[172:175], v[252:255]\n"
    "v_mul_f32 v83, v67, v67\n"
    "v_mul_f32 v84, v68, v68\n"
    "v_mul_f32 v85, v69, v69\n"
    "v_mfma_f32_16x16x32_bf16 v[128:131], a[128:131], a[176:179], v[128:131]\n"
    "ds_read_b64_tr_b16 a[156:157], v15 offset:9736\n"
    "v_mul_f32 v86, v70, v70\n"
    "v_mul_f32 v87, v71, v71\n"
    "v_mfma_f32_16x16x32_bf16 v[136:139], a[128:131], a[184:187], v[136:139]\n"
    "ds_read_b64_tr_b16 a[158:159], v15 offset:13832\n"
    "v_fma_f32 v80, v80, v46, v42\n"
    "v_mfma_f32_16x16x32_bf16 v[144:147], a[128:131], a[180:183], v[144:147]\n"
    "v_fma_f32 v81, v81, v46, v42\n"
    "v_fma_f32 v82, v82, v46, v42\n"
    "v_fma_f32 v83, v83, v46, v42\n"
    "v_mfma_f32_16x16x32_bf16 v[152:155], a[128:131], a[188:191], v[152:155]\n"
    "v_fma_f32 v84, v84, v46, v42\n"
    "v_fma_f32 v85, v85, v46, v42\n"
    "v_fma_f32 v86, v86, v46, v42\n"
    "v_mfma_f32_16x16x32_bf16 v[132:135], a[132:135], a[176:179], v[132:135]\n"
    "s_waitcnt vmcnt(12)\n"
    "ds_read_b64_tr_b16 a[128:129], v16\n"
    "v_fma_f32 v87, v87, v46, v42\n"
    "v_mul_f32 v80, v80, v64\n"
    "v_mfma_f32_16x16x32_bf16 v[140:143], a[132:135], a[184:187], v[140:143]\n"
    "s_waitcnt vmcnt(8)\n"
    "ds_read_b64_tr_b16 a[130:131], v16 offset:16384\n"
    "v_mul_f32 v81, v81, v65\n"
    "v_mul_f32 v82, v82, v66\n"
    "v_mfma_f32_16x16x32_bf16 v[148:151], a[132:135], a[180:183], v[148:151]\n"
    "ds_read_b128 a[160:163], v62 offset:20480\n"
    "s_add_u32 m0, s89, 0x2000\n"
    "v_mfma_f32_16x16x32_bf16 v[156:159], a[132:135], a[188:191], v[156:159]\n"
    "s_waitcnt lgkmcnt(3)\n"
    "buffer_load_dwordx4 v29, s[52:55], s88 offen lds\n"
    "v_mul_f32 v83, v83, v67\n"
    "v_mul_f32 v84, v84, v68\n"
    "v_mfma_f32_16x16x32_bf16 v[160:163], a[136:139], a[176:179], v[160:163]\n"
    "ds_read_b64_tr_b16 a[132:133], v16 offset:8\n"
    "v_mul_f32 v85, v85, v69\n"
    "v_mul_f32 v86, v86, v70\n"
    "v_mfma_f32_16x16x32_bf16 v[168:171], a[136:139], a[184:187], v[168:171]\n"
    "ds_read_b64_tr_b16 a[134:135], v16 offset:16392\n"
    "v_mul_f32 v87, v87, v71\n"
    "v_accvgpr_read_b32 v72, a112\n"
    "v_mfma_f32_16x16x32_bf16 v[176:179], a[136:139], a[180:183], v[176:179]\n"
    "ds_read_b128 a[168:171], v62 offset:21504\n"
    "buffer_load_dwordx4 v30, s[52:55], s88 offen offset:1024 lds\n"
    "v_accvgpr_read_b32 v73, a113\n"
    "v_mfma_f32_16x16x32_bf16 v[184:187], a[136:139], a[188:191], v[184:187]\n"
    "v_exp_f32 v80, v80\n"
    "v_mfma_f32_16x16x32_bf16 v[164:167], a[140:143], a[176:179], v[164:167]\n"
    "ds_read_b64_tr_b16 a[136:137], v16 offset:512\n"
    "v_exp_f32 v81, v81\n"
    "v_mfma_f32_16x16x32_bf16 v[172:175], a[140:143], a[184:187], v[172:175]\n"
    "ds_read_b64_tr_b16 a[138:139], v16 offset:16896\n"
    "buffer_load_dwordx4 v31, s[52:55], s88 offen offset:2048 lds\n"
    "v_accvgpr_read_b32 v74, a114\n"
    "v_mfma_f32_16x16x32_bf16 v[180:183], a[140:143], a[180:183], v[180:183]\n"
    "ds_read_b128 a[164:167], v62 offset:53248\n"
    "v_exp_f32 v82, v82\n"
    "v_mfma_f32_16x16x32_bf16 v[188:191], a[140:143], a[188:191], v[188:191]\n"
    "v_exp_f32 v83, v83\n"
    "v_accvgpr_read_b32 v75, a115\n"
    "v_mfma_f32_16x16x32_bf16 v[192:195], a[144:147], a[176:179], v[192:195]\n"
    "ds_read_b64_tr_b16 a[140:141], v16 offset:520\n"
    "buffer_load_dwordx4 v32, s[52:55], s88 offen offset:3072 lds\n"
    "v_mfma_f32_16x16x32_bf16 v[200:203], a[144:147], a[184:187], v[200:203]\n"
    "ds_read_b64_tr_b16 a[142:143], v16 offset:16904\n"
    "s_add_u32 s88, s88, 0x20000\n"
    "v_accvgpr_read_b32 v76, a116\n"
    "v_mfma_f32_16x16x32_bf16 v[208:211], a[144:147], a[180:183], v[208:211]\n"
    "ds_read_b128 a[172:175], v62 offset:54272\n"
    "v_exp_f32 v84, v84\n"
    "v_mfma_f32_16x16x32_bf16 v[216:219], a[144:147], a[188:191], v[216:219]\n"
    "s_add_u32 m0, s89, 0x3000\n"
    "v_exp_f32 v85, v85\n"
    "v_accvgpr_read_b32 v77, a117\n"
    "v_mfma_f32_16x16x32_bf16 v[196:199], a[148:151], a[176:179], v[196:199]\n"
    "ds_read_b64_tr_b16 a[144:145], v16 offset:1024\n"
    "buffer_load_dwordx4 v29, s[52:55], s88 offen lds\n"
    "v_accvgpr_read_b32 v78, a118\n"
    "v_mfma_f32_16x16x32_bf16 v[204:207], a[148:151], a[184:187], v[204:207]\n"
    "ds_read_b64_tr_b16 a[146:147], v16 offset:17408\n"
    "v_accvgpr_read_b32 v79, a119\n"
    "v_mfma_f32_16x16x32_bf16 v[212:215], a[148:151], a[180:183], v[212:215]\n"
    "v_exp_f32 v86, v86\n"
    "v_fma_f32 v80, v80, v50, v50\n"
    "v_mfma_f32_16x16x32_bf16 v[220:223], a[148:151], a[188:191], v[220:223]\n"
    "buffer_load_dwordx4 v30, s[52:55], s88 offen offset:1024 lds\n"
    "v_exp_f32 v87, v87\n"
    "v_mfma_f32_16x16x32_bf16 v[224:227], a[152:155], a[176:179], v[224:227]\n"
    "ds_read_b64_tr_b16 a[148:149], v16 offset:1032\n"
    "v_fma_f32 v81, v81, v50, v50\n"
    "v_fma_f32 v82, v82, v50, v50\n"
    "v_mfma_f32_16x16x32_bf16 v[232:235], a[152:155], a[184:187], v[232:235]\n"
    "s_waitcnt lgkmcnt(2)\n"
    "ds_read_b64_tr_b16 a[150:151], v16 offset:17416\n"
    "v_fma_f32 v83, v83, v50, v50\n"
    "v_fma_f32 v84, v84, v50, v50\n"
    "v_mfma_f32_16x16x32_bf16 v[240:243], a[152:155], a[180:183], v[240:243]\n"
    "buffer_load_dwordx4 v31, s[52:55], s88 offen offset:2048 lds\n"
    "v_fma_f32 v85, v85, v50, v50\n"
    "v_mfma_f32_16x16x32_bf16 v[248:251], a[152:155], a[188:191], v[248:251]\n"
    "v_fma_f32 v86, v86, v50, v50\n"
    "v_fma_f32 v87, v87, v50, v50\n"
    "v_mul_f32 v64, v64, v72\n"
    "v_mfma_f32_16x16x32_bf16 v[228:231], a[156:159], a[176:179], v[228:231]\n"
    "ds_read_b64_tr_b16 a[152:153], v16 offset:1536\n"
    "v_rcp_f32 v80, v80\n"
    "v_mfma_f32_16x16x32_bf16 v[236:239], a[156:159], a[184:187], v[236:239]\n"
    "ds_read_b64_tr_b16 a[154:155], v16 offset:17920\n"
    "buffer_load_dwordx4 v32, s[52:55], s88 offen offset:3072 lds\n"
    "v_mul_f32 v65, v65, v73\n"
    "v_mfma_f32_16x16x32_bf16 v[244:247], a[156:159], a[180:183], v[244:247]\n"
    "s_add_u32 s88, s88, 0x20000\n"
    "v_rcp_f32 v81, v81\n"
    "v_mul_f32 v66, v66, v74\n"
    "v_mfma_f32_16x16x32_bf16 v[252:255], a[156:159], a[188:191], v[252:255]\n"
    "v_rcp_f32 v82, v82\n"
    "v_mfma_f32_16x16x32_bf16 v[128:131], a[128:131], a[160:163], v[128:131]\n"
    "ds_read_b64_tr_b16 a[156:157], v16 offset:1544\n"
    "v_rcp_f32 v83, v83\n"
    "v_mfma_f32_16x16x32_bf16 v[136:139], a[128:131], a[168:171], v[136:139]\n"
    "ds_read_b64_tr_b16 a[158:159], v16 offset:17928\n"
    "v_rcp_f32 v84, v84\n"
    "v_mfma_f32_16x16x32_bf16 v[144:147], a[128:131], a[164:167], v[144:147]\n"
    "v_rcp_f32 v85, v85\n"
    "v_mul_f32 v67, v67, v75\n"
    "v_mfma_f32_16x16x32_bf16 v[152:155], a[128:131], a[172:175], v[152:155]\n"
    "v_rcp_f32 v86, v86\n"
    "v_mul_f32 v68, v68, v76\n"
    "v_mfma_f32_16x16x32_bf16 v[132:135], a[132:135], a[160:163], v[132:135]\n"
    "s_waitcnt vmcnt(12)\n"
    "ds_read_b64_tr_b16 a[128:129], v15\n"
    "v_mul_f32 v69, v69, v77\n"
    "v_mfma_f32_16x16x32_bf16 v[140:143], a[132:135], a[168:171], v[140:143]\n"
    "s_waitcnt vmcnt(8)\n"
    "ds_read_b64_tr_b16 a[130:131], v15 offset:4096\n"
    "v_rcp_f32 v87, v87\n"
    "v_mfma_f32_16x16x32_bf16 v[148:151], a[132:135], a[164:167], v[148:151]\n"
    "ds_read_b128 a[176:179], v63 offset:22528\n"
    "s_add_u32 m0, s91, 0x10000\n"
    "v_mul_f32 v70, v70, v78\n"
    "v_mfma_f32_16x16x32_bf16 v[156:159], a[132:135], a[172:175], v[156:159]\n"
    "s_waitcnt lgkmcnt(3)\n"
    "buffer_load_dwordx4 v29, s[52:55], s88 offen lds\n"
    "v_mul_f32 v71, v71, v79\n"
    "v_mul_f32 v64, v64, v80\n"
    "v_mfma_f32_16x16x32_bf16 v[160:163], a[136:139], a[160:163], v[160:163]\n"
    "ds_read_b64_tr_b16 a[132:133], v15 offset:8\n"
    "v_mul_f32 v65, v65, v81\n"
    "v_mul_f32 v66, v66, v82\n"
    "v_mfma_f32_16x16x32_bf16 v[168:171], a[136:139], a[168:171], v[168:171]\n"
    "ds_read_b64_tr_b16 a[134:135], v15 offset:4104\n"
    "v_mul_f32 v67, v67, v83\n"
    "v_mfma_f32_16x16x32_bf16 v[176:179], a[136:139], a[164:167], v[176:179]\n"
    "ds_read_b128 a[184:187], v63 offset:23552\n"
    "buffer_load_dwordx4 v30, s[52:55], s88 offen offset:1024 lds\n"
    "v_mul_f32 v68, v68, v84\n"
    "v_mfma_f32_16x16x32_bf16 v[184:187], a[136:139], a[172:175], v[184:187]\n"
    "v_mul_f32 v69, v69, v85\n"
    "v_mul_f32 v70, v70, v86\n"
    "v_mul_f32 v71, v71, v87\n"
    "v_mfma_f32_16x16x32_bf16 v[164:167], a[140:143], a[160:163], v[164:167]\n"
    "ds_read_b64_tr_b16 a[136:137], v15 offset:512\n"
    "v_cvt_pk_bf16_f32 v108, v64, v65\n"
    "v_cvt_pk_bf16_f32 v109, v66, v67\n"
    "v_mfma_f32_16x16x32_bf16 v[172:175], a[140:143], a[168:171], v[172:175]\n"
    "ds_read_b64_tr_b16 a[138:139], v15 offset:4608\n"
    "buffer_load_dwordx4 v31, s[52:55], s88 offen offset:2048 lds\n"
    "v_cvt_pk_bf16_f32 v110, v68, v69\n"
    "v_mfma_f32_16x16x32_bf16 v[180:183], a[140:143], a[164:167], v[180:183]\n"
    "ds_read_b128 a[180:183], v63 offset:55296\n"
    "v_cvt_pk_bf16_f32 v111, v70, v71\n"
    "v_mfma_f32_16x16x32_bf16 v[188:191], a[140:143], a[172:175], v[188:191]\n"
    "v_accvgpr_read_b32 v64, a40\n"
    "v_accvgpr_read_b32 v65, a41\n"
    "v_accvgpr_read_b32 v66, a42\n"
    "v_mfma_f32_16x16x32_bf16 v[192:195], a[144:147], a[160:163], v[192:195]\n"
    "ds_read_b64_tr_b16 a[140:141], v15 offset:520\n"
    "buffer_load_dwordx4 v32, s[52:55], s88 offen offset:3072 lds\n"
    "v_accvgpr_read_b32 v67, a43\n"
    "v_mfma_f32_16x16x32_bf16 v[200:203], a[144:147], a[168:171], v[200:203]\n"
    "ds_read_b64_tr_b16 a[142:143], v15 offset:4616\n"
    "s_add_u32 s88, s88, 0x20000\n"
    "v_accvgpr_read_b32 v68, a44\n"
    "v_mfma_f32_16x16x32_bf16 v[208:211], a[144:147], a[164:167], v[208:211]\n"
    "ds_read_b128 a[188:191], v63 offset:56320\n"
    "v_accvgpr_read_b32 v69, a45\n"
    "v_accvgpr_read_b32 v70, a46\n"
    "v_mfma_f32_16x16x32_bf16 v[216:219], a[144:147], a[172:175], v[216:219]\n"
    "s_add_u32 m0, s91, 0x14000\n"
    "v_accvgpr_read_b32 v71, a47\n"
    "v_mul_f32 v80, v64, v64\n"
    "v_mfma_f32_16x16x32_bf16 v[196:199], a[148:151], a[160:163], v[196:199]\n"
    "ds_read_b64_tr_b16 a[144:145], v15 offset:1024\n"
    "buffer_load_dwordx4 v29, s[52:55], s88 offen lds\n"
    "v_mul_f32 v81, v65, v65\n"
    "v_mfma_f32_16x16x32_bf16 v[204:207], a[148:151], a[168:171], v[204:207]\n"
    "ds_read_b64_tr_b16 a[146:147], v15 offset:5120\n"
    "v_mul_f32 v82, v66, v66\n"
    "v_mul_f32 v83, v67, v67\n"
    "v_mfma_f32_16x16x32_bf16 v[212:215], a[148:151], a[164:167], v[212:215]\n"
    "v_mul_f32 v84, v68, v68\n"
    "v_mul_f32 v85, v69, v69\n"
    "v_mul_f32 v86, v70, v70\n"
    "v_mfma_f32_16x16x32_bf16 v[220:223], a[148:151], a[172:175], v[220:223]\n"
    "buffer_load_dwordx4 v30, s[52:55], s88 offen offset:1024 lds\n"
    "v_mul_f32 v87, v71, v71\n"
    "v_fma_f32 v80, v80, v45, v41\n"
    "v_mfma_f32_16x16x32_bf16 v[224:227], a[152:155], a[160:163], v[224:227]\n"
    "ds_read_b64_tr_b16 a[148:149], v15 offset:1032\n"
    "v_fma_f32 v81, v81, v45, v41\n"
    "v_mfma_f32_16x16x32_bf16 v[232:235], a[152:155], a[168:171], v[232:235]\n"
    "s_waitcnt lgkmcnt(1)\n"
    "ds_read_b64_tr_b16 a[150:151], v15 offset:5128\n"
    "v_fma_f32 v82, v82, v45, v41\n"
    "v_fma_f32 v83, v83, v45, v41\n"
    "v_mfma_f32_16x16x32_bf16 v[240:243], a[152:155], a[164:167], v[240:243]\n"
    "buffer_load_dwordx4 v31, s[52:55], s88 offen offset:2048 lds\n"
    "v_fma_f32 v84, v84, v45, v41\n"
    "v_fma_f32 v85, v85, v45, v41\n"
    "v_mfma_f32_16x16x32_bf16 v[248:251], a[152:155], a[172:175], v[248:251]\n"
    "v_fma_f32 v86, v86, v45, v41\n"
    "v_fma_f32 v87, v87, v45, v41\n"
    "v_mul_f32 v80, v80, v64\n"
    "v_mfma_f32_16x16x32_bf16 v[228:231], a[156:159], a[160:163], v[228:231]\n"
    "ds_read_b64_tr_b16 a[152:153], v15 offset:1536\n"
    "v_mul_f32 v81, v81, v65\n"
    "v_mul_f32 v82, v82, v66\n"
    "v_mfma_f32_16x16x32_bf16 v[236:239], a[156:159], a[168:171], v[236:239]\n"
    "ds_read_b64_tr_b16 a[154:155], v15 offset:5632\n"
    "buffer_load_dwordx4 v32, s[52:55], s88 offen offset:3072 lds\n"
    "v_mfma_f32_16x16x32_bf16 v[244:247], a[156:159], a[164:167], v[244:247]\n"
    "s_add_u32 s88, s88, 0x20000\n"
    "v_mul_f32 v83, v83, v67\n"
    "v_mul_f32 v84, v84, v68\n"
    "v_mul_f32 v85, v85, v69\n"
    "v_mfma_f32_16x16x32_bf16 v[252:255], a[156:159], a[172:175], v[252:255]\n"
    "v_mul_f32 v86, v86, v70\n"
    "v_mul_f32 v87, v87, v71\n"
    "v_accvgpr_read_b32 v72, a104\n"
    "v_mfma_f32_16x16x32_bf16 v[128:131], a[128:131], a[176:179], v[128:131]\n"
    "ds_read_b64_tr_b16 a[156:157], v15 offset:1544\n"
    "v_exp_f32 v80, v80\n"
    "v_mfma_f32_16x16x32_bf16 v[136:139], a[128:131], a[184:187], v[136:139]\n"
    "ds_read_b64_tr_b16 a[158:159], v15 offset:5640\n"
    "v_exp_f32 v81, v81\n"
    "v_mfma_f32_16x16x32_bf16 v[144:147], a[128:131], a[180:183], v[144:147]\n"
    "v_exp_f32 v82, v82\n"
    "v_mfma_f32_16x16x32_bf16 v[152:155], a[128:131], a[188:191], v[152:155]\n"
    "v_exp_f32 v83, v83\n"
    "v_accvgpr_read_b32 v73, a105\n"
    "v_mfma_f32_16x16x32_bf16 v[132:135], a[132:135], a[176:179], v[132:135]\n"
    "s_waitcnt vmcnt(12)\n"
    "ds_read_b64_tr_b16 a[128:129], v15 offset:8192\n"
    "v_exp_f32 v84, v84\n"
    "v_mfma_f32_16x16x32_bf16 v[140:143], a[132:135], a[184:187], v[140:143]\n"
    "s_waitcnt vmcnt(8)\n"
    "ds_read_b64_tr_b16 a[130:131], v15 offset:12288\n"
    "v_exp_f32 v85, v85\n"
    "v_mfma_f32_16x16x32_bf16 v[148:151], a[132:135], a[180:183], v[148:151]\n"
    "ds_read_b128 a[160:163], v62 offset:24576\n"
    "s_add_u32 m0, s89, 0\n"
    "v_accvgpr_read_b32 v74, a106\n"
    "v_mfma_f32_16x16x32_bf16 v[156:159], a[132:135], a[188:191], v[156:159]\n"
    "s_waitcnt lgkmcnt(3)\n"
    "buffer_load_dwordx4 v29, s[52:55], s88 offen lds\n"
    "v_accvgpr_read_b32 v75, a107\n"
    "v_mfma_f32_16x16x32_bf16 v[160:163], a[136:139], a[176:179], v[160:163]\n"
    "ds_read_b64_tr_b16 a[132:133], v15 offset:8200\n"
    "v_exp_f32 v86, v86\n"
    "v_mfma_f32_16x16x32_bf16 v[168:171], a[136:139], a[184:187], v[168:171]\n"
    "ds_read_b64_tr_b16 a[134:135], v15 offset:12296\n"
    "v_exp_f32 v87, v87\n"
    "v_mfma_f32_16x16x32_bf16 v[176:179], a[136:139], a[180:183], v[176:179]\n"
    "ds_read_b128 a[168:171], v62 offset:25600\n"
    "buffer_load_dwordx4 v30, s[52:55], s88 offen offset:1024 lds\n"
    "v_accvgpr_read_b32 v76, a108\n"
    "v_mfma_f32_16x16x32_bf16 v[184:187], a[136:139], a[188:191], v[184:187]\n"
    "v_accvgpr_read_b32 v77, a109\n"
    "v_accvgpr_read_b32 v78, a110\n"
    "v_accvgpr_read_b32 v79, a111\n"
    "v_mfma_f32_16x16x32_bf16 v[164:167], a[140:143], a[176:179], v[164:167]\n"
    "ds_read_b64_tr_b16 a[136:137], v15 offset:8704\n"
    "v_fma_f32 v80, v80, v49, v49\n"
    "v_mfma_f32_16x16x32_bf16 v[172:175], a[140:143], a[184:187], v[172:175]\n"
    "ds_read_b64_tr_b16 a[138:139], v15 offset:12800\n"
    "buffer_load_dwordx4 v31, s[52:55], s88 offen offset:2048 lds\n"
    "v_fma_f32 v81, v81, v49, v49\n"
    "v_mfma_f32_16x16x32_bf16 v[180:183], a[140:143], a[180:183], v[180:183]\n"
    "ds_read_b128 a[164:167], v62 offset:57344\n"
    "v_fma_f32 v82, v82, v49, v49\n"
    "v_fma_f32 v83, v83, v49, v49\n"
    "v_mfma_f32_16x16x32_bf16 v[188:191], a[140:143], a[188:191], v[188:191]\n"
    "v_fma_f32 v84, v84, v49, v49\n"
    "v_fma_f32 v85, v85, v49, v49\n"
    "v_fma_f32 v86, v86, v49, v49\n"
    "v_mfma_f32_16x16x32_bf16 v[192:195], a[144:147], a[176:179], v[192:195]\n"
    "ds_read_b64_tr_b16 a[140:141], v15 offset:8712\n"
    "buffer_load_dwordx4 v32, s[52:55], s88 offen offset:3072 lds\n"
    "v_fma_f32 v87, v87, v49, v49\n"
    "v_mfma_f32_16x16x32_bf16 v[200:203], a[144:147], a[184:187], v[200:203]\n"
    "ds_read_b64_tr_b16 a[142:143], v15 offset:12808\n"
    "s_add_u32 s88, s88, 0x20000\n"
    "v_mfma_f32_16x16x32_bf16 v[208:211], a[144:147], a[180:183], v[208:211]\n"
    "ds_read_b128 a[172:175], v62 offset:58368\n"
    "v_rcp_f32 v80, v80\n"
    "v_mfma_f32_16x16x32_bf16 v[216:219], a[144:147], a[188:191], v[216:219]\n"
    "s_add_u32 m0, s89, 0x1000\n"
    "v_rcp_f32 v81, v81\n"
    "v_mul_f32 v64, v64, v72\n"
    "v_mfma_f32_16x16x32_bf16 v[196:199], a[148:151], a[176:179], v[196:199]\n"
    "ds_read_b64_tr_b16 a[144:145], v15 offset:9216\n"
    "buffer_load_dwordx4 v29, s[52:55], s88 offen lds\n"
    "v_mul_f32 v65, v65, v73\n"
    "v_mfma_f32_16x16x32_bf16 v[204:207], a[148:151], a[184:187], v[204:207]\n"
    "ds_read_b64_tr_b16 a[146:147], v15 offset:13312\n"
    "v_rcp_f32 v82, v82\n"
    "v_mfma_f32_16x16x32_bf16 v[212:215], a[148:151], a[180:183], v[212:215]\n"
    "v_rcp_f32 v83, v83\n"
    "v_mfma_f32_16x16x32_bf16 v[220:223], a[148:151], a[188:191], v[220:223]\n"
    "buffer_load_dwordx4 v30, s[52:55], s88 offen offset:1024 lds\n"
    "v_rcp_f32 v84, v84\n"
    "v_mfma_f32_16x16x32_bf16 v[224:227], a[152:155], a[176:179], v[224:227]\n"
    "ds_read_b64_tr_b16 a[148:149], v15 offset:9224\n"
    "v_rcp_f32 v85, v85\n"
    "v_mfma_f32_16x16x32_bf16 v[232:235], a[152:155], a[184:187], v[232:235]\n"
    "s_waitcnt lgkmcnt(2)\n"
    "ds_read_b64_tr_b16 a[150:151], v15 offset:13320\n"
    "v_rcp_f32 v86, v86\n"
    "v_mfma_f32_16x16x32_bf16 v[240:243], a[152:155], a[180:183], v[240:243]\n"
    "buffer_load_dwordx4 v31, s[52:55], s88 offen offset:2048 lds\n"
    "v_rcp_f32 v87, v87\n"
    "v_mfma_f32_16x16x32_bf16 v[248:251], a[152:155], a[188:191], v[248:251]\n"
    "v_mul_f32 v66, v66, v74\n"
    "v_mul_f32 v67, v67, v75\n"
    "v_mfma_f32_16x16x32_bf16 v[228:231], a[156:159], a[176:179], v[228:231]\n"
    "ds_read_b64_tr_b16 a[152:153], v15 offset:9728\n"
    "v_mul_f32 v68, v68, v76\n"
    "v_mul_f32 v69, v69, v77\n"
    "v_mfma_f32_16x16x32_bf16 v[236:239], a[156:159], a[184:187], v[236:239]\n"
    "ds_read_b64_tr_b16 a[154:155], v15 offset:13824\n"
    "buffer_load_dwordx4 v32, s[52:55], s88 offen offset:3072 lds\n"
    "v_mul_f32 v70, v70, v78\n"
    "v_mfma_f32_16x16x32_bf16 v[244:247], a[156:159], a[180:183], v[244:247]\n"
    "s_add_u32 s88, s88, 0x20000\n"
    "v_mul_f32 v71, v71, v79\n"
    "v_mul_f32 v64, v64, v80\n"
    "v_mul_f32 v65, v65, v81\n"
    "v_mfma_f32_16x16x32_bf16 v[252:255], a[156:159], a[188:191], v[252:255]\n"
    "v_mul_f32 v66, v66, v82\n"
    "v_mul_f32 v67, v67, v83\n"
    "v_mul_f32 v68, v68, v84\n"
    "v_mfma_f32_16x16x32_bf16 v[128:131], a[128:131], a[160:163], v[128:131]\n"
    "ds_read_b64_tr_b16 a[156:157], v15 offset:9736\n"
    "v_mul_f32 v69, v69, v85\n"
    "v_mfma_f32_16x16x32_bf16 v[136:139], a[128:131], a[168:171], v[136:139]\n"
    "ds_read_b64_tr_b16 a[158:159], v15 offset:13832\n"
    "v_mul_f32 v70, v70, v86\n"
    "v_mul_f32 v71, v71, v87\n"
    "v_mfma_f32_16x16x32_bf16 v[144:147], a[128:131], a[164:167], v[144:147]\n"
    "v_cvt_pk_bf16_f32 v112, v64, v65\n"
    "v_cvt_pk_bf16_f32 v113, v66, v67\n"
    "v_cvt_pk_bf16_f32 v114, v68, v69\n"
    "v_mfma_f32_16x16x32_bf16 v[152:155], a[128:131], a[172:175], v[152:155]\n"
    "v_cvt_pk_bf16_f32 v115, v70, v71\n"
    "v_accvgpr_read_b32 v64, a56\n"
    "v_accvgpr_read_b32 v65, a57\n"
    "v_mfma_f32_16x16x32_bf16 v[132:135], a[132:135], a[160:163], v[132:135]\n"
    "s_waitcnt vmcnt(12)\n"
    "ds_read_b64_tr_b16 a[128:129], v16\n"
    "v_accvgpr_read_b32 v66, a58\n"
    "v_accvgpr_read_b32 v67, a59\n"
    "v_mfma_f32_16x16x32_bf16 v[140:143], a[132:135], a[168:171], v[140:143]\n"
    "s_waitcnt vmcnt(8)\n"
    "ds_read_b64_tr_b16 a[130:131], v16 offset:16384\n"
    "v_accvgpr_read_b32 v68, a60\n"
    "v_mfma_f32_16x16x32_bf16 v[148:151], a[132:135], a[164:167], v[148:151]\n"
    "ds_read_b128 a[176:179], v63 offset:26624\n"
    "s_add_u32 m0, s89, 0x2000\n"
    "v_accvgpr_read_b32 v69, a61\n"
    "v_mfma_f32_16x16x32_bf16 v[156:159], a[132:135], a[172:175], v[156:159]\n"
    "s_waitcnt lgkmcnt(3)\n"
    "buffer_load_dwordx4 v29, s[52:55], s88 offen lds\n"
    "v_accvgpr_read_b32 v70, a62\n"
    "v_accvgpr_read_b32 v71, a63\n"
    "v_mfma_f32_16x16x32_bf16 v[160:163], a[136:139], a[160:163], v[160:163]\n"
    "ds_read_b64_tr_b16 a[132:133], v16 offset:8\n"
    "v_mul_f32 v80, v64, v64\n"
    "v_mul_f32 v81, v65, v65\n"
    "v_mfma_f32_16x16x32_bf16 v[168:171], a[136:139], a[168:171], v[168:171]\n"
    "ds_read_b64_tr_b16 a[134:135], v16 offset:16392\n"
    "v_mul_f32 v82, v66, v66\n"
    "v_mul_f32 v83, v67, v67\n"
    "v_mfma_f32_16x16x32_bf16 v[176:179], a[136:139], a[164:167], v[176:179]\n"
    "ds_read_b128 a[184:187], v63 offset:27648\n"
    "buffer_load_dwordx4 v30, s[52:55], s88 offen offset:1024 lds\n"
    "v_mfma_f32_16x16x32_bf16 v[184:187], a[136:139], a[172:175], v[184:187]\n"
    "v_mul_f32 v84, v68, v68\n"
    "v_mul_f32 v85, v69, v69\n"
    "v_mul_f32 v86, v70, v70\n"
    "v_mfma_f32_16x16x32_bf16 v[164:167], a[140:143], a[160:163], v[164:167]\n"
    "ds_read_b64_tr_b16 a[136:137], v16 offset:512\n"
    "v_mul_f32 v87, v71, v71\n"
    "v_fma_f32 v80, v80, v47, v43\n"
    "v_mfma_f32_16x16x32_bf16 v[172:175], a[140:143], a[168:171], v[172:175]\n"
    "ds_read_b64_tr_b16 a[138:139], v16 offset:16896\n"
    "buffer_load_dwordx4 v31, s[52:55], s88 offen offset:2048 lds\n"
    "v_fma_f32 v81, v81, v47, v43\n"
    "v_mfma_f32_16x16x32_bf16 v[180:183], a[140:143], a[164:167], v[180:183]\n"
    "ds_read_b128 a[180:183], v63 offset:59392\n"
    "v_fma_f32 v82, v82, v47, v43\n"
    "v_fma_f32 v83, v83, v47, v43\n"
    "v_mfma_f32_16x16x32_bf16 v[188:191], a[140:143], a[172:175], v[188:191]\n"
    "v_fma_f32 v84, v84, v47, v43\n"
    "v_fma_f32 v85, v85, v47, v43\n"
    "v_mfma_f32_16x16x32_bf16 v[192:195], a[144:147], a[160:163], v[192:195]\n"
    "ds_read_b64_tr_b16 a[140:141], v16 offset:520\n"
    "buffer_load_dwordx4 v32, s[52:55], s88 offen offset:3072 lds\n"
    "v_fma_f32 v86, v86, v47, v43\n"
    "v_mfma_f32_16x16x32_bf16 v[200:203], a[144:147], a[168:171], v[200:203]\n"
    "ds_read_b64_tr_b16 a[142:143], v16 offset:16904\n"
    "s_add_u32 s88, s88, 0x20000\n"
    "v_fma_f32 v87, v87, v47, v43\n"
    "v_mfma_f32_16x16x32_bf16 v[208:211], a[144:147], a[164:167], v[208:211]\n"
    "ds_read_b128 a[188:191], v63 offset:60416\n"
    "v_mul_f32 v80, v80, v64\n"
    "v_mul_f32 v81, v81, v65\n"
    "v_mfma_f32_16x16x32_bf16 v[216:219], a[144:147], a[172:175], v[216:219]\n"
    "s_add_u32 m0, s89, 0x3000\n"
    "v_mul_f32 v82, v82, v66\n"
    "v_mul_f32 v83, v83, v67\n"
    "v_mul_f32 v84, v84, v68\n"
    "v_mfma_f32_16x16x32_bf16 v[196:199], a[148:151], a[160:163], v[196:199]\n"
    "ds_read_b64_tr_b16 a[144:145], v16 offset:1024\n"
    "buffer_load_dwordx4 v29, s[52:55], s88 offen lds\n"
    "v_mfma_f32_16x16x32_bf16 v[204:207], a[148:151], a[168:171], v[204:207]\n"
    "ds_read_b64_tr_b16 a[146:147], v16 offset:17408\n"
    "v_mul_f32 v85, v85, v69\n"
    "v_mul_f32 v86, v86, v70\n"
    "v_mfma_f32_16x16x32_bf16 v[212:215], a[148:151], a[164:167], v[212:215]\n"
    "v_mul_f32 v87, v87, v71\n"
    "v_exp_f32 v80, v80\n"
    "v_mfma_f32_16x16x32_bf16 v[220:223], a[148:151], a[172:175], v[220:223]\n"
    "buffer_load_dwordx4 v30, s[52:55], s88 offen offset:1024 lds\n"
    "v_exp_f32 v81, v81\n"
    "v_mfma_f32_16x16x32_bf16 v[224:227], a[152:155], a[160:163], v[224:227]\n"
    "ds_read_b64_tr_b16 a[148:149], v16 offset:1032\n"
    "v_exp_f32 v82, v82\n"
    "v_mfma_f32_16x16x32_bf16 v[232:235], a[152:155], a[168:171], v[232:235]\n"
    "s_waitcnt lgkmcnt(2)\n"
    "ds_read_b64_tr_b16 a[150:151], v16 offset:17416\n"
    "v_accvgpr_read_b32 v72, a120\n"
    "v_mfma_f32_16x16x32_bf16 v[240:243], a[152:155], a[164:167], v[240:243]\n"
    "buffer_load_dwordx4 v31, s[52:55], s88 offen offset:2048 lds\n"
    "v_exp_f32 v83, v83\n"
    "v_mfma_f32_16x16x32_bf16 v[248:251], a[152:155], a[172:175], v[248:251]\n"
    "v_exp_f32 v84, v84\n"
    "v_accvgpr_read_b32 v73, a121\n"
    "v_mfma_f32_16x16x32_bf16 v[228:231], a[156:159], a[160:163], v[228:231]\n"
    "ds_read_b64_tr_b16 a[152:153], v16 offset:1536\n"
    "v_exp_f32 v85, v85\n"
    "v_mfma_f32_16x16x32_bf16 v[236:239], a[156:159], a[168:171], v[236:239]\n"
    "ds_read_b64_tr_b16 a[154:155], v16 offset:17920\n"
    "buffer_load_dwordx4 v32, s[52:55], s88 offen offset:3072 lds\n"
    "v_accvgpr_read_b32 v74, a122\n"
    "v_mfma_f32_16x16x32_bf16 v[244:247], a[156:159], a[164:167], v[244:247]\n"
    "s_add_u32 s88, s88, 0x20000\n"
    "v_exp_f32 v86, v86\n"
    "v_mfma_f32_16x16x32_bf16 v[252:255], a[156:159], a[172:175], v[252:255]\n"
    "v_exp_f32 v87, v87\n"
    "v_accvgpr_read_b32 v75, a123\n"
    "v_mfma_f32_16x16x32_bf16 v[128:131], a[128:131], a[176:179], v[128:131]\n"
    "ds_read_b64_tr_b16 a[156:157], v16 offset:1544\n"
    "v_accvgpr_read_b32 v76, a124\n"
    "v_accvgpr_read_b32 v77, a125\n"
    "v_mfma_f32_16x16x32_bf16 v[136:139], a[128:131], a[184:187], v[136:139]\n"
    "ds_read_b64_tr_b16 a[158:159], v16 offset:17928\n"
    "v_accvgpr_read_b32 v78, a126\n"
    "v_accvgpr_read_b32 v79, a127\n"
    "v_mfma_f32_16x16x32_bf16 v[144:147], a[128:131], a[180:183], v[144:147]\n"
    "v_fma_f32 v80, v80, v51, v51\n"
    "v_fma_f32 v81, v81, v51, v51\n"
    "v_fma_f32 v82, v82, v51, v51\n"
    "v_mfma_f32_16x16x32_bf16 v[152:155], a[128:131], a[188:191], v[152:155]\n"
    "v_fma_f32 v83, v83, v51, v51\n"
    "v_fma_f32 v84, v84, v51, v51\n"
    "v_mfma_f32_16x16x32_bf16 v[132:135], a[132:135], a[176:179], v[132:135]\n"
    "v_fma_f32 v85, v85, v51, v51\n"
    "v_fma_f32 v86, v86, v51, v51\n"
    "v_fma_f32 v87, v87, v51, v51\n"
    "v_mfma_f32_16x16x32_bf16 v[140:143], a[132:135], a[184:187], v[140:143]\n"
    "v_rcp_f32 v80, v80\n"
    "v_mul_f32 v64, v64, v72\n"
    "v_mfma_f32_16x16x32_bf16 v[148:151], a[132:135], a[180:183], v[148:151]\n"
    "ds_read_b128 a[160:163], v62 offset:28672\n"
    "v_rcp_f32 v81, v81\n"
    "v_mfma_f32_16x16x32_bf16 v[156:159], a[132:135], a[188:191], v[156:159]\n"
    "v_rcp_f32 v82, v82\n"
    "v_mul_f32 v65, v65, v73\n"
    "v_mfma_f32_16x16x32_bf16 v[160:163], a[136:139], a[176:179], v[160:163]\n"
    "v_rcp_f32 v83, v83\n"
    "v_mfma_f32_16x16x32_bf16 v[168:171], a[136:139], a[184:187], v[168:171]\n"
    "v_rcp_f32 v84, v84\n"
    "v_mul_f32 v66, v66, v74\n"
    "v_mfma_f32_16x16x32_bf16 v[176:179], a[136:139], a[180:183], v[176:179]\n"
    "ds_read_b128 a[168:171], v62 offset:29696\n"
    "v_rcp_f32 v85, v85\n"
    "v_mfma_f32_16x16x32_bf16 v[184:187], a[136:139], a[188:191], v[184:187]\n"
    "v_rcp_f32 v86, v86\n"
    "v_mul_f32 v67, v67, v75\n"
    "v_mfma_f32_16x16x32_bf16 v[164:167], a[140:143], a[176:179], v[164:167]\n"
    "v_rcp_f32 v87, v87\n"
    "v_mul_f32 v68, v68, v76\n"
    "v_mfma_f32_16x16x32_bf16 v[172:175], a[140:143], a[184:187], v[172:175]\n"
    "v_mul_f32 v69, v69, v77\n"
    "v_mul_f32 v70, v70, v78\n"
    "v_mfma_f32_16x16x32_bf16 v[180:183], a[140:143], a[180:183], v[180:183]\n"
    "ds_read_b128 a[164:167], v62 offset:61440\n"
    "v_mul_f32 v71, v71, v79\n"
    "v_mul_f32 v64, v64, v80\n"
    "v_mfma_f32_16x16x32_bf16 v[188:191], a[140:143], a[188:191], v[188:191]\n"
    "s_waitcnt lgkmcnt(1)\n"
    "v_mul_f32 v65, v65, v81\n"
    "v_mul_f32 v66, v66, v82\n"
    "v_mul_f32 v67, v67, v83\n"
    "v_mfma_f32_16x16x32_bf16 v[192:195], a[144:147], a[176:179], v[192:195]\n"
    "s_add_u32 m0, s91, 0x10000\n"
    "s_add_u32 s90, s91, 0\n"
    "v_mul_f32 v68, v68, v84\n"
    "v_mul_f32 v69, v69, v85\n"
    "v_mfma_f32_16x16x32_bf16 v[200:203], a[144:147], a[184:187], v[200:203]\n"
    "s_waitcnt vmcnt(12)\n"
    "ds_read_b64_tr_b16 a[128:129], v15\n"
    "v_bfe_u32 v57, v1, 2, 3\n"
    "v_mul_f32 v70, v70, v86\n"
    "v_mfma_f32_16x16x32_bf16 v[208:211], a[144:147], a[180:183], v[208:211]\n"
    "ds_read_b128 a[172:175], v62 offset:62464\n"
    "s_waitcnt vmcnt(8)\n"
    "ds_read_b64_tr_b16 a[130:131], v15 offset:4096\n"
    "buffer_load_dwordx4 v19, s[56:59], s90 offen lds\n"
    "v_mfma_f32_16x16x32_bf16 v[216:219], a[144:147], a[188:191], v[216:219]\n"
    "ds_read_b64_tr_b16 a[132:133], v15 offset:8\n"
    "v_mul_u32_u24 v56, 0x400, v57\n"
    "v_mul_f32 v71, v71, v87\n"
    "v_mfma_f32_16x16x32_bf16 v[196:199], a[148:151], a[176:179], v[196:199]\n"
    "ds_read_b64_tr_b16 a[134:135], v15 offset:4104\n"
    "buffer_load_dwordx4 v20, s[56:59], s90 offen offset:1024 lds\n"
    "v_cvt_pk_bf16_f32 v116, v64, v65\n"
    "v_mfma_f32_16x16x32_bf16 v[204:207], a[148:151], a[184:187], v[204:207]\n"
    "s_waitcnt vmcnt(6)\n"
    "ds_read_b64_tr_b16 a[136:137], v15 offset:8192\n"
    "v_lshrrev_b32 v57, 5, v1\n"
    "v_cvt_pk_bf16_f32 v117, v66, v67\n"
    "v_mfma_f32_16x16x32_bf16 v[212:215], a[148:151], a[180:183], v[212:215]\n"
    "s_waitcnt vmcnt(2)\n"
    "ds_read_b64_tr_b16 a[138:139], v15 offset:12288\n"
    "buffer_load_dwordx4 v21, s[56:59], s90 offen offset:2048 lds\n"
    "v_cvt_pk_bf16_f32 v118, v68, v69\n"
    "v_mfma_f32_16x16x32_bf16 v[220:223], a[148:151], a[188:191], v[220:223]\n"
    "ds_read_b64_tr_b16 a[140:141], v15 offset:8200\n"
    "v_lshl_add_u32 v56, v57, 6, v56\n"
    "v_cvt_pk_bf16_f32 v119, v70, v71\n"
    "v_mfma_f32_16x16x32_bf16 v[224:227], a[152:155], a[176:179], v[224:227]\n"
    "ds_read_b64_tr_b16 a[142:143], v15 offset:12296\n"
    "buffer_load_dwordx4 v22, s[56:59], s90 offen offset:3072 lds\n"
    "v_mfma_f32_16x16x32_bf16 v[232:235], a[152:155], a[184:187], v[232:235]\n"
    "v_lshrrev_b32 v57, 3, v1\n"
    "v_and_b32 v57, 2, v57\n"
    "v_mfma_f32_16x16x32_bf16 v[240:243], a[152:155], a[180:183], v[240:243]\n"
    "v_xor_b32 v57, v57, v1\n"
    "v_and_b32 v57, 3, v57\n"
    "v_mfma_f32_16x16x32_bf16 v[248:251], a[152:155], a[188:191], v[248:251]\n"
    "v_lshl_add_u32 v56, v57, 4, v56\n"
    "v_mfma_f32_16x16x32_bf16 v[228:231], a[156:159], a[176:179], v[228:231]\n"
    "v_add_u32 v25, 0x1000, v56\n"
    "v_add_u32 v26, 0xc80, v56\n"
    "v_mfma_f32_16x16x32_bf16 v[236:239], a[156:159], a[184:187], v[236:239]\n"
    "v_add_u32 v27, 0x2800, v56\n"
    "v_add_u32 v28, 0x2480, v56\n"
    "v_mfma_f32_16x16x32_bf16 v[244:247], a[156:159], a[180:183], v[244:247]\n"
    "v_add_u32 v29, 0x1000, v56\n"
    "v_add_u32 v30, 0x4c00, v56\n"
    "v_mfma_f32_16x16x32_bf16 v[252:255], a[156:159], a[188:191], v[252:255]\n"
    "s_waitcnt lgkmcnt(0)\n"
    "v_add_u32 v31, 0x2800, v56\n"
    "v_add_u32 v32, 0x6400, v56\n"
    "v_mfma_f32_16x16x32_bf16 v[128:131], a[128:131], a[160:163], v[128:131]\n"
    "ds_read_b128 a[176:179], v63 offset:30720\n"
    "s_add_u32 m0, s91, 0x14000\n"
    "s_add_u32 s90, s91, 0\n"
    "v_mfma_f32_16x16x32_bf16 v[136:139], a[128:131], a[168:171], v[136:139]\n"
    "ds_read_b128 a[184:187], v63 offset:31744\n"
    "ds_read_b64_tr_b16 a[144:145], v15 offset:512\n"
    "v_mfma_f32_16x16x32_bf16 v[144:147], a[128:131], a[164:167], v[144:147]\n"
    "ds_read_b128 a[180:183], v63 offset:63488\n"
    "ds_read_b64_tr_b16 a[146:147], v15 offset:4608\n"
    "buffer_load_dwordx4 v23, s[60:63], s90 offen lds\n"
    "v_mfma_f32_16x16x32_bf16 v[152:155], a[128:131], a[172:175], v[152:155]\n"
    "ds_read_b128 a[188:191], v63 offset:64512\n"
    "ds_read_b64_tr_b16 a[148:149], v15 offset:520\n"
    "v_mfma_f32_16x16x32_bf16 v[132:135], a[132:135], a[160:163], v[132:135]\n"
    "ds_read_b64_tr_b16 a[150:151], v15 offset:4616\n"
    "buffer_load_dwordx4 v24, s[60:63], s90 offen offset:1024 lds\n"
    "v_mfma_f32_16x16x32_bf16 v[140:143], a[132:135], a[168:171], v[140:143]\n"
    "ds_read_b64_tr_b16 a[152:153], v15 offset:8704\n"
    "v_mfma_f32_16x16x32_bf16 v[148:151], a[132:135], a[164:167], v[148:151]\n"
    "ds_read_b64_tr_b16 a[154:155], v15 offset:12800\n"
    "buffer_load_dwordx4 v23, s[60:63], s90 offen offset:2048 lds\n"
    "v_mfma_f32_16x16x32_bf16 v[156:159], a[132:135], a[172:175], v[156:159]\n"
    "s_waitcnt lgkmcnt(5)\n"
    "ds_read_b64_tr_b16 a[156:157], v15 offset:8712\n"
    "v_mfma_f32_16x16x32_bf16 v[128:131], a[136:139], a[176:179], v[128:131]\n"
    "ds_read_b64_tr_b16 a[158:159], v15 offset:12808\n"
    "buffer_load_dwordx4 v24, s[60:63], s90 offen offset:3072 lds\n"
    "v_mfma_f32_16x16x32_bf16 v[136:139], a[136:139], a[184:187], v[136:139]\n"
    "v_mfma_f32_16x16x32_bf16 v[144:147], a[136:139], a[180:183], v[144:147]\n"
    "s_waitcnt lgkmcnt(4)\n"
    "buffer_store_dwordx4 v[88:91], v2, s[72:75], s95 offen sc1\n"
    "v_mfma_f32_16x16x32_bf16 v[152:155], a[136:139], a[188:191], v[152:155]\n"
    "v_mfma_f32_16x16x32_bf16 v[132:135], a[140:143], a[176:179], v[132:135]\n"
    "buffer_store_dwordx4 v[92:95], v2, s[72:75], s95 offen offset:1024 sc1\n"
    "v_mfma_f32_16x16x32_bf16 v[140:143], a[140:143], a[184:187], v[140:143]\n"
    "v_mfma_f32_16x16x32_bf16 v[148:151], a[140:143], a[180:183], v[148:151]\n"
    "buffer_store_dwordx4 v[96:99], v2, s[72:75], s95 offen offset:2048 sc1\n"
    "v_mfma_f32_16x16x32_bf16 v[156:159], a[140:143], a[188:191], v[156:159]\n"
    "v_mfma_f32_16x16x32_bf16 v[160:163], a[144:147], a[160:163], v[160:163]\n"
    "buffer_store_dwordx4 v[100:103], v2, s[72:75], s95 offen offset:3072 sc1\n"
    "v_mfma_f32_16x16x32_bf16 v[168:171], a[144:147], a[168:171], v[168:171]\n"
    "ds_read_b64_tr_b16 a[128:129], v15 offset:1024\n"
    "v_mfma_f32_16x16x32_bf16 v[176:179], a[144:147], a[164:167], v[176:179]\n"
    "ds_read_b64_tr_b16 a[130:131], v15 offset:5120\n"
    "buffer_store_dwordx4 v[104:107], v2, s[72:75], s93 offen sc1\n"
    "v_mfma_f32_16x16x32_bf16 v[184:187], a[144:147], a[172:175], v[184:187]\n"
    "ds_read_b64_tr_b16 a[132:133], v15 offset:1032\n"
    "v_mul_f32 v128, v128, v52\n"
    "v_mul_f32 v129, v129, v52\n"
    "v_mfma_f32_16x16x32_bf16 v[164:167], a[148:151], a[160:163], v[164:167]\n"
    "ds_read_b64_tr_b16 a[134:135], v15 offset:5128\n"
    "buffer_store_dwordx4 v[108:111], v2, s[72:75], s93 offen offset:1024 sc1\n"
    "v_mfma_f32_16x16x32_bf16 v[172:175], a[148:151], a[168:171], v[172:175]\n"
    "ds_read_b64_tr_b16 a[136:137], v15 offset:9216\n"
    "v_mul_f32 v130, v130, v52\n"
    "v_mul_f32 v131, v131, v52\n"
    "v_mfma_f32_16x16x32_bf16 v[180:183], a[148:151], a[164:167], v[180:183]\n"
    "ds_read_b64_tr_b16 a[138:139], v15 offset:13312\n"
    "buffer_store_dwordx4 v[112:115], v2, s[72:75], s93 offen offset:2048 sc1\n"
    "v_mfma_f32_16x16x32_bf16 v[188:191], a[148:151], a[172:175], v[188:191]\n"
    "s_waitcnt lgkmcnt(3)\n"
    "ds_read_b64_tr_b16 a[140:141], v15 offset:9224\n"
    "v_mul_f32 v132, v132, v52\n"
    "v_mul_f32 v133, v133, v52\n"
    "v_mul_f32 v134, v134, v52\n"
    "v_mfma_f32_16x16x32_bf16 v[160:163], a[152:155], a[176:179], v[160:163]\n"
    "ds_read_b64_tr_b16 a[142:143], v15 offset:13320\n"
    "buffer_store_dwordx4 v[116:119], v2, s[72:75], s93 offen offset:3072 sc1\n"
    "v_mfma_f32_16x16x32_bf16 v[168:171], a[152:155], a[184:187], v[168:171]\n"
    "v_mul_f32 v135, v135, v52\n"
    "v_cvt_pk_bf16_f32 v128, v128, v129\n"
    "v_cvt_pk_bf16_f32 v129, v130, v131\n"
    "v_cvt_pk_bf16_f32 v130, v132, v133\n"
    "v_mfma_f32_16x16x32_bf16 v[176:179], a[152:155], a[180:183], v[176:179]\n"
    "v_cvt_pk_bf16_f32 v131, v134, v135\n"
    "v_mul_f32 v144, v144, v54\n"
    "v_mul_f32 v145, v145, v54\n"
    "v_mul_f32 v146, v146, v54\n"
    "v_mfma_f32_16x16x32_bf16 v[184:187], a[152:155], a[188:191], v[184:187]\n"
    "v_mul_f32 v147, v147, v54\n"
    "v_mul_f32 v148, v148, v54\n"
    "v_mul_f32 v149, v149, v54\n"
    "v_mul_f32 v150, v150, v54\n"
    "v_mul_f32 v151, v151, v54\n"
    "v_mfma_f32_16x16x32_bf16 v[164:167], a[156:159], a[176:179], v[164:167]\n"
    "v_cvt_pk_bf16_f32 v144, v144, v145\n"
    "v_cvt_pk_bf16_f32 v145, v146, v147\n"
    "v_cvt_pk_bf16_f32 v146, v148, v149\n"
    "v_cvt_pk_bf16_f32 v147, v150, v151\n"
    "v_mfma_f32_16x16x32_bf16 v[172:175], a[156:159], a[184:187], v[172:175]\n"
    "v_mul_f32 v136, v136, v53\n"
    "v_mul_f32 v137, v137, v53\n"
    "v_mul_f32 v138, v138, v53\n"
    "v_mul_f32 v139, v139, v53\n"
    "v_mfma_f32_16x16x32_bf16 v[180:183], a[156:159], a[180:183], v[180:183]\n"
    "v_mul_f32 v140, v140, v53\n"
    "v_mul_f32 v141, v141, v53\n"
    "v_mul_f32 v142, v142, v53\n"
    "v_mul_f32 v143, v143, v53\n"
    "v_cvt_pk_bf16_f32 v136, v136, v137\n"
    "v_mfma_f32_16x16x32_bf16 v[188:191], a[156:159], a[188:191], v[188:191]\n"
    "v_cvt_pk_bf16_f32 v137, v138, v139\n"
    "v_cvt_pk_bf16_f32 v138, v140, v141\n"
    "v_cvt_pk_bf16_f32 v139, v142, v143\n"
    "v_mul_f32 v152, v152, v55\n"
    "v_mfma_f32_16x16x32_bf16 v[192:195], a[128:131], a[160:163], v[192:195]\n"
    "v_mul_f32 v153, v153, v55\n"
    "v_mul_f32 v154, v154, v55\n"
    "v_mul_f32 v155, v155, v55\n"
    "v_mul_f32 v156, v156, v55\n"
    "v_mfma_f32_16x16x32_bf16 v[200:203], a[128:131], a[168:171], v[200:203]\n"
    "ds_read_b64_tr_b16 a[144:145], v15 offset:1536\n"
    "v_mul_f32 v157, v157, v55\n"
    "v_mul_f32 v158, v158, v55\n"
    "v_mul_f32 v159, v159, v55\n"
    "v_mfma_f32_16x16x32_bf16 v[208:211], a[128:131], a[164:167], v[208:211]\n"
    "ds_read_b64_tr_b16 a[146:147], v15 offset:5632\n"
    "v_cvt_pk_bf16_f32 v152, v152, v153\n"
    "v_cvt_pk_bf16_f32 v153, v154, v155\n"
    "v_mfma_f32_16x16x32_bf16 v[216:219], a[128:131], a[172:175], v[216:219]\n"
    "ds_read_b64_tr_b16 a[148:149], v15 offset:1544\n"
    "v_cvt_pk_bf16_f32 v154, v156, v157\n"
    "v_cvt_pk_bf16_f32 v155, v158, v159\n"
    "s_waitcnt lgkmcnt(3)\n"
    "v_mfma_f32_16x16x32_bf16 v[196:199], a[132:135], a[160:163], v[196:199]\n"
    "ds_read_b64_tr_b16 a[150:151], v15 offset:5640\n"
    "v_mul_f32 v160, v160, v52\n"
    "v_mul_f32 v161, v161, v52\n"
    "v_mfma_f32_16x16x32_bf16 v[204:207], a[132:135], a[168:171], v[204:207]\n"
    "ds_read_b64_tr_b16 a[152:153], v15 offset:9728\n"
    "v_mul_f32 v162, v162, v52\n"
    "v_mul_f32 v163, v163, v52\n"
    "v_mfma_f32_16x16x32_bf16 v[212:215], a[132:135], a[164:167], v[212:215]\n"
    "ds_read_b64_tr_b16 a[154:155], v15 offset:13824\n"
    "v_mul_f32 v164, v164, v52\n"
    "v_mfma_f32_16x16x32_bf16 v[220:223], a[132:135], a[172:175], v[220:223]\n"
    "ds_read_b64_tr_b16 a[156:157], v15 offset:9736\n"
    "v_mul_f32 v165, v165, v52\n"
    "v_mul_f32 v166, v166, v52\n"
    "v_mfma_f32_16x16x32_bf16 v[192:195], a[136:139], a[176:179], v[192:195]\n"
    "ds_read_b64_tr_b16 a[158:159], v15 offset:13832\n"
    "v_mul_f32 v167, v167, v52\n"
    "v_cvt_pk_bf16_f32 v160, v160, v161\n"
    "v_mfma_f32_16x16x32_bf16 v[200:203], a[136:139], a[184:187], v[200:203]\n"
    "v_cvt_pk_bf16_f32 v161, v162, v163\n"
    "v_cvt_pk_bf16_f32 v162, v164, v165\n"
    "v_cvt_pk_bf16_f32 v163, v166, v167\n"
    "v_mfma_f32_16x16x32_bf16 v[208:211], a[136:139], a[180:183], v[208:211]\n"
    "v_mul_f32 v176, v176, v54\n"
    "v_mul_f32 v177, v177, v54\n"
    "v_mul_f32 v178, v178, v54\n"
    "v_mul_f32 v179, v179, v54\n"
    "v_mfma_f32_16x16x32_bf16 v[216:219], a[136:139], a[188:191], v[216:219]\n"
    "v_mul_f32 v180, v180, v54\n"
    "v_mul_f32 v181, v181, v54\n"
    "v_mul_f32 v182, v182, v54\n"
    "v_mul_f32 v183, v183, v54\n"
    "v_mfma_f32_16x16x32_bf16 v[196:199], a[140:143], a[176:179], v[196:199]\n"
    "v_cvt_pk_bf16_f32 v176, v176, v177\n"
    "v_cvt_pk_bf16_f32 v177, v178, v179\n"
    "v_cvt_pk_bf16_f32 v178, v180, v181\n"
    "v_mfma_f32_16x16x32_bf16 v[204:207], a[140:143], a[184:187], v[204:207]\n"
    "v_cvt_pk_bf16_f32 v179, v182, v183\n"
    "v_mul_f32 v168, v168, v53\n"
    "v_mul_f32 v169, v169, v53\n"
    "v_mul_f32 v170, v170, v53\n"
    "v_mfma_f32_16x16x32_bf16 v[212:215], a[140:143], a[180:183], v[212:215]\n"
    "v_mul_f32 v171, v171, v53\n"
    "v_mul_f32 v172, v172, v53\n"
    "v_mul_f32 v173, v173, v53\n"
    "v_mfma_f32_16x16x32_bf16 v[220:223], a[140:143], a[188:191], v[220:223]\n"
    "v_mul_f32 v174, v174, v53\n"
    "v_mul_f32 v175, v175, v53\n"
    "v_cvt_pk_bf16_f32 v168, v168, v169\n"
    "v_cvt_pk_bf16_f32 v169, v170, v171\n"
    "s_waitcnt lgkmcnt(0)\n"
    "v_mfma_f32_16x16x32_bf16 v[224:227], a[144:147], a[160:163], v[224:227]\n"
    "v_cvt_pk_bf16_f32 v170, v172, v173\n"
    "v_cvt_pk_bf16_f32 v171, v174, v175\n"
    "v_mul_f32 v184, v184, v55\n"
    "v_mfma_f32_16x16x32_bf16 v[232:235], a[144:147], a[168:171], v[232:235]\n"
    "v_mul_f32 v185, v185, v55\n"
    "v_mul_f32 v186, v186, v55\n"
    "v_mul_f32 v187, v187, v55\n"
    "v_mul_f32 v188, v188, v55\n"
    "v_mfma_f32_16x16x32_bf16 v[240:243], a[144:147], a[164:167], v[240:243]\n"
    "v_mul_f32 v189, v189, v55\n"
    "v_mul_f32 v190, v190, v55\n"
    "v_mul_f32 v191, v191, v55\n"
    "v_mfma_f32_16x16x32_bf16 v[248:251], a[144:147], a[172:175], v[248:251]\n"
    "v_cvt_pk_bf16_f32 v184, v184, v185\n"
    "v_cvt_pk_bf16_f32 v185, v186, v187\n"
    "v_cvt_pk_bf16_f32 v186, v188, v189\n"
    "v_cvt_pk_bf16_f32 v187, v190, v191\n"
    "v_mfma_f32_16x16x32_bf16 v[228:231], a[148:151], a[160:163], v[228:231]\n"
    "v_mul_f32 v192, v192, v52\n"
    "v_mul_f32 v193, v193, v52\n"
    "v_mfma_f32_16x16x32_bf16 v[236:239], a[148:151], a[168:171], v[236:239]\n"
    "v_mul_f32 v194, v194, v52\n"
    "v_mul_f32 v195, v195, v52\n"
    "v_mfma_f32_16x16x32_bf16 v[244:247], a[148:151], a[164:167], v[244:247]\n"
    "v_mul_f32 v196, v196, v52\n"
    "v_mul_f32 v197, v197, v52\n"
    "v_mfma_f32_16x16x32_bf16 v[252:255], a[148:151], a[172:175], v[252:255]\n"
    "v_mul_f32 v198, v198, v52\n"
    "v_mul_f32 v199, v199, v52\n"
    "v_mfma_f32_16x16x32_bf16 v[224:227], a[152:155], a[176:179], v[224:227]\n"
    "v_cvt_pk_bf16_f32 v192, v192, v193\n"
    "v_cvt_pk_bf16_f32 v193, v194, v195\n"
    "v_mfma_f32_16x16x32_bf16 v[232:235], a[152:155], a[184:187], v[232:235]\n"
    "v_cvt_pk_bf16_f32 v194, v196, v197\n"
    "v_cvt_pk_bf16_f32 v195, v198, v199\n"
    "v_mfma_f32_16x16x32_bf16 v[240:243], a[152:155], a[180:183], v[240:243]\n"
    "v_mul_f32 v208, v208, v54\n"
    "v_mul_f32 v209, v209, v54\n"
    "v_mfma_f32_16x16x32_bf16 v[248:251], a[152:155], a[188:191], v[248:251]\n"
    "v_mul_f32 v210, v210, v54\n"
    "v_mul_f32 v211, v211, v54\n"
    "v_mfma_f32_16x16x32_bf16 v[228:231], a[156:159], a[176:179], v[228:231]\n"
    "v_mul_f32 v212, v212, v54\n"
    "v_mul_f32 v213, v213, v54\n"
    "v_mfma_f32_16x16x32_bf16 v[236:239], a[156:159], a[184:187], v[236:239]\n"
    "v_mul_f32 v214, v214, v54\n"
    "v_mul_f32 v215, v215, v54\n"
    "v_mfma_f32_16x16x32_bf16 v[244:247], a[156:159], a[180:183], v[244:247]\n"
    "v_cvt_pk_bf16_f32 v208, v208, v209\n"
    "v_cvt_pk_bf16_f32 v209, v210, v211\n"
    "v_mfma_f32_16x16x32_bf16 v[252:255], a[156:159], a[188:191], v[252:255]\n"
    "v_cvt_pk_bf16_f32 v210, v212, v213\n"
    "v_cvt_pk_bf16_f32 v211, v214, v215\n"
    "; PHASE Qep\n"
    "s_waitcnt vmcnt(8)\n"
    "s_barrier\n"
    "; PHASE T3\n"
    "ds_read_b128 a[128:131], v11 offset:32768\n"
    "ds_read_b128 a[132:135], v12 offset:32768\n"
    "ds_read_b128 a[136:139], v13 offset:32768\n"
    "ds_read_b128 a[140:143], v14 offset:32768\n"
    "ds_read_b128 a[192:195], v11 offset:36864\n"
    "ds_read_b128 a[196:199], v12 offset:36864\n"
    "ds_read_b128 a[200:203], v13 offset:36864\n"
    "ds_read_b128 a[204:207], v14 offset:36864\n"
    "s_waitcnt lgkmcnt(7)\n"
    "v_mfma_f32_16x16x32_bf16 v[64:67], a[128:131], v[128:131], 0\n"
    "s_add_u32 m0, s91, 0x8000\n"
    "v_mul_f32 v200, v200, v53\n"
    "v_mul_f32 v201, v201, v53\n"
    "v_mul_f32 v202, v202, v53\n"
    "v_mul_f32 v203, v203, v53\n"
    "v_mfma_f32_16x16x32_bf16 v[80:83], a[128:131], v[144:147], 0\n"
    "s_add_u32 s90, s91, 0x4000\n"
    "v_mul_f32 v204, v204, v53\n"
    "v_mul_f32 v205, v205, v53\n"
    "v_mul_f32 v206, v206, v53\n"
    "v_mfma_f32_16x16x32_bf16 v[72:75], a[128:131], v[136:139], 0\n"
    "buffer_load_dwordx4 v19, s[56:59], s90 offen lds\n"
    "v_mul_f32 v207, v207, v53\n"
    "v_cvt_pk_bf16_f32 v200, v200, v201\n"
    "v_cvt_pk_bf16_f32 v201, v202, v203\n"
    "v_mfma_f32_16x16x32_bf16 v[88:91], a[128:131], v[152:155], 0\n"
    "s_waitcnt lgkmcnt(0)\n"
    "v_cvt_pk_bf16_f32 v202, v204, v205\n"
    "v_cvt_pk_bf16_f32 v203, v206, v207\n"
    "v_mul_f32 v216, v216, v55\n"
    "v_mul_f32 v217, v217, v55\n"
    "v_mfma_f32_16x16x32_bf16 v[64:67], a[132:135], v[160:163], v[64:67]\n"
    "buffer_load_dwordx4 v20, s[56:59], s90 offen offset:1024 lds\n"
    "v_mul_f32 v218, v218, v55\n"
    "v_mul_f32 v219, v219, v55\n"
    "v_mul_f32 v220, v220, v55\n"
    "v_mul_f32 v224, v224, v52\n"
    "v_mul_f32 v225, v225, v52\n"
    "v_mul_f32 v226, v226, v52\n"
    "v_mul_f32 v227, v227, v52\n"
    "v_mul_f32 v228, v228, v52\n"
    "v_mfma_f32_16x16x32_bf16 v[80:83], a[132:135], v[176:179], v[80:83]\n"
    "v_mul_f32 v221, v221, v55\n"
    "v_mul_f32 v222, v222, v55\n"
    "v_mul_f32 v223, v223, v55\n"
    "v_cvt_pk_bf16_f32 v216, v216, v217\n"
    "v_mul_f32 v229, v229, v52\n"
    "v_mul_f32 v230, v230, v52\n"
    "v_mul_f32 v231, v231, v52\n"
    "v_cvt_pk_bf16_f32 v224, v224, v225\n"
    "v_cvt_pk_bf16_f32 v225, v226, v227\n"
    "v_mfma_f32_16x16x32_bf16 v[72:75], a[132:135], v[168:171], v[72:75]\n"
    "buffer_load_dwordx4 v21, s[56:59], s90 offen offset:2048 lds\n"
    "v_cvt_pk_bf16_f32 v217, v218, v219\n"
    "v_cvt_pk_bf16_f32 v218, v220, v221\n"
    "v_cvt_pk_bf16_f32 v219, v222, v223\n"
    "v_cvt_pk_bf16_f32 v226, v228, v229\n"
    "v_cvt_pk_bf16_f32 v227, v230, v231\n"
    "v_mul_f32 v240, v240, v54\n"
    "v_mul_f32 v241, v241, v54\n"
    "v_mul_f32 v242, v242, v54\n"
    "v_mfma_f32_16x16x32_bf16 v[88:91], a[132:135], v[184:187], v[88:91]\n"
    "v_mul_f32 v243, v243, v54\n"
    "v_mul_f32 v244, v244, v54\n"
    "v_mul_f32 v245, v245, v54\n"
    "v_mul_f32 v246, v246, v54\n"
    "v_mul_f32 v247, v247, v54\n"
    "v_cvt_pk_bf16_f32 v240, v240, v241\n"
    "v_cvt_pk_bf16_f32 v241, v242, v243\n"
    "v_cvt_pk_bf16_f32 v242, v244, v245\n"
    "v_cvt_pk_bf16_f32 v243, v246, v247\n"
    "v_mfma_f32_16x16x32_bf16 v[64:67], a[136:139], v[192:195], v[64:67]\n"
    "buffer_load_dwordx4 v22, s[56:59], s90 offen offset:3072 lds\n"
    "v_mul_f32 v232, v232, v53\n"
    "v_mul_f32 v233, v233, v53\n"
    "v_mul_f32 v234, v234, v53\n"
    "v_mul_f32 v235, v235, v53\n"
    "v_mul_f32 v236, v236, v53\n"
    "v_mul_f32 v237, v237, v53\n"
    "v_mul_f32 v238, v238, v53\n"
    "v_mfma_f32_16x16x32_bf16 v[80:83], a[136:139], v[208:211], v[80:83]\n"
    "v_mul_f32 v239, v239, v53\n"
    "v_cvt_pk_bf16_f32 v232, v232, v233\n"
    "v_cvt_pk_bf16_f32 v233, v234, v235\n"
    "v_cvt_pk_bf16_f32 v234, v236, v237\n"
    "v_cvt_pk_bf16_f32 v235, v238, v239\n"
    "v_mul_f32 v248, v248, v55\n"
    "v_mul_f32 v249, v249, v55\n"
    "v_mul_f32 v250, v250, v55\n"
    "v_mul_f32 v251, v251, v55\n"
    "v_mfma_f32_16x16x32_bf16 v[72:75], a[136:139], v[200:203], v[72:75]\n"
    "s_add_u32 m0, s91, 0\n"
    "v_mul_f32 v252, v252, v55\n"
    "v_mul_f32 v253, v253, v55\n"
    "v_mul_f32 v254, v254, v55\n"
    "v_mul_f32 v255, v255, v55\n"
    "v_cvt_pk_bf16_f32 v248, v248, v249\n"
    "v_cvt_pk_bf16_f32 v249, v250, v251\n"
    "v_cvt_pk_bf16_f32 v250, v252, v253\n"
    "v_cvt_pk_bf16_f32 v251, v254, v255\n"
    "v_mfma_f32_16x16x32_bf16 v[88:91], a[136:139], v[216:219], v[88:91]\n"
    "s_add_u32 s90, s91, 0x4000\n"
    "v_mfma_f32_16x16x32_bf16 v[64:67], a[140:143], v[224:227], v[64:67]\n"
    "buffer_load_dwordx4 v23, s[60:63], s90 offen lds\n"
    "v_mfma_f32_16x16x32_bf16 v[80:83], a[140:143], v[240:243], v[80:83]\n"
    "v_mfma_f32_16x16x32_bf16 v[72:75], a[140:143], v[232:235], v[72:75]\n"
    "buffer_load_dwordx4 v24, s[60:63], s90 offen offset:1024 lds\n"
    "v_mfma_f32_16x16x32_bf16 v[88:91], a[140:143], v[248:251], v[88:91]\n"
    "v_mfma_f32_16x16x32_bf16 v[68:71], a[192:195], v[128:131], 0\n"
    "buffer_load_dwordx4 v23, s[60:63], s90 offen offset:2048 lds\n"
    "v_mfma_f32_16x16x32_bf16 v[84:87], a[192:195], v[144:147], 0\n"
    "ds_read_b128 a[128:131], v11 offset:40960\n"
    "v_mfma_f32_16x16x32_bf16 v[76:79], a[192:195], v[136:139], 0\n"
    "buffer_load_dwordx4 v24, s[60:63], s90 offen offset:3072 lds\n"
    "v_mfma_f32_16x16x32_bf16 v[92:95], a[192:195], v[152:155], 0\n"
    "ds_read_b128 a[132:135], v12 offset:40960\n"
    "v_mfma_f32_16x16x32_bf16 v[68:71], a[196:199], v[160:163], v[68:71]\n"
    "s_add_u32 m0, s91, 0xc000\n"
    "v_mfma_f32_16x16x32_bf16 v[84:87], a[196:199], v[176:179], v[84:87]\n"
    "ds_read_b128 a[136:139], v13 offset:40960\n"
    "s_add_u32 s90, s91, 0x8000\n"
    "v_mfma_f32_16x16x32_bf16 v[76:79], a[196:199], v[168:171], v[76:79]\n"
    "buffer_load_dwordx4 v19, s[56:59], s90 offen lds\n"
    "v_mfma_f32_16x16x32_bf16 v[92:95], a[196:199], v[184:187], v[92:95]\n"
    "ds_read_b128 a[140:143], v14 offset:40960\n"
    "v_mfma_f32_16x16x32_bf16 v[68:71], a[200:203], v[192:195], v[68:71]\n"
    "buffer_load_dwordx4 v20, s[56:59], s90 offen offset:1024 lds\n"
    "v_mfma_f32_16x16x32_bf16 v[84:87], a[200:203], v[208:211], v[84:87]\n"
    "v_lshlrev_b32 v62, 6, v1\n"
    "v_mfma_f32_16x16x32_bf16 v[76:79], a[200:203], v[200:203], v[76:79]\n"
    "buffer_load_dwordx4 v21, s[56:59], s90 offen offset:2048 lds\n"
    "v_mfma_f32_16x16x32_bf16 v[92:95], a[200:203], v[216:219], v[92:95]\n"
    "v_add_u32 v62, 0x1000, v62\n"
    "v_mfma_f32_16x16x32_bf16 v[68:71], a[204:207], v[224:227], v[68:71]\n"
    "buffer_load_dwordx4 v22, s[56:59], s90 offen offset:3072 lds\n"
    "v_mfma_f32_16x16x32_bf16 v[84:87], a[204:207], v[240:243], v[84:87]\n"
    "v_mfma_f32_16x16x32_bf16 v[76:79], a[204:207], v[232:235], v[76:79]\n"
    "v_mfma_f32_16x16x32_bf16 v[92:95], a[204:207], v[248:251], v[92:95]\n"
    "s_waitcnt lgkmcnt(0)\n"
    "v_mfma_f32_16x16x32_bf16 v[96:99], a[128:131], v[128:131], 0\n"
    "v_mfma_f32_16x16x32_bf16 v[112:115], a[128:131], v[144:147], 0\n"
    "ds_read_b64_tr_b16 a[160:161], v7\n"
    "v_mfma_f32_16x16x32_bf16 v[104:107], a[128:131], v[136:139], 0\n"
    "v_mfma_f32_16x16x32_bf16 v[120:123], a[128:131], v[152:155], 0\n"
    "ds_read_b64_tr_b16 a[162:163], v7 offset:4096\n"
    "v_exp_f32 v64, v64\n"
    "v_mfma_f32_16x16x32_bf16 v[96:99], a[132:135], v[160:163], v[96:99]\n"
    "ds_read_b128 a[128:131], v11 offset:45056\n"
    "v_exp_f32 v65, v65\n"
    "v_mfma_f32_16x16x32_bf16 v[112:115], a[132:135], v[176:179], v[112:115]\n"
    "ds_read_b64_tr_b16 a[164:165], v8\n"
    "v_exp_f32 v66, v66\n"
    "v_cvt_pk_bf16_f32 v64, v64, v65\n"
    "v_mfma_f32_16x16x32_bf16 v[104:107], a[132:135], v[168:171], v[104:107]\n"
    "v_exp_f32 v67, v67\n"
    "v_mfma_f32_16x16x32_bf16 v[120:123], a[132:135], v[184:187], v[120:123]\n"
    "ds_read_b64_tr_b16 a[166:167], v8 offset:4096\n"
    "v_exp_f32 v68, v68\n"
    "v_cvt_pk_bf16_f32 v65, v66, v67\n"
    "v_mfma_f32_16x16x32_bf16 v[96:99], a[136:139], v[192:195], v[96:99]\n"
    "ds_read_b128 a[132:135], v12 offset:45056\n"
    "v_exp_f32 v69, v69\n"
    "v_mfma_f32_16x16x32_bf16 v[112:115], a[136:139], v[208:211], v[112:115]\n"
    "ds_read_b64_tr_b16 a[168:169], v7 offset:512\n"
    "v_exp_f32 v70, v70\n"
    "v_mfma_f32_16x16x32_bf16 v[104:107], a[136:139], v[200:203], v[104:107]\n"
    "v_exp_f32 v71, v71\n"
    "v_cvt_pk_bf16_f32 v66, v68, v69\n"
    "v_cvt_pk_bf16_f32 v67, v70, v71\n"
    "v_mfma_f32_16x16x32_bf16 v[120:123], a[136:139], v[216:219], v[120:123]\n"
    "ds_read_b64_tr_b16 a[170:171], v7 offset:4608\n"
    "v_exp_f32 v80, v80\n"
    "v_mfma_f32_16x16x32_bf16 v[96:99], a[140:143], v[224:227], v[96:99]\n"
    "ds_read_b128 a[136:139], v13 offset:45056\n"
    "v_exp_f32 v81, v81\n"
    "v_mfma_f32_16x16x32_bf16 v[112:115], a[140:143], v[240:243], v[112:115]\n"
    "ds_read_b64_tr_b16 a[172:173], v8 offset:512\n"
    "v_exp_f32 v82, v82\n"
    "v_mfma_f32_16x16x32_bf16 v[104:107], a[140:143], v[232:235], v[104:107]\n"
    "v_exp_f32 v83, v83\n"
    "v_cvt_pk_bf16_f32 v80, v80, v81\n"
    "v_mfma_f32_16x16x32_bf16 v[120:123], a[140:143], v[248:251], v[120:123]\n"
    "s_waitcnt lgkmcnt(3)\n"
    "ds_read_b64_tr_b16 a[174:175], v8 offset:4608\n"
    "v_exp_f32 v84, v84\n"
    "v_cvt_pk_bf16_f32 v81, v82, v83\n"
    "v_mfma_f32_16x16x32_bf16 v[100:103], a[128:131], v[128:131], 0\n"
    "ds_read_b128 a[140:143], v14 offset:45056\n"
    "v_exp_f32 v85, v85\n"
    "v_mfma_f32_16x16x32_bf16 v[116:119], a[128:131], v[144:147], 0\n"
    "ds_read_b64_tr_b16 a[176:177], v7 offset:1024\n"
    "v_exp_f32 v86, v86\n"
    "v_cvt_pk_bf16_f32 v82, v84, v85\n"
    "v_mfma_f32_16x16x32_bf16 v[108:111], a[128:131], v[136:139], 0\n"
    "v_exp_f32 v87, v87\n"
    "v_mfma_f32_16x16x32_bf16 v[124:127], a[128:131], v[152:155], 0\n"
    "ds_read_b64_tr_b16 a[178:179], v7 offset:5120\n"
    "v_exp_f32 v72, v72\n"
    "v_cvt_pk_bf16_f32 v83, v86, v87\n"
    "v_mfma_f32_16x16x32_bf16 v[100:103], a[132:135], v[160:163], v[100:103]\n"
    "v_exp_f32 v73, v73\n"
    "v_mfma_f32_16x16x32_bf16 v[116:119], a[132:135], v[176:179], v[116:119]\n"
    "ds_read_b64_tr_b16 a[180:181], v8 offset:1024\n"
    "v_exp_f32 v74, v74\n"
    "v_mfma_f32_16x16x32_bf16 v[108:111], a[132:135], v[168:171], v[108:111]\n"
    "v_exp_f32 v75, v75\n"
    "v_cvt_pk_bf16_f32 v72, v72, v73\n"
    "v_cvt_pk_bf16_f32 v73, v74, v75\n"
    "v_mfma_f32_16x16x32_bf16 v[124:127], a[132:135], v[184:187], v[124:127]\n"
    "s_waitcnt lgkmcnt(2)\n"
    "ds_read_b64_tr_b16 a[182:183], v8 offset:5120\n"
    "v_exp_f32 v76, v76\n"
    "v_mfma_f32_16x16x32_bf16 v[100:103], a[136:139], v[192:195], v[100:103]\n"
    "v_exp_f32 v77, v77\n"
    "v_exp_f32 v78, v78\n"
    "v_mfma_f32_16x16x32_bf16 v[116:119], a[136:139], v[208:211], v[116:119]\n"
    "ds_read_b64_tr_b16 a[184:185], v7 offset:1536\n"
    "v_exp_f32 v79, v79\n"
    "v_mfma_f32_16x16x32_bf16 v[108:111], a[136:139], v[200:203], v[108:111]\n"
    "v_cvt_pk_bf16_f32 v74, v76, v77\n"
    "v_cvt_pk_bf16_f32 v75, v78, v79\n"
    "v_mfma_f32_16x16x32_bf16 v[124:127], a[136:139], v[216:219], v[124:127]\n"
    "ds_read_b64_tr_b16 a[186:187], v7 offset:5632\n"
    "v_exp_f32 v88, v88\n"
    "v_mfma_f32_16x16x32_bf16 v[100:103], a[140:143], v[224:227], v[100:103]\n"
    "v_exp_f32 v89, v89\n"
    "v_mfma_f32_16x16x32_bf16 v[116:119], a[140:143], v[240:243], v[116:119]\n"
    "ds_read_b64_tr_b16 a[188:189], v8 offset:1536\n"
    "v_exp_f32 v90, v90\n"
    "v_cvt_pk_bf16_f32 v88, v88, v89\n"
    "v_mfma_f32_16x16x32_bf16 v[108:111], a[140:143], v[232:235], v[108:111]\n"
    "v_exp_f32 v91, v91\n"
    "v_mfma_f32_16x16x32_bf16 v[124:127], a[140:143], v[248:251], v[124:127]\n"
    "ds_read_b64_tr_b16 a[190:191], v8 offset:5632\n"
    "v_exp_f32 v92, v92\n"
    "v_cvt_pk_bf16_f32 v89, v90, v91\n"
    "v_exp_f32 v93, v93\n"
    "v_exp_f32 v94, v94\n"
    "v_exp_f32 v95, v95\n"
    "v_cvt_pk_bf16_f32 v90, v92, v93\n"
    "v_cvt_pk_bf16_f32 v91, v94, v95\n"
    "v_mfma_f32_16x16x32_bf16 a[144:147], v[34:37], v[64:67], 0\n"
    "v_mfma_f32_16x16x32_bf16 a[152:155], v[34:37], v[80:83], 0\n"
    "v_mfma_f32_16x16x32_bf16 a[148:151], v[34:37], v[72:75], 0\n"
    "v_mfma_f32_16x16x32_bf16 a[156:159], v[34:37], v[88:91], 0\n"
    "v_exp_f32 v96, v96\n"
    "s_waitcnt vmcnt(12)\n"
    "s_waitcnt vmcnt(4)\n"
    "s_barrier\n"
    "v_mfma_f32_16x16x32_bf16 a[0:3], a[160:163], v[64:67], 0\n"
    "s_lshl_b32 s37, s34, 8\n"
    "v_mfma_f32_16x16x32_bf16 a[16:19], a[160:163], v[80:83], 0\n"
    "s_lshl_b32 s38, s33, 3\n"
    "v_mfma_f32_16x16x32_bf16 a[8:11], a[160:163], v[72:75], 0\n"
    "s_add_u32 s37, s37, s38\n"
    "v_mfma_f32_16x16x32_bf16 a[24:27], a[160:163], v[88:91], 0\n"
    "s_add_u32 s37, s37, 64\n"
    "v_mfma_f32_16x16x32_bf16 a[4:7], a[164:167], v[64:67], 0\n"
    "ds_read_b128 a[128:131], v11\n"
    "s_cmp_eq_u32 s32, 0\n"
    "s_cselect_b32 s37, s37, 0x40000000\n"
    "v_mfma_f32_16x16x32_bf16 a[20:23], a[164:167], v[80:83], 0\n"
    "v_exp_f32 v97, v97\n"
    "v_mfma_f32_16x16x32_bf16 a[12:15], a[164:167], v[72:75], 0\n"
    "v_exp_f32 v98, v98\n"
    "v_mfma_f32_16x16x32_bf16 a[28:31], a[164:167], v[88:91], 0\n"
    "ds_read_b128 a[132:135], v12\n"
    "v_cvt_pk_bf16_f32 v96, v96, v97\n"
    "v_mfma_f32_16x16x32_bf16 a[32:35], a[168:171], v[64:67], 0\n"
    "v_cmp_eq_u32 vcc, 0, v1\n"
    "v_mov_b32 v54, 0x40000000\n"
    "v_mov_b32 v53, s37\n"
    "v_cndmask_b32 v53, v54, v53\n"
    "v_mfma_f32_16x16x32_bf16 a[48:51], a[168:171], v[80:83], 0\n"
    "v_exp_f32 v99, v99\n"
    "v_mfma_f32_16x16x32_bf16 a[40:43], a[168:171], v[72:75], 0\n"
    "ds_read_b128 a[136:139], v13\n"
    "v_add_co_u32 v50, vcc, 0x7f4a7c15, v38\n"
    "v_mov_b32 v54, 0x9e3779b9\n"
    "s_nop 0\n"
    "v_addc_co_u32 v51, vcc, v54, v39, vcc\n"
    "v_mfma_f32_16x16x32_bf16 a[56:59], a[168:171], v[88:91], 0\n"
    "buffer_store_dwordx2 v[50:51], v53, s[84:87], 0 offen sc1\n"
    "v_mfma_f32_16x16x32_bf16 a[36:39], a[172:175], v[64:67], 0\n"
    "s_add_u32 m0, s91, 0x4000\n"
    "v_exp_f32 v100, v100\n"
    "v_mfma_f32_16x16x32_bf16 a[52:55], a[172:175], v[80:83], 0\n"
    "ds_read_b128 a[140:143], v14\n"
    "s_add_u32 s90, s91, 0x8000\n"
    "v_mfma_f32_16x16x32_bf16 a[44:47], a[172:175], v[72:75], 0\n"
    "buffer_load_dwordx4 v23, s[60:63], s90 offen lds\n"
    "v_cvt_pk_bf16_f32 v97, v98, v99\n"
    "v_mfma_f32_16x16x32_bf16 a[60:63], a[172:175], v[88:91], 0\n"
    "v_exp_f32 v101, v101\n"
    "s_waitcnt lgkmcnt(1)\n"
    "v_mfma_f32_16x16x32_bf16 a[64:67], a[176:179], v[64:67], 0\n"
    "v_exp_f32 v102, v102\n"
    "v_mfma_f32_16x16x32_bf16 a[80:83], a[176:179], v[80:83], 0\n"
    "v_exp_f32 v103, v103\n"
    "v_mfma_f32_16x16x32_bf16 a[72:75], a[176:179], v[72:75], 0\n"
    "buffer_load_dwordx4 v24, s[60:63], s90 offen offset:1024 lds\n"
    "v_cvt_pk_bf16_f32 v98, v100, v101\n"
    "v_mfma_f32_16x16x32_bf16 a[88:91], a[176:179], v[88:91], 0\n"
    "v_cvt_pk_bf16_f32 v99, v102, v103\n"
    "v_mfma_f32_16x16x32_bf16 a[68:71], a[180:183], v[64:67], 0\n"
    "v_exp_f32 v112, v112\n"
    "v_mfma_f32_16x16x32_bf16 a[84:87], a[180:183], v[80:83], 0\n"
    "v_exp_f32 v113, v113\n"
    "v_mfma_f32_16x16x32_bf16 a[76:79], a[180:183], v[72:75], 0\n"
    "buffer_load_dwordx4 v23, s[60:63], s90 offen offset:2048 lds\n"
    "v_mfma_f32_16x16x32_bf16 a[92:95], a[180:183], v[88:91], 0\n"
    "v_exp_f32 v114, v114\n"
    "v_mfma_f32_16x16x32_bf16 a[96:99], a[184:187], v[64:67], 0\n"
    "v_exp_f32 v115, v115\n"
    "v_mfma_f32_16x16x32_bf16 a[112:115], a[184:187], v[80:83], 0\n"
    "v_cvt_pk_bf16_f32 v112, v112, v113\n"
    "v_cvt_pk_bf16_f32 v113, v114, v115\n"
    "v_mfma_f32_16x16x32_bf16 a[104:107], a[184:187], v[72:75], 0\n"
    "buffer_load_dwordx4 v24, s[60:63], s90 offen offset:3072 lds\n"
    "v_mfma_f32_16x16x32_bf16 a[120:123], a[184:187], v[88:91], 0\n"
    "v_exp_f32 v116, v116\n"
    "v_mfma_f32_16x16x32_bf16 a[100:103], a[188:191], v[64:67], 0\n"
    "v_exp_f32 v117, v117\n"
    "v_mfma_f32_16x16x32_bf16 a[116:119], a[188:191], v[80:83], 0\n"
    "v_exp_f32 v118, v118\n"
    "v_mfma_f32_16x16x32_bf16 a[108:111], a[188:191], v[72:75], 0\n"
    "v_exp_f32 v119, v119\n"
    "v_mfma_f32_16x16x32_bf16 a[124:127], a[188:191], v[88:91], 0\n"
    "v_cvt_pk_bf16_f32 v114, v116, v117\n"
    "v_cvt_pk_bf16_f32 v115, v118, v119\n"
    "v_mfma_f32_16x16x32_bf16 v[64:67], a[128:131], v[128:131], 0\n"
    "v_exp_f32 v104, v104\n"
    "v_mfma_f32_16x16x32_bf16 v[80:83], a[128:131], v[144:147], 0\n"
    "ds_read_b64_tr_b16 a[160:161], v7 offset:8192\n"
    "v_mfma_f32_16x16x32_bf16 v[72:75], a[128:131], v[136:139], 0\n"
    "v_exp_f32 v105, v105\n"
    "v_mfma_f32_16x16x32_bf16 v[88:91], a[128:131], v[152:155], 0\n"
    "ds_read_b64_tr_b16 a[162:163], v7 offset:12288\n"
    "v_mfma_f32_16x16x32_bf16 v[64:67], a[132:135], v[160:163], v[64:67]\n"
    "ds_read_b128 a[128:131], v11 offset:4096\n"
    "v_mfma_f32_16x16x32_bf16 v[80:83], a[132:135], v[176:179], v[80:83]\n"
    "ds_read_b64_tr_b16 a[164:165], v8 offset:8192\n"
    "s_mul_i32 s37, s32, 0x70000\n"
    "s_lshl_b32 s38, s36, 7\n"
    "s_add_u32 s94, s37, s38\n"
    "v_mfma_f32_16x16x32_bf16 v[72:75], a[132:135], v[168:171], v[72:75]\n"
    "s_add_u32 m0, s89, 0\n"
    "v_exp_f32 v106, v106\n"
    "v_mfma_f32_16x16x32_bf16 v[88:91], a[132:135], v[184:187], v[88:91]\n"
    "ds_read_b64_tr_b16 a[166:167], v8 offset:12288\n"
    "v_cvt_pk_bf16_f32 v104, v104, v105\n"
    "v_mfma_f32_16x16x32_bf16 v[64:67], a[136:139], v[192:195], v[64:67]\n"
    "ds_read_b128 a[132:135], v12 offset:4096\n"
    "buffer_load_dwordx4 v29, s[68:71], s94 offen lds\n"
    "v_mfma_f32_16x16x32_bf16 v[80:83], a[136:139], v[208:211], v[80:83]\n"
    "ds_read_b64_tr_b16 a[168:169], v7 offset:8704\n"
    "v_mfma_f32_16x16x32_bf16 v[72:75], a[136:139], v[200:203], v[72:75]\n"
    "v_exp_f32 v107, v107\n"
    "v_mfma_f32_16x16x32_bf16 v[88:91], a[136:139], v[216:219], v[88:91]\n"
    "ds_read_b64_tr_b16 a[170:171], v7 offset:12800\n"
    "s_waitcnt lgkmcnt(4)\n"
    "v_mfma_f32_16x16x32_bf16 v[64:67], a[140:143], v[224:227], v[64:67]\n"
    "ds_read_b128 a[136:139], v13 offset:4096\n"
    "v_mfma_f32_16x16x32_bf16 v[80:83], a[140:143], v[240:243], v[80:83]\n"
    "ds_read_b64_tr_b16 a[172:173], v8 offset:8704\n"
    "v_mfma_f32_16x16x32_bf16 v[72:75], a[140:143], v[232:235], v[72:75]\n"
    "buffer_load_dwordx4 v30, s[68:71], s94 offen offset:1024 lds\n"
    "v_mfma_f32_16x16x32_bf16 v[88:91], a[140:143], v[248:251], v[88:91]\n"
    "ds_read_b64_tr_b16 a[174:175], v8 offset:12800\n"
    "v_mfma_f32_16x16x32_bf16 v[68:71], a[128:131], v[128:131], 0\n"
    "ds_read_b128 a[140:143], v14 offset:4096\n"
    "v_mfma_f32_16x16x32_bf16 v[84:87], a[128:131], v[144:147], 0\n"
    "ds_read_b64_tr_b16 a[176:177], v7 offset:9216\n"
    "v_mfma_f32_16x16x32_bf16 v[76:79], a[128:131], v[136:139], 0\n"
    "v_exp_f32 v108, v108\n"
    "v_mfma_f32_16x16x32_bf16 v[92:95], a[128:131], v[152:155], 0\n"
    "ds_read_b64_tr_b16 a[178:179], v7 offset:13312\n"
    "v_cvt_pk_bf16_f32 v105, v106, v107\n"
    "s_waitcnt lgkmcnt(4)\n"
    "v_mfma_f32_16x16x32_bf16 v[68:71], a[132:135], v[160:163], v[68:71]\n"
    "buffer_load_dwordx4 v31, s[68:71], s94 offen offset:2048 lds\n"
    "v_mfma_f32_16x16x32_bf16 v[84:87], a[132:135], v[176:179], v[84:87]\n"
    "ds_read_b64_tr_b16 a[180:181], v8 offset:9216\n"
    "v_mfma_f32_16x16x32_bf16 v[76:79], a[132:135], v[168:171], v[76:79]\n"
    "v_exp_f32 v109, v109\n"
    "v_mfma_f32_16x16x32_bf16 v[92:95], a[132:135], v[184:187], v[92:95]\n"
    "ds_read_b64_tr_b16 a[182:183], v8 offset:13312\n"
    "v_mfma_f32_16x16x32_bf16 v[68:71], a[136:139], v[192:195], v[68:71]\n"
    "v_exp_f32 v110, v110\n"
    "v_mfma_f32_16x16x32_bf16 v[84:87], a[136:139], v[208:211], v[84:87]\n"
    "ds_read_b64_tr_b16 a[184:185], v7 offset:9728\n"
    "v_cvt_pk_bf16_f32 v106, v108, v109\n"
    "v_mfma_f32_16x16x32_bf16 v[76:79], a[136:139], v[200:203], v[76:79]\n"
    "buffer_load_dwordx4 v32, s[68:71], s94 offen offset:3072 lds\n"
    "v_mfma_f32_16x16x32_bf16 v[92:95], a[136:139], v[216:219], v[92:95]\n"
    "ds_read_b64_tr_b16 a[186:187], v7 offset:13824\n"
    "s_waitcnt lgkmcnt(3)\n"
    "v_mfma_f32_16x16x32_bf16 v[68:71], a[140:143], v[224:227], v[68:71]\n"
    "s_add_u32 s94, s94, 0x8000\n"
    "v_exp_f32 v111, v111\n"
    "v_mfma_f32_16x16x32_bf16 v[84:87], a[140:143], v[240:243], v[84:87]\n"
    "ds_read_b64_tr_b16 a[188:189], v8 offset:9728\n"
    "v_mfma_f32_16x16x32_bf16 v[76:79], a[140:143], v[232:235], v[76:79]\n"
    "v_exp_f32 v120, v120\n"
    "v_mfma_f32_16x16x32_bf16 v[92:95], a[140:143], v[248:251], v[92:95]\n"
    "ds_read_b64_tr_b16 a[190:191], v8 offset:13824\n"
    "v_cvt_pk_bf16_f32 v107, v110, v111\n"
    "v_exp_f32 v121, v121\n"
    "v_exp_f32 v122, v122\n"
    "v_exp_f32 v123, v123\n"
    "v_cvt_pk_bf16_f32 v120, v120, v121\n"
    "v_exp_f32 v124, v124\n"
    "v_cvt_pk_bf16_f32 v121, v122, v123\n"
    "v_exp_f32 v125, v125\n"
    "v_exp_f32 v126, v126\n"
    "v_exp_f32 v127, v127\n"
    "v_cvt_pk_bf16_f32 v122, v124, v125\n"
    "v_cvt_pk_bf16_f32 v123, v126, v127\n"
    "v_mfma_f32_16x16x32_bf16 a[144:147], v[34:37], v[96:99], a[144:147]\n"
    "v_mfma_f32_16x16x32_bf16 a[152:155], v[34:37], v[112:115], a[152:155]\n"
    "v_mfma_f32_16x16x32_bf16 a[148:151], v[34:37], v[104:107], a[148:151]\n"
    "v_mfma_f32_16x16x32_bf16 a[156:159], v[34:37], v[120:123], a[156:159]\n"
    "v_exp_f32 v64, v64\n"
    "v_mfma_f32_16x16x32_bf16 a[0:3], a[160:163], v[96:99], a[0:3]\n"
    "v_exp_f32 v65, v65\n"
    "v_mfma_f32_16x16x32_bf16 a[16:19], a[160:163], v[112:115], a[16:19]\n"
    "v_exp_f32 v66, v66\n"
    "v_mfma_f32_16x16x32_bf16 a[8:11], a[160:163], v[104:107], a[8:11]\n"
    "v_exp_f32 v67, v67\n"
    "v_mfma_f32_16x16x32_bf16 a[24:27], a[160:163], v[120:123], a[24:27]\n"
    "v_cvt_pk_bf16_f32 v64, v64, v65\n"
    "v_cvt_pk_bf16_f32 v65, v66, v67\n"
    "v_mfma_f32_16x16x32_bf16 a[4:7], a[164:167], v[96:99], a[4:7]\n"
    "ds_read_b128 a[128:131], v11 offset:8192\n"
    "v_mfma_f32_16x16x32_bf16 a[20:23], a[164:167], v[112:115], a[20:23]\n"
    "v_exp_f32 v68, v68\n"
    "v_mfma_f32_16x16x32_bf16 a[12:15], a[164:167], v[104:107], a[12:15]\n"
    "v_exp_f32 v69, v69\n"
    "v_mfma_f32_16x16x32_bf16 a[28:31], a[164:167], v[120:123], a[28:31]\n"
    "ds_read_b128 a[132:135], v12 offset:8192\n"
    "v_mfma_f32_16x16x32_bf16 a[32:35], a[168:171], v[96:99], a[32:35]\n"
    "v_exp_f32 v70, v70\n"
    "v_mfma_f32_16x16x32_bf16 a[48:51], a[168:171], v[112:115], a[48:51]\n"
    "v_exp_f32 v71, v71\n"
    "v_mfma_f32_16x16x32_bf16 a[40:43], a[168:171], v[104:107], a[40:43]\n"
    "ds_read_b128 a[136:139], v13 offset:8192\n"
    "v_cvt_pk_bf16_f32 v66, v68, v69\n"
    "v_mfma_f32_16x16x32_bf16 a[56:59], a[168:171], v[120:123], a[56:59]\n"
    "v_cvt_pk_bf16_f32 v67, v70, v71\n"
    "v_mfma_f32_16x16x32_bf16 a[36:39], a[172:175], v[96:99], a[36:39]\n"
    "s_add_u32 m0, s91, 0x10000\n"
    "v_exp_f32 v80, v80\n"
    "v_mfma_f32_16x16x32_bf16 a[52:55], a[172:175], v[112:115], a[52:55]\n"
    "ds_read_b128 a[140:143], v14 offset:8192\n"
    "s_add_u32 s90, s91, 0xc000\n"
    "v_mfma_f32_16x16x32_bf16 a[44:47], a[172:175], v[104:107], a[44:47]\n"
    "buffer_load_dwordx4 v19, s[56:59], s90 offen lds\n"
    "s_lshr_b32 s37, s32, 1\n"
    "v_mfma_f32_16x16x32_bf16 a[60:63], a[172:175], v[120:123], a[60:63]\n"
    "s_lshl_b32 s37, s37, 9\n"
    "v_mfma_f32_16x16x32_bf16 a[64:67], a[176:179], v[96:99], a[64:67]\n"
    "v_add_u32 v40, s37, v3\n"
    "v_mfma_f32_16x16x32_bf16 a[80:83], a[176:179], v[112:115], a[80:83]\n"
    "v_add_u32 v41, s37, v4\n"
    "v_mfma_f32_16x16x32_bf16 a[72:75], a[176:179], v[104:107], a[72:75]\n"
    "buffer_load_dwordx4 v20, s[56:59], s90 offen offset:1024 lds\n"
    "s_xor_b32 s37, s37, 0x200\n"
    "v_mfma_f32_16x16x32_bf16 a[88:91], a[176:179], v[120:123], a[88:91]\n"
    "v_add_u32 v42, s37, v3\n"
    "s_waitcnt lgkmcnt(0)\n"
    "v_mfma_f32_16x16x32_bf16 a[68:71], a[180:183], v[96:99], a[68:71]\n"
    "v_add_u32 v43, s37, v4\n"
    "s_and_b32 s37, s32, 1\n"
    "v_mfma_f32_16x16x32_bf16 a[84:87], a[180:183], v[112:115], a[84:87]\n"
    "s_lshl_b32 s37, s37, 10\n"
    "v_mfma_f32_16x16x32_bf16 a[76:79], a[180:183], v[104:107], a[76:79]\n"
    "buffer_load_dwordx4 v21, s[56:59], s90 offen offset:2048 lds\n"
    "v_mfma_f32_16x16x32_bf16 a[92:95], a[180:183], v[120:123], a[92:95]\n"
    "v_add_u32 v56, s37, v2\n"
    "s_xor_b32 s37, s37, 0x400\n"
    "v_mfma_f32_16x16x32_bf16 a[96:99], a[184:187], v[96:99], a[96:99]\n"
    "v_add_u32 v57, s37, v2\n"
    "v_mfma_f32_16x16x32_bf16 a[112:115], a[184:187], v[112:115], a[112:115]\n"
    "s_mul_i32 s96, s35, 0x38000\n"
    "v_mfma_f32_16x16x32_bf16 a[104:107], a[184:187], v[104:107], a[104:107]\n"
    "buffer_load_dwordx4 v22, s[56:59], s90 offen offset:3072 lds\n"
    "s_mul_i32 s37, s32, 0xe000\n"
    "v_mfma_f32_16x16x32_bf16 a[120:123], a[184:187], v[120:123], a[120:123]\n"
    "s_add_u32 s96, s96, s37\n"
    "v_mfma_f32_16x16x32_bf16 a[100:103], a[188:191], v[96:99], a[100:103]\n"
    "v_exp_f32 v81, v81\n"
    "v_mfma_f32_16x16x32_bf16 a[116:119], a[188:191], v[112:115], a[116:119]\n"
    "v_exp_f32 v82, v82\n"
    "v_mfma_f32_16x16x32_bf16 a[108:111], a[188:191], v[104:107], a[108:111]\n"
    "v_exp_f32 v83, v83\n"
    "v_mfma_f32_16x16x32_bf16 a[124:127], a[188:191], v[120:123], a[124:127]\n"
    "v_cvt_pk_bf16_f32 v80, v80, v81\n"
    "v_cvt_pk_bf16_f32 v81, v82, v83\n"
    "v_mfma_f32_16x16x32_bf16 v[96:99], a[128:131], v[128:131], 0\n"
    "v_exp_f32 v84, v84\n"
    "v_mfma_f32_16x16x32_bf16 v[112:115], a[128:131], v[144:147], 0\n"
    "ds_read_b64_tr_b16 a[160:161], v9\n"
    "v_mfma_f32_16x16x32_bf16 v[104:107], a[128:131], v[136:139], 0\n"
    "s_add_u32 m0, s89, 0x1000\n"
    "v_exp_f32 v85, v85\n"
    "v_mfma_f32_16x16x32_bf16 v[120:123], a[128:131], v[152:155], 0\n"
    "ds_read_b64_tr_b16 a[162:163], v9 offset:4096\n"
    "v_mfma_f32_16x16x32_bf16 v[96:99], a[132:135], v[160:163], v[96:99]\n"
    "ds_read_b128 a[128:131], v11 offset:12288\n"
    "buffer_load_dwordx4 v29, s[68:71], s94 offen lds\n"
    "v_mfma_f32_16x16x32_bf16 v[112:115], a[132:135], v[176:179], v[112:115]\n"
    "ds_read_b64_tr_b16 a[164:165], v10\n"
    "v_mfma_f32_16x16x32_bf16 v[104:107], a[132:135], v[168:171], v[104:107]\n"
    "v_exp_f32 v86, v86\n"
    "v_mfma_f32_16x16x32_bf16 v[120:123], a[132:135], v[184:187], v[120:123]\n"
    "ds_read_b64_tr_b16 a[166:167], v10 offset:4096\n"
    "v_cvt_pk_bf16_f32 v82, v84, v85\n"
    "v_mfma_f32_16x16x32_bf16 v[96:99], a[136:139], v[192:195], v[96:99]\n"
    "ds_read_b128 a[132:135], v12 offset:12288\n"
    "v_mfma_f32_16x16x32_bf16 v[112:115], a[136:139], v[208:211], v[112:115]\n"
    "ds_read_b64_tr_b16 a[168:169], v9 offset:512\n"
    "v_mfma_f32_16x16x32_bf16 v[104:107], a[136:139], v[200:203], v[104:107]\n"
    "buffer_load_dwordx4 v30, s[68:71], s94 offen offset:1024 lds\n"
    "v_mfma_f32_16x16x32_bf16 v[120:123], a[136:139], v[216:219], v[120:123]\n"
    "ds_read_b64_tr_b16 a[170:171], v9 offset:4608\n"
    "v_mfma_f32_16x16x32_bf16 v[96:99], a[140:143], v[224:227], v[96:99]\n"
    "ds_read_b128 a[136:139], v13 offset:12288\n"
    "v_mfma_f32_16x16x32_bf16 v[112:115], a[140:143], v[240:243], v[112:115]\n"
    "ds_read_b64_tr_b16 a[172:173], v10 offset:512\n"
    "v_mfma_f32_16x16x32_bf16 v[104:107], a[140:143], v[232:235], v[104:107]\n"
    "v_exp_f32 v87, v87\n"
    "v_mfma_f32_16x16x32_bf16 v[120:123], a[140:143], v[248:251], v[120:123]\n"
    "ds_read_b64_tr_b16 a[174:175], v10 offset:4608\n"
    "s_waitcnt lgkmcnt(5)\n"
    "v_mfma_f32_16x16x32_bf16 v[100:103], a[128:131], v[128:131], 0\n"
    "ds_read_b128 a[140:143], v14 offset:12288\n"
    "buffer_load_dwordx4 v31, s[68:71], s94 offen offset:2048 lds\n"
    "v_mfma_f32_16x16x32_bf16 v[116:119], a[128:131], v[144:147], 0\n"
    "ds_read_b64_tr_b16 a[176:177], v9 offset:1024\n"
    "v_mfma_f32_16x16x32_bf16 v[108:111], a[128:131], v[136:139], 0\n"
    "v_exp_f32 v72, v72\n"
    "v_mfma_f32_16x16x32_bf16 v[124:127], a[128:131], v[152:155], 0\n"
    "ds_read_b64_tr_b16 a[178:179], v9 offset:5120\n"
    "v_cvt_pk_bf16_f32 v83, v86, v87\n"
    "v_mfma_f32_16x16x32_bf16 v[100:103], a[132:135], v[160:163], v[100:103]\n"
    "v_exp_f32 v73, v73\n"
    "v_mfma_f32_16x16x32_bf16 v[116:119], a[132:135], v[176:179], v[116:119]\n"
    "ds_read_b64_tr_b16 a[180:181], v10 offset:1024\n"
    "v_mfma_f32_16x16x32_bf16 v[108:111], a[132:135], v[168:171], v[108:111]\n"
    "buffer_load_dwordx4 v32, s[68:71], s94 offen offset:3072 lds\n"
    "v_mfma_f32_16x16x32_bf16 v[124:127], a[132:135], v[184:187], v[124:127]\n"
    "ds_read_b64_tr_b16 a[182:183], v10 offset:5120\n"
    "s_waitcnt lgkmcnt(3)\n"
    "v_mfma_f32_16x16x32_bf16 v[100:103], a[136:139], v[192:195], v[100:103]\n"
    "s_add_u32 s94, s94, 0x8000\n"
    "v_exp_f32 v74, v74\n"
    "v_mfma_f32_16x16x32_bf16 v[116:119], a[136:139], v[208:211], v[116:119]\n"
    "ds_read_b64_tr_b16 a[184:185], v9 offset:1536\n"
    "v_cvt_pk_bf16_f32 v72, v72, v73\n"
    "v_mfma_f32_16x16x32_bf16 v[108:111], a[136:139], v[200:203], v[108:111]\n"
    "v_exp_f32 v75, v75\n"
    "v_mfma_f32_16x16x32_bf16 v[124:127], a[136:139], v[216:219], v[124:127]\n"
    "ds_read_b64_tr_b16 a[186:187], v9 offset:5632\n"
    "v_mfma_f32_16x16x32_bf16 v[100:103], a[140:143], v[224:227], v[100:103]\n"
    "v_exp_f32 v76, v76\n"
    "v_mfma_f32_16x16x32_bf16 v[116:119], a[140:143], v[240:243], v[116:119]\n"
    "ds_read_b64_tr_b16 a[188:189], v10 offset:1536\n"
    "v_cmp_gt_u32 vcc, 8, v1\n"
    "v_lshlrev_b32 v52, 3, v1\n"
    "v_add_u32 v52, 64, v52\n"
    "v_cndmask_b32 v52, v0, v52\n"
    "v_mfma_f32_16x16x32_bf16 v[108:111], a[140:143], v[232:235], v[108:111]\n"
    "s_lshl_b32 s39, s35, 8\n"
    "v_cvt_pk_bf16_f32 v73, v74, v75\n"
    "v_mfma_f32_16x16x32_bf16 v[124:127], a[140:143], v[248:251], v[124:127]\n"
    "ds_read_b64_tr_b16 a[190:191], v10 offset:5632\n"
    "buffer_load_dwordx2 v[48:49], v52, s[84:87], s39 offen sc1\n"
    "v_exp_f32 v77, v77\n"
    "v_exp_f32 v78, v78\n"
    "v_exp_f32 v79, v79\n"
    "v_cvt_pk_bf16_f32 v74, v76, v77\n"
    "v_cvt_pk_bf16_f32 v75, v78, v79\n"
    "v_exp_f32 v88, v88\n"
    "v_exp_f32 v89, v89\n"
    "v_exp_f32 v90, v90\n"
    "v_exp_f32 v91, v91\n"
    "v_cvt_pk_bf16_f32 v88, v88, v89\n"
    "v_exp_f32 v92, v92\n"
    "v_cvt_pk_bf16_f32 v89, v90, v91\n"
    "v_exp_f32 v93, v93\n"
    "v_exp_f32 v94, v94\n"
    "v_exp_f32 v95, v95\n"
    "v_cvt_pk_bf16_f32 v90, v92, v93\n"
    "v_cvt_pk_bf16_f32 v91, v94, v95\n"
    "v_mfma_f32_16x16x32_bf16 a[144:147], v[34:37], v[64:67], a[144:147]\n"
    "v_mfma_f32_16x16x32_bf16 a[152:155], v[34:37], v[80:83], a[152:155]\n"
    "v_mfma_f32_16x16x32_bf16 a[148:151], v[34:37], v[72:75], a[148:151]\n"
    "v_mfma_f32_16x16x32_bf16 a[156:159], v[34:37], v[88:91], a[156:159]\n"
    "v_exp_f32 v96, v96\n"
    "s_waitcnt vmcnt(13)\n"
    "s_barrier\n"
    "v_mfma_f32_16x16x32_bf16 a[0:3], a[160:163], v[64:67], a[0:3]\n"
    "v_mfma_f32_16x16x32_bf16 a[16:19], a[160:163], v[80:83], a[16:19]\n"
    "v_exp_f32 v97, v97\n"
    "v_mfma_f32_16x16x32_bf16 a[8:11], a[160:163], v[72:75], a[8:11]\n"
    "v_exp_f32 v98, v98\n"
    "v_mfma_f32_16x16x32_bf16 a[24:27], a[160:163], v[88:91], a[24:27]\n"
    "v_exp_f32 v99, v99\n"
    "v_mfma_f32_16x16x32_bf16 a[4:7], a[164:167], v[64:67], a[4:7]\n"
    "ds_read_b128 a[128:131], v11 offset:16384\n"
    "v_cvt_pk_bf16_f32 v96, v96, v97\n"
    "v_mfma_f32_16x16x32_bf16 a[20:23], a[164:167], v[80:83], a[20:23]\n"
    "v_exp_f32 v100, v100\n"
    "v_mfma_f32_16x16x32_bf16 a[12:15], a[164:167], v[72:75], a[12:15]\n"
    "v_cvt_pk_bf16_f32 v97, v98, v99\n"
    "v_mfma_f32_16x16x32_bf16 a[28:31], a[164:167], v[88:91], a[28:31]\n"
    "ds_read_b128 a[132:135], v12 offset:16384\n"
    "v_mfma_f32_16x16x32_bf16 a[32:35], a[168:171], v[64:67], a[32:35]\n"
    "v_exp_f32 v101, v101\n"
    "v_mfma_f32_16x16x32_bf16 a[48:51], a[168:171], v[80:83], a[48:51]\n"
    "v_exp_f32 v102, v102\n"
    "v_mfma_f32_16x16x32_bf16 a[40:43], a[168:171], v[72:75], a[40:43]\n"
    "ds_read_b128 a[136:139], v13 offset:16384\n"
    "v_cvt_pk_bf16_f32 v98, v100, v101\n"
    "v_mfma_f32_16x16x32_bf16 a[56:59], a[168:171], v[88:91], a[56:59]\n"
    "v_exp_f32 v103, v103\n"
    "v_mfma_f32_16x16x32_bf16 a[36:39], a[172:175], v[64:67], a[36:39]\n"
    "s_add_u32 m0, s91, 0x14000\n"
    "v_exp_f32 v112, v112\n"
    "v_mfma_f32_16x16x32_bf16 a[52:55], a[172:175], v[80:83], a[52:55]\n"
    "ds_read_b128 a[140:143], v14 offset:16384\n"
    "s_add_u32 s90, s91, 0xc000\n"
    "v_mfma_f32_16x16x32_bf16 a[44:47], a[172:175], v[72:75], a[44:47]\n"
    "buffer_load_dwordx4 v23, s[60:63], s90 offen lds\n"
    "v_cvt_pk_bf16_f32 v99, v102, v103\n"
    "v_mfma_f32_16x16x32_bf16 a[60:63], a[172:175], v[88:91], a[60:63]\n"
    "v_exp_f32 v113, v113\n"
    "s_waitcnt lgkmcnt(1)\n"
    "v_mfma_f32_16x16x32_bf16 a[64:67], a[176:179], v[64:67], a[64:67]\n"
    "v_exp_f32 v114, v114\n"
    "v_mfma_f32_16x16x32_bf16 a[80:83], a[176:179], v[80:83], a[80:83]\n"
    "v_exp_f32 v115, v115\n"
    "v_mfma_f32_16x16x32_bf16 a[72:75], a[176:179], v[72:75], a[72:75]\n"
    "buffer_load_dwordx4 v24, s[60:63], s90 offen offset:1024 lds\n"
    "v_cvt_pk_bf16_f32 v112, v112, v113\n"
    "v_mfma_f32_16x16x32_bf16 a[88:91], a[176:179], v[88:91], a[88:91]\n"
    "v_exp_f32 v116, v116\n"
    "v_mfma_f32_16x16x32_bf16 a[68:71], a[180:183], v[64:67], a[68:71]\n"
    "v_cvt_pk_bf16_f32 v113, v114, v115\n"
    "v_mfma_f32_16x16x32_bf16 a[84:87], a[180:183], v[80:83], a[84:87]\n"
    "v_exp_f32 v117, v117\n"
    "v_mfma_f32_16x16x32_bf16 a[76:79], a[180:183], v[72:75], a[76:79]\n"
    "buffer_load_dwordx4 v23, s[60:63], s90 offen offset:2048 lds\n"
    "v_mfma_f32_16x16x32_bf16 a[92:95], a[180:183], v[88:91], a[92:95]\n"
    "v_exp_f32 v118, v118\n"
    "v_mfma_f32_16x16x32_bf16 a[96:99], a[184:187], v[64:67], a[96:99]\n"
    "v_exp_f32 v119, v119\n"
    "v_mfma_f32_16x16x32_bf16 a[112:115], a[184:187], v[80:83], a[112:115]\n"
    "v_cvt_pk_bf16_f32 v114, v116, v117\n"
    "v_cvt_pk_bf16_f32 v115, v118, v119\n"
    "v_mfma_f32_16x16x32_bf16 a[104:107], a[184:187], v[72:75], a[104:107]\n"
    "buffer_load_dwordx4 v24, s[60:63], s90 offen offset:3072 lds\n"
    "v_mfma_f32_16x16x32_bf16 a[120:123], a[184:187], v[88:91], a[120:123]\n"
    "v_exp_f32 v104, v104\n"
    "v_mfma_f32_16x16x32_bf16 a[100:103], a[188:191], v[64:67], a[100:103]\n"
    "v_exp_f32 v105, v105\n"
    "v_mfma_f32_16x16x32_bf16 a[116:119], a[188:191], v[80:83], a[116:119]\n"
    "v_exp_f32 v106, v106\n"
    "v_mfma_f32_16x16x32_bf16 a[108:111], a[188:191], v[72:75], a[108:111]\n"
    "v_exp_f32 v107, v107\n"
    "v_mfma_f32_16x16x32_bf16 a[124:127], a[188:191], v[88:91], a[124:127]\n"
    "v_cvt_pk_bf16_f32 v104, v104, v105\n"
    "v_cvt_pk_bf16_f32 v105, v106, v107\n"
    "v_mfma_f32_16x16x32_bf16 v[64:67], a[128:131], v[128:131], 0\n"
    "v_exp_f32 v108, v108\n"
    "v_mfma_f32_16x16x32_bf16 v[80:83], a[128:131], v[144:147], 0\n"
    "ds_read_b64_tr_b16 a[160:161], v9 offset:8192\n"
    "v_mfma_f32_16x16x32_bf16 v[72:75], a[128:131], v[136:139], 0\n"
    "s_add_u32 m0, s89, 0x2000\n"
    "v_exp_f32 v109, v109\n"
    "v_mfma_f32_16x16x32_bf16 v[88:91], a[128:131], v[152:155], 0\n"
    "ds_read_b64_tr_b16 a[162:163], v9 offset:12288\n"
    "v_mfma_f32_16x16x32_bf16 v[64:67], a[132:135], v[160:163], v[64:67]\n"
    "ds_read_b128 a[128:131], v11 offset:20480\n"
    "buffer_load_dwordx4 v29, s[68:71], s94 offen lds\n"
    "v_mfma_f32_16x16x32_bf16 v[80:83], a[132:135], v[176:179], v[80:83]\n"
    "ds_read_b64_tr_b16 a[164:165], v10 offset:8192\n"
    "v_mfma_f32_16x16x32_bf16 v[72:75], a[132:135], v[168:171], v[72:75]\n"
    "v_exp_f32 v110, v110\n"
    "v_mfma_f32_16x16x32_bf16 v[88:91], a[132:135], v[184:187], v[88:91]\n"
    "ds_read_b64_tr_b16 a[166:167], v10 offset:12288\n"
    "v_cvt_pk_bf16_f32 v106, v108, v109\n"
    "v_mfma_f32_16x16x32_bf16 v[64:67], a[136:139], v[192:195], v[64:67]\n"
    "ds_read_b128 a[132:135], v12 offset:20480\n"
    "v_mfma_f32_16x16x32_bf16 v[80:83], a[136:139], v[208:211], v[80:83]\n"
    "ds_read_b64_tr_b16 a[168:169], v9 offset:8704\n"
    "v_mfma_f32_16x16x32_bf16 v[72:75], a[136:139], v[200:203], v[72:75]\n"
    "buffer_load_dwordx4 v30, s[68:71], s94 offen offset:1024 lds\n"
    "v_mfma_f32_16x16x32_bf16 v[88:91], a[136:139], v[216:219], v[88:91]\n"
    "ds_read_b64_tr_b16 a[170:171], v9 offset:12800\n"
    "s_waitcnt lgkmcnt(4)\n"
    "v_mfma_f32_16x16x32_bf16 v[64:67], a[140:143], v[224:227], v[64:67]\n"
    "ds_read_b128 a[136:139], v13 offset:20480\n"
    "v_mfma_f32_16x16x32_bf16 v[80:83], a[140:143], v[240:243], v[80:83]\n"
    "ds_read_b64_tr_b16 a[172:173], v10 offset:8704\n"
    "v_mfma_f32_16x16x32_bf16 v[72:75], a[140:143], v[232:235], v[72:75]\n"
    "v_exp_f32 v111, v111\n"
    "v_mfma_f32_16x16x32_bf16 v[88:91], a[140:143], v[248:251], v[88:91]\n"
    "ds_read_b64_tr_b16 a[174:175], v10 offset:12800\n"
    "v_mfma_f32_16x16x32_bf16 v[68:71], a[128:131], v[128:131], 0\n"
    "ds_read_b128 a[140:143], v14 offset:20480\n"
    "buffer_load_dwordx4 v31, s[68:71], s94 offen offset:2048 lds\n"
    "v_mfma_f32_16x16x32_bf16 v[84:87], a[128:131], v[144:147], 0\n"
    "ds_read_b64_tr_b16 a[176:177], v9 offset:9216\n"
    "v_mfma_f32_16x16x32_bf16 v[76:79], a[128:131], v[136:139], 0\n"
    "v_exp_f32 v120, v120\n"
    "v_mfma_f32_16x16x32_bf16 v[92:95], a[128:131], v[152:155], 0\n"
    "ds_read_b64_tr_b16 a[178:179], v9 offset:13312\n"
    "v_cvt_pk_bf16_f32 v107, v110, v111\n"
    "s_waitcnt lgkmcnt(4)\n"
    "v_mfma_f32_16x16x32_bf16 v[68:71], a[132:135], v[160:163], v[68:71]\n"
    "v_exp_f32 v121, v121\n"
    "v_mfma_f32_16x16x32_bf16 v[84:87], a[132:135], v[176:179], v[84:87]\n"
    "ds_read_b64_tr_b16 a[180:181], v10 offset:9216\n"
    "v_mfma_f32_16x16x32_bf16 v[76:79], a[132:135], v[168:171], v[76:79]\n"
    "buffer_load_dwordx4 v32, s[68:71], s94 offen offset:3072 lds\n"
    "v_mfma_f32_16x16x32_bf16 v[92:95], a[132:135], v[184:187], v[92:95]\n"
    "ds_read_b64_tr_b16 a[182:183], v10 offset:13312\n"
    "v_mfma_f32_16x16x32_bf16 v[68:71], a[136:139], v[192:195], v[68:71]\n"
    "s_add_u32 s94, s94, 0x8000\n"
    "v_exp_f32 v122, v122\n"
    "v_mfma_f32_16x16x32_bf16 v[84:87], a[136:139], v[208:211], v[84:87]\n"
    "ds_read_b64_tr_b16 a[184:185], v9 offset:9728\n"
    "v_cvt_pk_bf16_f32 v120, v120, v121\n"
    "v_mfma_f32_16x16x32_bf16 v[76:79], a[136:139], v[200:203], v[76:79]\n"
    "s_waitcnt vmcnt(8)\n"
    "v_readlane_b32 s98, v48, 8\n"
    "v_mfma_f32_16x16x32_bf16 v[92:95], a[136:139], v[216:219], v[92:95]\n"
    "ds_read_b64_tr_b16 a[186:187], v9 offset:13824\n"
    "v_readlane_b32 s99, v49, 8\n"
    "s_waitcnt lgkmcnt(3)\n"
    "v_mfma_f32_16x16x32_bf16 v[68:71], a[140:143], v[224:227], v[68:71]\n"
    "v_exp_f32 v123, v123\n"
    "v_mfma_f32_16x16x32_bf16 v[84:87], a[140:143], v[240:243], v[84:87]\n"
    "ds_read_b64_tr_b16 a[188:189], v10 offset:9728\n"
    "v_mfma_f32_16x16x32_bf16 v[76:79], a[140:143], v[232:235], v[76:79]\n"
    "s_add_u32 s98, s98, 0x7f4a7c14\n"
    "s_addc_u32 s99, s99, 0x9e3779b9\n"
    "v_mfma_f32_16x16x32_bf16 v[92:95], a[140:143], v[248:251], v[92:95]\n"
    "ds_read_b64_tr_b16 a[190:191], v10 offset:13824\n"
    "v_mov_b32 v51, s99\n"
    "v_exp_f32 v124, v124\n"
    "v_cvt_pk_bf16_f32 v121, v122, v123\n"
    "v_exp_f32 v125, v125\n"
    "v_exp_f32 v126, v126\n"
    "v_exp_f32 v127, v127\n"
    "v_cvt_pk_bf16_f32 v122, v124, v125\n"
    "v_cvt_pk_bf16_f32 v123, v126, v127\n"
    "v_mfma_f32_16x16x32_bf16 a[144:147], v[34:37], v[96:99], a[144:147]\n"
    "v_mfma_f32_16x16x32_bf16 a[152:155], v[34:37], v[112:115], a[152:155]\n"
    "v_sub_co_u32 v50, vcc, s98, v48\n"
    "s_nop 1\n"
    "v_subb_co_u32 v51, vcc, v51, v49, vcc\n"
    "v_mfma_f32_16x16x32_bf16 a[148:151], v[34:37], v[104:107], a[148:151]\n"
    "v_and_b32 v50, 0xfff8fff8, v50\n"
    "v_mfma_f32_16x16x32_bf16 a[156:159], v[34:37], v[120:123], a[156:159]\n"
    "v_or_b32 v50, v50, v51\n"
    "v_mfma_f32_16x16x32_bf16 a[0:3], a[160:163], v[96:99], a[0:3]\n"
    "v_exp_f32 v64, v64\n"
    "v_mfma_f32_16x16x32_bf16 a[16:19], a[160:163], v[112:115], a[16:19]\n"
    "v_cmp_ne_u32 vcc, 0, v50\n"
    "s_and_b32 s39, vcc_lo, 0xff\n"
    "v_mfma_f32_16x16x32_bf16 a[8:11], a[160:163], v[104:107], a[8:11]\n"
    "v_exp_f32 v65, v65\n"
    "v_mfma_f32_16x16x32_bf16 a[24:27], a[160:163], v[120:123], a[24:27]\n"
    "s_cmp_lg_u32 s39, 0\n"
    "s_cbranch_scc1 SLOW0_%=\n"
    "BACK0_%=:\n"
    "v_mfma_f32_16x16x32_bf16 a[4:7], a[164:167], v[96:99], a[4:7]\n"
    "ds_read_b128 a[128:131], v11 offset:24576\n"
    "v_mfma_f32_16x16x32_bf16 a[20:23], a[164:167], v[112:115], a[20:23]\n"
    "v_exp_f32 v66, v66\n"
    "v_mfma_f32_16x16x32_bf16 a[12:15], a[164:167], v[104:107], a[12:15]\n"
    "v_exp_f32 v67, v67\n"
    "v_mfma_f32_16x16x32_bf16 a[28:31], a[164:167], v[120:123], a[28:31]\n"
    "ds_read_b128 a[132:135], v12 offset:24576\n"
    "v_cvt_pk_bf16_f32 v64, v64, v65\n"
    "v_mfma_f32_16x16x32_bf16 a[32:35], a[168:171], v[96:99], a[32:35]\n"
    "v_exp_f32 v68, v68\n"
    "v_mfma_f32_16x16x32_bf16 a[48:51], a[168:171], v[112:115], a[48:51]\n"
    "v_cvt_pk_bf16_f32 v65, v66, v67\n"
    "v_mfma_f32_16x16x32_bf16 a[40:43], a[168:171], v[104:107], a[40:43]\n"
    "ds_read_b128 a[136:139], v13 offset:24576\n"
    "v_mfma_f32_16x16x32_bf16 a[56:59], a[168:171], v[120:123], a[56:59]\n"
    "v_exp_f32 v69, v69\n"
    "v_mfma_f32_16x16x32_bf16 a[36:39], a[172:175], v[96:99], a[36:39]\n"
    "s_add_u32 m0, s91, 0x8000\n"
    "v_exp_f32 v70, v70\n"
    "v_mfma_f32_16x16x32_bf16 a[52:55], a[172:175], v[112:115], a[52:55]\n"
    "ds_read_b128 a[140:143], v14 offset:24576\n"
    "s_add_u32 s90, s91, 0x10000\n"
    "v_mfma_f32_16x16x32_bf16 a[44:47], a[172:175], v[104:107], a[44:47]\n"
    "buffer_load_dwordx4 v19, s[56:59], s90 offen lds\n"
    "v_cvt_pk_bf16_f32 v66, v68, v69\n"
    "v_mfma_f32_16x16x32_bf16 a[60:63], a[172:175], v[120:123], a[60:63]\n"
    "v_exp_f32 v71, v71\n"
    "v_mfma_f32_16x16x32_bf16 a[64:67], a[176:179], v[96:99], a[64:67]\n"
    "v_exp_f32 v80, v80\n"
    "v_mfma_f32_16x16x32_bf16 a[80:83], a[176:179], v[112:115], a[80:83]\n"
    "v_cvt_pk_bf16_f32 v67, v70, v71\n"
    "v_mfma_f32_16x16x32_bf16 a[72:75], a[176:179], v[104:107], a[72:75]\n"
    "buffer_load_dwordx4 v20, s[56:59], s90 offen offset:1024 lds\n"
    "v_mfma_f32_16x16x32_bf16 a[88:91], a[176:179], v[120:123], a[88:91]\n"
    "v_exp_f32 v81, v81\n"
    "s_waitcnt lgkmcnt(0)\n"
    "v_mfma_f32_16x16x32_bf16 a[68:71], a[180:183], v[96:99], a[68:71]\n"
    "buffer_load_dwordx4 v[196:199], v56, s[72:75], s96 offen sc1\n"
    "v_mfma_f32_16x16x32_bf16 a[84:87], a[180:183], v[112:115], a[84:87]\n"
    "v_exp_f32 v82, v82\n"
    "v_mfma_f32_16x16x32_bf16 a[76:79], a[180:183], v[104:107], a[76:79]\n"
    "buffer_load_dwordx4 v21, s[56:59], s90 offen offset:2048 lds\n"
    "v_cvt_pk_bf16_f32 v80, v80, v81\n"
    "v_mfma_f32_16x16x32_bf16 a[92:95], a[180:183], v[120:123], a[92:95]\n"
    "v_exp_f32 v83, v83\n"
    "v_mfma_f32_16x16x32_bf16 a[96:99], a[184:187], v[96:99], a[96:99]\n"
    "v_exp_f32 v84, v84\n"
    "v_mfma_f32_16x16x32_bf16 a[112:115], a[184:187], v[112:115], a[112:115]\n"
    "v_cvt_pk_bf16_f32 v81, v82, v83\n"
    "v_mfma_f32_16x16x32_bf16 a[104:107], a[184:187], v[104:107], a[104:107]\n"
    "buffer_load_dwordx4 v22, s[56:59], s90 offen offset:3072 lds\n"
    "buffer_load_dwordx4 v[204:207], v57, s[72:75], s96 offen sc1\n"
    "v_mfma_f32_16x16x32_bf16 a[120:123], a[184:187], v[120:123], a[120:123]\n"
    "v_exp_f32 v85, v85\n"
    "v_mfma_f32_16x16x32_bf16 a[100:103], a[188:191], v[96:99], a[100:103]\n"
    "v_exp_f32 v86, v86\n"
    "v_mfma_f32_16x16x32_bf16 a[116:119], a[188:191], v[112:115], a[116:119]\n"
    "v_exp_f32 v87, v87\n"
    "v_mfma_f32_16x16x32_bf16 a[108:111], a[188:191], v[104:107], a[108:111]\n"
    "v_cvt_pk_bf16_f32 v82, v84, v85\n"
    "v_cvt_pk_bf16_f32 v83, v86, v87\n"
    "v_mfma_f32_16x16x32_bf16 a[124:127], a[188:191], v[120:123], a[124:127]\n"
    "v_exp_f32 v72, v72\n"
    "v_mfma_f32_16x16x32_bf16 v[96:99], a[128:131], v[128:131], 0\n"
    "v_exp_f32 v73, v73\n"
    "v_mfma_f32_16x16x32_bf16 v[112:115], a[128:131], v[144:147], 0\n"
    "ds_read_b64_tr_b16 a[160:161], v9 offset:16384\n"
    "v_mfma_f32_16x16x32_bf16 v[104:107], a[128:131], v[136:139], 0\n"
    "s_add_u32 m0, s89, 0x3000\n"
    "v_exp_f32 v74, v74\n"
    "v_mfma_f32_16x16x32_bf16 v[120:123], a[128:131], v[152:155], 0\n"
    "ds_read_b64_tr_b16 a[162:163], v9 offset:20480\n"
    "v_cvt_pk_bf16_f32 v72, v72, v73\n"
    "v_mfma_f32_16x16x32_bf16 v[96:99], a[132:135], v[160:163], v[96:99]\n"
    "ds_read_b128 a[128:131], v11 offset:28672\n"
    "buffer_load_dwordx4 v29, s[68:71], s94 offen lds\n"
    "v_mfma_f32_16x16x32_bf16 v[112:115], a[132:135], v[176:179], v[112:115]\n"
    "ds_read_b64_tr_b16 a[164:165], v10 offset:16384\n"
    "v_mfma_f32_16x16x32_bf16 v[104:107], a[132:135], v[168:171], v[104:107]\n"
    "v_exp_f32 v75, v75\n"
    "v_mfma_f32_16x16x32_bf16 v[120:123], a[132:135], v[184:187], v[120:123]\n"
    "ds_read_b64_tr_b16 a[166:167], v10 offset:20480\n"
    "v_mfma_f32_16x16x32_bf16 v[96:99], a[136:139], v[192:195], v[96:99]\n"
    "ds_read_b128 a[132:135], v12 offset:28672\n"
    "v_mfma_f32_16x16x32_bf16 v[112:115], a[136:139], v[208:211], v[112:115]\n"
    "ds_read_b64_tr_b16 a[168:169], v9 offset:16896\n"
    "v_mfma_f32_16x16x32_bf16 v[104:107], a[136:139], v[200:203], v[104:107]\n"
    "buffer_load_dwordx4 v30, s[68:71], s94 offen offset:1024 lds\n"
    "buffer_load_dwordx4 v[212:215], v56, s[72:75], s96 offen offset:2048 sc1\n"
    "v_mfma_f32_16x16x32_bf16 v[120:123], a[136:139], v[216:219], v[120:123]\n"
    "ds_read_b64_tr_b16 a[170:171], v9 offset:20992\n"
    "v_mfma_f32_16x16x32_bf16 v[96:99], a[140:143], v[224:227], v[96:99]\n"
    "ds_read_b128 a[136:139], v13 offset:28672\n"
    "v_mfma_f32_16x16x32_bf16 v[112:115], a[140:143], v[240:243], v[112:115]\n"
    "ds_read_b64_tr_b16 a[172:173], v10 offset:16896\n"
    "v_mfma_f32_16x16x32_bf16 v[104:107], a[140:143], v[232:235], v[104:107]\n"
    "v_exp_f32 v76, v76\n"
    "v_mfma_f32_16x16x32_bf16 v[120:123], a[140:143], v[248:251], v[120:123]\n"
    "ds_read_b64_tr_b16 a[174:175], v10 offset:20992\n"
    "v_cvt_pk_bf16_f32 v73, v74, v75\n"
    "s_waitcnt lgkmcnt(4)\n"
    "v_mfma_f32_16x16x32_bf16 v[100:103], a[128:131], v[128:131], 0\n"
    "ds_read_b128 a[140:143], v14 offset:28672\n"
    "buffer_load_dwordx4 v31, s[68:71], s94 offen offset:2048 lds\n"
    "buffer_load_dwordx4 v[220:223], v57, s[72:75], s96 offen offset:2048 sc1\n"
    "s_add_u32 s96, s96, 0x1000\n"
    "v_mfma_f32_16x16x32_bf16 v[116:119], a[128:131], v[144:147], 0\n"
    "ds_read_b64_tr_b16 a[176:177], v9 offset:17408\n"
    "v_mfma_f32_16x16x32_bf16 v[108:111], a[128:131], v[136:139], 0\n"
    "v_exp_f32 v77, v77\n"
    "v_mfma_f32_16x16x32_bf16 v[124:127], a[128:131], v[152:155], 0\n"
    "ds_read_b64_tr_b16 a[178:179], v9 offset:21504\n"
    "v_mfma_f32_16x16x32_bf16 v[100:103], a[132:135], v[160:163], v[100:103]\n"
    "v_exp_f32 v78, v78\n"
    "v_mfma_f32_16x16x32_bf16 v[116:119], a[132:135], v[176:179], v[116:119]\n"
    "ds_read_b64_tr_b16 a[180:181], v10 offset:17408\n"
    "v_cvt_pk_bf16_f32 v74, v76, v77\n"
    "v_mfma_f32_16x16x32_bf16 v[108:111], a[132:135], v[168:171], v[108:111]\n"
    "buffer_load_dwordx4 v32, s[68:71], s94 offen offset:3072 lds\n"
    "v_mfma_f32_16x16x32_bf16 v[124:127], a[132:135], v[184:187], v[124:127]\n"
    "ds_read_b64_tr_b16 a[182:183], v10 offset:21504\n"
    "s_waitcnt lgkmcnt(3)\n"
    "v_mfma_f32_16x16x32_bf16 v[100:103], a[136:139], v[192:195], v[100:103]\n"
    "s_add_u32 s94, s94, 0x8000\n"
    "v_exp_f32 v79, v79\n"
    "v_mfma_f32_16x16x32_bf16 v[116:119], a[136:139], v[208:211], v[116:119]\n"
    "ds_read_b64_tr_b16 a[184:185], v9 offset:17920\n"
    "v_mfma_f32_16x16x32_bf16 v[108:111], a[136:139], v[200:203], v[108:111]\n"
    "v_exp_f32 v88, v88\n"
    "v_mfma_f32_16x16x32_bf16 v[124:127], a[136:139], v[216:219], v[124:127]\n"
    "ds_read_b64_tr_b16 a[186:187], v9 offset:22016\n"
    "v_cvt_pk_bf16_f32 v75, v78, v79\n"
    "v_mfma_f32_16x16x32_bf16 v[100:103], a[140:143], v[224:227], v[100:103]\n"
    "buffer_load_dwordx4 v[228:231], v56, s[72:75], s96 offen sc1\n"
    "v_mfma_f32_16x16x32_bf16 v[116:119], a[140:143], v[240:243], v[116:119]\n"
    "ds_read_b64_tr_b16 a[188:189], v10 offset:17920\n"
    "v_mfma_f32_16x16x32_bf16 v[108:111], a[140:143], v[232:235], v[108:111]\n"
    "v_exp_f32 v89, v89\n"
    "v_mfma_f32_16x16x32_bf16 v[124:127], a[140:143], v[248:251], v[124:127]\n"
    "ds_read_b64_tr_b16 a[190:191], v10 offset:22016\n"
    "v_exp_f32 v90, v90\n"
    "v_exp_f32 v91, v91\n"
    "v_cvt_pk_bf16_f32 v88, v88, v89\n"
    "v_exp_f32 v92, v92\n"
    "v_cvt_pk_bf16_f32 v89, v90, v91\n"
    "v_exp_f32 v93, v93\n"
    "v_exp_f32 v94, v94\n"
    "v_exp_f32 v95, v95\n"
    "v_cvt_pk_bf16_f32 v90, v92, v93\n"
    "v_cvt_pk_bf16_f32 v91, v94, v95\n"
    "v_mfma_f32_16x16x32_bf16 a[144:147], v[34:37], v[64:67], a[144:147]\n"
    "v_mfma_f32_16x16x32_bf16 a[152:155], v[34:37], v[80:83], a[152:155]\n"
    "v_mfma_f32_16x16x32_bf16 a[148:151], v[34:37], v[72:75], a[148:151]\n"
    "buffer_load_dwordx4 v[236:239], v57, s[72:75], s96 offen sc1\n"
    "v_mfma_f32_16x16x32_bf16 a[156:159], v[34:37], v[88:91], a[156:159]\n"
    "v_exp_f32 v96, v96\n"
    "s_waitcnt vmcnt(18)\n"
    "s_barrier\n"
    "v_mfma_f32_16x16x32_bf16 a[0:3], a[160:163], v[64:67], a[0:3]\n"
    "v_mfma_f32_16x16x32_bf16 a[16:19], a[160:163], v[80:83], a[16:19]\n"
    "v_exp_f32 v97, v97\n"
    "v_mfma_f32_16x16x32_bf16 a[8:11], a[160:163], v[72:75], a[8:11]\n"
    "v_exp_f32 v98, v98\n"
    "v_mfma_f32_16x16x32_bf16 a[24:27], a[160:163], v[88:91], a[24:27]\n"
    "v_exp_f32 v99, v99\n"
    "v_mfma_f32_16x16x32_bf16 a[4:7], a[164:167], v[64:67], a[4:7]\n"
    "ds_read_b128 a[128:131], v11 offset:32768\n"
    "buffer_load_dwordx4 v[244:247], v56, s[72:75], s96 offen offset:2048 sc1\n"
    "v_mfma_f32_16x16x32_bf16 a[20:23], a[164:167], v[80:83], a[20:23]\n"
    "v_cvt_pk_bf16_f32 v96, v96, v97\n"
    "v_cvt_pk_bf16_f32 v97, v98, v99\n"
    "v_mfma_f32_16x16x32_bf16 a[12:15], a[164:167], v[72:75], a[12:15]\n"
    "v_exp_f32 v100, v100\n"
    "v_mfma_f32_16x16x32_bf16 a[28:31], a[164:167], v[88:91], a[28:31]\n"
    "ds_read_b128 a[132:135], v12 offset:32768\n"
    "v_mfma_f32_16x16x32_bf16 a[32:35], a[168:171], v[64:67], a[32:35]\n"
    "v_exp_f32 v101, v101\n"
    "v_mfma_f32_16x16x32_bf16 a[48:51], a[168:171], v[80:83], a[48:51]\n"
    "v_exp_f32 v102, v102\n"
    "v_mfma_f32_16x16x32_bf16 a[40:43], a[168:171], v[72:75], a[40:43]\n"
    "ds_read_b128 a[136:139], v13 offset:32768\n"
    "buffer_load_dwordx4 v[252:255], v57, s[72:75], s96 offen offset:2048 sc1\n"
    "s_add_u32 s96, s96, 0x1000\n"
    "v_mfma_f32_16x16x32_bf16 a[56:59], a[168:171], v[88:91], a[56:59]\n"
    "v_exp_f32 v103, v103\n"
    "v_mfma_f32_16x16x32_bf16 a[36:39], a[172:175], v[64:67], a[36:39]\n"
    "s_add_u32 m0, s91, 0\n"
    "v_cvt_pk_bf16_f32 v98, v100, v101\n"
    "v_cvt_pk_bf16_f32 v99, v102, v103\n"
    "v_mfma_f32_16x16x32_bf16 a[52:55], a[172:175], v[80:83], a[52:55]\n"
    "ds_read_b128 a[140:143], v14 offset:32768\n"
    "s_add_u32 s90, s91, 0x10000\n"
    "v_mfma_f32_16x16x32_bf16 a[44:47], a[172:175], v[72:75], a[44:47]\n"
    "buffer_load_dwordx4 v23, s[60:63], s90 offen lds\n"
    "v_mfma_f32_16x16x32_bf16 a[60:63], a[172:175], v[88:91], a[60:63]\n"
    "v_exp_f32 v112, v112\n"
    "s_waitcnt lgkmcnt(1)\n"
    "v_mfma_f32_16x16x32_bf16 a[64:67], a[176:179], v[64:67], a[64:67]\n"
    "v_exp_f32 v113, v113\n"
    "v_mfma_f32_16x16x32_bf16 a[80:83], a[176:179], v[80:83], a[80:83]\n"
    "v_exp_f32 v114, v114\n"
    "v_mfma_f32_16x16x32_bf16 a[72:75], a[176:179], v[72:75], a[72:75]\n"
    "buffer_load_dwordx4 v24, s[60:63], s90 offen offset:1024 lds\n"
    "ds_read_b64_tr_b16 v[132:133], v40\n"
    "v_mfma_f32_16x16x32_bf16 a[88:91], a[176:179], v[88:91], a[88:91]\n"
    "ds_read_b64_tr_b16 v[134:135], v40 offset:1024\n"
    "v_cvt_pk_bf16_f32 v112, v112, v113\n"
    "v_mfma_f32_16x16x32_bf16 a[68:71], a[180:183], v[64:67], a[68:71]\n"
    "ds_read_b64_tr_b16 v[140:141], v41\n"
    "v_mfma_f32_16x16x32_bf16 a[84:87], a[180:183], v[80:83], a[84:87]\n"
    "ds_read_b64_tr_b16 v[142:143], v41 offset:1024\n"
    "v_mfma_f32_16x16x32_bf16 a[76:79], a[180:183], v[72:75], a[76:79]\n"
    "buffer_load_dwordx4 v23, s[60:63], s90 offen offset:2048 lds\n"
    "ds_read_b64_tr_b16 v[148:149], v42\n"
    "v_mfma_f32_16x16x32_bf16 a[92:95], a[180:183], v[88:91], a[92:95]\n"
    "ds_read_b64_tr_b16 v[150:151], v42 offset:1024\n"
    "v_mfma_f32_16x16x32_bf16 a[96:99], a[184:187], v[64:67], a[96:99]\n"
    "ds_read_b64_tr_b16 v[156:157], v43\n"
    "v_mfma_f32_16x16x32_bf16 a[112:115], a[184:187], v[80:83], a[112:115]\n"
    "ds_read_b64_tr_b16 v[158:159], v43 offset:1024\n"
    "v_mfma_f32_16x16x32_bf16 a[104:107], a[184:187], v[72:75], a[104:107]\n"
    "buffer_load_dwordx4 v24, s[60:63], s90 offen offset:3072 lds\n"
    "v_mfma_f32_16x16x32_bf16 a[120:123], a[184:187], v[88:91], a[120:123]\n"
    "v_exp_f32 v115, v115\n"
    "v_mfma_f32_16x16x32_bf16 a[100:103], a[188:191], v[64:67], a[100:103]\n"
    "v_exp_f32 v116, v116\n"
    "v_mfma_f32_16x16x32_bf16 a[116:119], a[188:191], v[80:83], a[116:119]\n"
    "v_cvt_pk_bf16_f32 v113, v114, v115\n"
    "v_mfma_f32_16x16x32_bf16 a[108:111], a[188:191], v[72:75], a[108:111]\n"
    "v_exp_f32 v117, v117\n"
    "v_mfma_f32_16x16x32_bf16 a[124:127], a[188:191], v[88:91], a[124:127]\n"
    "s_waitcnt vmcnt(17) lgkmcnt(1)\n"
    "v_exp_f32 v118, v118\n"
    "v_mfma_f32_16x16x32_bf16 a[192:195], v[132:135], v[196:199], 0\n"
    "s_waitcnt vmcnt(11)\n"
    "v_exp_f32 v119, v119\n"
    "v_mfma_f32_16x16x32_bf16 a[200:203], v[132:135], v[212:215], 0\n"
    "v_cvt_pk_bf16_f32 v114, v116, v117\n"
    "v_cvt_pk_bf16_f32 v115, v118, v119\n"
    "v_mfma_f32_16x16x32_bf16 a[208:211], v[132:135], v[204:207], 0\n"
    "s_waitcnt vmcnt(9)\n"
    "v_exp_f32 v104, v104\n"
    "v_mfma_f32_16x16x32_bf16 a[216:219], v[132:135], v[220:223], 0\n"
    "v_exp_f32 v105, v105\n"
    "v_mfma_f32_16x16x32_bf16 a[196:199], v[140:143], v[196:199], 0\n"
    "v_exp_f32 v106, v106\n"
    "v_mfma_f32_16x16x32_bf16 a[204:207], v[140:143], v[212:215], 0\n"
    "v_exp_f32 v107, v107\n"
    "v_mfma_f32_16x16x32_bf16 a[212:215], v[140:143], v[204:207], 0\n"
    "v_cvt_pk_bf16_f32 v104, v104, v105\n"
    "v_cvt_pk_bf16_f32 v105, v106, v107\n"
    "v_mfma_f32_16x16x32_bf16 a[220:223], v[140:143], v[220:223], 0\n"
    "v_exp_f32 v108, v108\n"
    "v_mfma_f32_16x16x32_bf16 a[224:227], v[148:151], v[196:199], 0\n"
    "v_exp_f32 v109, v109\n"
    "v_mfma_f32_16x16x32_bf16 a[232:235], v[148:151], v[212:215], 0\n"
    "v_exp_f32 v110, v110\n"
    "v_mfma_f32_16x16x32_bf16 a[240:243], v[148:151], v[204:207], 0\n"
    "v_exp_f32 v111, v111\n"
    "v_mfma_f32_16x16x32_bf16 a[248:251], v[148:151], v[220:223], 0\n"
    "s_waitcnt lgkmcnt(0)\n"
    "v_cvt_pk_bf16_f32 v106, v108, v109\n"
    "v_cvt_pk_bf16_f32 v107, v110, v111\n"
    "v_mfma_f32_16x16x32_bf16 a[228:231], v[156:159], v[196:199], 0\n"
    "v_exp_f32 v120, v120\n"
    "v_mfma_f32_16x16x32_bf16 a[236:239], v[156:159], v[212:215], 0\n"
    "v_exp_f32 v121, v121\n"
    "v_mfma_f32_16x16x32_bf16 a[244:247], v[156:159], v[204:207], 0\n"
    "v_exp_f32 v122, v122\n"
    "v_mfma_f32_16x16x32_bf16 a[252:255], v[156:159], v[220:223], 0\n"
    "v_exp_f32 v123, v123\n"
    "v_mfma_f32_16x16x32_bf16 v[64:67], a[128:131], v[128:131], 0\n"
    "v_cvt_pk_bf16_f32 v120, v120, v121\n"
    "v_cvt_pk_bf16_f32 v121, v122, v123\n"
    "v_mfma_f32_16x16x32_bf16 v[80:83], a[128:131], v[144:147], 0\n"
    "ds_read_b64_tr_b16 a[160:161], v9 offset:24576\n"
    "v_mfma_f32_16x16x32_bf16 v[72:75], a[128:131], v[136:139], 0\n"
    "s_add_u32 m0, s89, 0\n"
    "v_exp_f32 v124, v124\n"
    "v_mfma_f32_16x16x32_bf16 v[88:91], a[128:131], v[152:155], 0\n"
    "ds_read_b64_tr_b16 a[162:163], v9 offset:28672\n"
    "v_mfma_f32_16x16x32_bf16 v[64:67], a[132:135], v[160:163], v[64:67]\n"
    "ds_read_b128 a[128:131], v11 offset:36864\n"
    "buffer_load_dwordx4 v29, s[68:71], s94 offen lds\n"
    "v_mfma_f32_16x16x32_bf16 v[80:83], a[132:135], v[176:179], v[80:83]\n"
    "ds_read_b64_tr_b16 a[164:165], v10 offset:24576\n"
    "v_mfma_f32_16x16x32_bf16 v[72:75], a[132:135], v[168:171], v[72:75]\n"
    "v_exp_f32 v125, v125\n"
    "v_mfma_f32_16x16x32_bf16 v[88:91], a[132:135], v[184:187], v[88:91]\n"
    "ds_read_b64_tr_b16 a[166:167], v10 offset:28672\n"
    "v_mfma_f32_16x16x32_bf16 v[64:67], a[136:139], v[192:195], v[64:67]\n"
    "ds_read_b128 a[132:135], v12 offset:36864\n"
    "v_mfma_f32_16x16x32_bf16 v[80:83], a[136:139], v[208:211], v[80:83]\n"
    "ds_read_b64_tr_b16 a[168:169], v9 offset:25088\n"
    "v_mfma_f32_16x16x32_bf16 v[72:75], a[136:139], v[200:203], v[72:75]\n"
    "buffer_load_dwordx4 v30, s[68:71], s94 offen offset:1024 lds\n"
    "v_mfma_f32_16x16x32_bf16 v[88:91], a[136:139], v[216:219], v[88:91]\n"
    "ds_read_b64_tr_b16 a[170:171], v9 offset:29184\n"
    "v_mfma_f32_16x16x32_bf16 v[64:67], a[140:143], v[224:227], v[64:67]\n"
    "ds_read_b128 a[136:139], v13 offset:36864\n"
    "v_mfma_f32_16x16x32_bf16 v[80:83], a[140:143], v[240:243], v[80:83]\n"
    "ds_read_b64_tr_b16 a[172:173], v10 offset:25088\n"
    "v_mfma_f32_16x16x32_bf16 v[72:75], a[140:143], v[232:235], v[72:75]\n"
    "v_exp_f32 v126, v126\n"
    "v_mfma_f32_16x16x32_bf16 v[88:91], a[140:143], v[248:251], v[88:91]\n"
    "s_waitcnt lgkmcnt(4)\n"
    "ds_read_b64_tr_b16 a[174:175], v10 offset:29184\n"
    "v_cvt_pk_bf16_f32 v122, v124, v125\n"
    "v_mfma_f32_16x16x32_bf16 v[68:71], a[128:131], v[128:131], 0\n"
    "ds_read_b128 a[140:143], v14 offset:36864\n"
    "buffer_load_dwordx4 v31, s[68:71], s94 offen offset:2048 lds\n"
    "v_mfma_f32_16x16x32_bf16 v[84:87], a[128:131], v[144:147], 0\n"
    "ds_read_b64_tr_b16 a[176:177], v9 offset:25600\n"
    "v_mfma_f32_16x16x32_bf16 v[76:79], a[128:131], v[136:139], 0\n"
    "v_exp_f32 v127, v127\n"
    "v_mfma_f32_16x16x32_bf16 v[92:95], a[128:131], v[152:155], 0\n"
    "ds_read_b64_tr_b16 a[178:179], v9 offset:29696\n"
    "v_mfma_f32_16x16x32_bf16 v[68:71], a[132:135], v[160:163], v[68:71]\n"
    "v_mfma_f32_16x16x32_bf16 v[84:87], a[132:135], v[176:179], v[84:87]\n"
    "ds_read_b64_tr_b16 a[180:181], v10 offset:25600\n"
    "v_mfma_f32_16x16x32_bf16 v[76:79], a[132:135], v[168:171], v[76:79]\n"
    "buffer_load_dwordx4 v32, s[68:71], s94 offen offset:3072 lds\n"
    "v_mfma_f32_16x16x32_bf16 v[92:95], a[132:135], v[184:187], v[92:95]\n"
    "s_waitcnt lgkmcnt(3)\n"
    "ds_read_b64_tr_b16 a[182:183], v10 offset:29696\n"
    "v_mfma_f32_16x16x32_bf16 v[68:71], a[136:139], v[192:195], v[68:71]\n"
    "s_add_u32 s94, s94, 0x8000\n"
    "v_mfma_f32_16x16x32_bf16 v[84:87], a[136:139], v[208:211], v[84:87]\n"
    "ds_read_b64_tr_b16 a[184:185], v9 offset:26112\n"
    "v_mfma_f32_16x16x32_bf16 v[76:79], a[136:139], v[200:203], v[76:79]\n"
    "v_mfma_f32_16x16x32_bf16 v[92:95], a[136:139], v[216:219], v[92:95]\n"
    "ds_read_b64_tr_b16 a[186:187], v9 offset:30208\n"
    "v_mfma_f32_16x16x32_bf16 v[68:71], a[140:143], v[224:227], v[68:71]\n"
    "buffer_load_dwordx4 v[196:199], v56, s[72:75], s96 offen sc1\n"
    "v_mfma_f32_16x16x32_bf16 v[84:87], a[140:143], v[240:243], v[84:87]\n"
    "ds_read_b64_tr_b16 a[188:189], v10 offset:26112\n"
    "v_mfma_f32_16x16x32_bf16 v[76:79], a[140:143], v[232:235], v[76:79]\n"
    "v_mfma_f32_16x16x32_bf16 v[92:95], a[140:143], v[248:251], v[92:95]\n"
    "ds_read_b64_tr_b16 a[190:191], v10 offset:30208\n"
    "v_cvt_pk_bf16_f32 v123, v126, v127\n"
    "v_mfma_f32_16x16x32_bf16 a[144:147], v[34:37], v[96:99], a[144:147]\n"
    "v_mfma_f32_16x16x32_bf16 a[152:155], v[34:37], v[112:115], a[152:155]\n"
    "v_mfma_f32_16x16x32_bf16 a[148:151], v[34:37], v[104:107], a[148:151]\n"
    "buffer_load_dwordx4 v[204:207], v57, s[72:75], s96 offen sc1\n"
    "v_mfma_f32_16x16x32_bf16 a[156:159], v[34:37], v[120:123], a[156:159]\n"
    "v_exp_f32 v64, v64\n"
    "v_mfma_f32_16x16x32_bf16 a[0:3], a[160:163], v[96:99], a[0:3]\n"
    "v_exp_f32 v65, v65\n"
    "v_mfma_f32_16x16x32_bf16 a[16:19], a[160:163], v[112:115], a[16:19]\n"
    "v_exp_f32 v66, v66\n"
    "v_mfma_f32_16x16x32_bf16 a[8:11], a[160:163], v[104:107], a[8:11]\n"
    "v_exp_f32 v67, v67\n"
    "v_mfma_f32_16x16x32_bf16 a[24:27], a[160:163], v[120:123], a[24:27]\n"
    "v_cvt_pk_bf16_f32 v64, v64, v65\n"
    "v_cvt_pk_bf16_f32 v65, v66, v67\n"
    "v_mfma_f32_16x16x32_bf16 a[4:7], a[164:167], v[96:99], a[4:7]\n"
    "ds_read_b128 a[128:131], v11 offset:40960\n"
    "buffer_load_dwordx4 v[212:215], v56, s[72:75], s96 offen offset:2048 sc1\n"
    "v_mfma_f32_16x16x32_bf16 a[20:23], a[164:167], v[112:115], a[20:23]\n"
    "v_exp_f32 v68, v68\n"
    "v_mfma_f32_16x16x32_bf16 a[12:15], a[164:167], v[104:107], a[12:15]\n"
    "v_exp_f32 v69, v69\n"
    "v_mfma_f32_16x16x32_bf16 a[28:31], a[164:167], v[120:123], a[28:31]\n"
    "ds_read_b128 a[132:135], v12 offset:40960\n"
    "v_mfma_f32_16x16x32_bf16 a[32:35], a[168:171], v[96:99], a[32:35]\n"
    "v_exp_f32 v70, v70\n"
    "v_mfma_f32_16x16x32_bf16 a[48:51], a[168:171], v[112:115], a[48:51]\n"
    "v_exp_f32 v71, v71\n"
    "v_mfma_f32_16x16x32_bf16 a[40:43], a[168:171], v[104:107], a[40:43]\n"
    "ds_read_b128 a[136:139], v13 offset:40960\n"
    "buffer_load_dwordx4 v[220:223], v57, s[72:75], s96 offen offset:2048 sc1\n"
    "s_add_u32 s96, s96, 0x1000\n"
    "v_mfma_f32_16x16x32_bf16 a[56:59], a[168:171], v[120:123], a[56:59]\n"
    "v_cvt_pk_bf16_f32 v66, v68, v69\n"
    "v_cvt_pk_bf16_f32 v67, v70, v71\n"
    "v_mfma_f32_16x16x32_bf16 a[36:39], a[172:175], v[96:99], a[36:39]\n"
    "s_add_u32 m0, s91, 0xc000\n"
    "v_exp_f32 v80, v80\n"
    "v_mfma_f32_16x16x32_bf16 a[52:55], a[172:175], v[112:115], a[52:55]\n"
    "ds_read_b128 a[140:143], v14 offset:40960\n"
    "s_add_u32 s90, s91, 0x14000\n"
    "v_mfma_f32_16x16x32_bf16 a[44:47], a[172:175], v[104:107], a[44:47]\n"
    "buffer_load_dwordx4 v19, s[56:59], s90 offen lds\n"
    "v_mfma_f32_16x16x32_bf16 a[60:63], a[172:175], v[120:123], a[60:63]\n"
    "s_waitcnt lgkmcnt(1)\n"
    "v_exp_f32 v81, v81\n"
    "v_mfma_f32_16x16x32_bf16 a[64:67], a[176:179], v[96:99], a[64:67]\n"
    "v_exp_f32 v82, v82\n"
    "v_mfma_f32_16x16x32_bf16 a[80:83], a[176:179], v[112:115], a[80:83]\n"
    "v_exp_f32 v83, v83\n"
    "v_mfma_f32_16x16x32_bf16 a[72:75], a[176:179], v[104:107], a[72:75]\n"
    "buffer_load_dwordx4 v20, s[56:59], s90 offen offset:1024 lds\n"
    "ds_read_b64_tr_b16 v[164:165], v40 offset:4096\n"
    "v_mfma_f32_16x16x32_bf16 a[88:91], a[176:179], v[120:123], a[88:91]\n"
    "ds_read_b64_tr_b16 v[166:167], v40 offset:5120\n"
    "v_cvt_pk_bf16_f32 v80, v80, v81\n"
    "v_mfma_f32_16x16x32_bf16 a[68:71], a[180:183], v[96:99], a[68:71]\n"
    "ds_read_b64_tr_b16 v[172:173], v41 offset:4096\n"
    "v_cvt_pk_bf16_f32 v81, v82, v83\n"
    "v_mfma_f32_16x16x32_bf16 a[84:87], a[180:183], v[112:115], a[84:87]\n"
    "ds_read_b64_tr_b16 v[174:175], v41 offset:5120\n"
    "v_mfma_f32_16x16x32_bf16 a[76:79], a[180:183], v[104:107], a[76:79]\n"
    "buffer_load_dwordx4 v21, s[56:59], s90 offen offset:2048 lds\n"
    "ds_read_b64_tr_b16 v[180:181], v42 offset:4096\n"
    "v_mfma_f32_16x16x32_bf16 a[92:95], a[180:183], v[120:123], a[92:95]\n"
    "ds_read_b64_tr_b16 v[182:183], v42 offset:5120\n"
    "v_mfma_f32_16x16x32_bf16 a[96:99], a[184:187], v[96:99], a[96:99]\n"
    "ds_read_b64_tr_b16 v[188:189], v43 offset:4096\n"
    "v_mfma_f32_16x16x32_bf16 a[112:115], a[184:187], v[112:115], a[112:115]\n"
    "ds_read_b64_tr_b16 v[190:191], v43 offset:5120\n"
    "v_mfma_f32_16x16x32_bf16 a[104:107], a[184:187], v[104:107], a[104:107]\n"
    "buffer_load_dwordx4 v22, s[56:59], s90 offen offset:3072 lds\n"
    "v_mfma_f32_16x16x32_bf16 a[120:123], a[184:187], v[120:123], a[120:123]\n"
    "v_exp_f32 v84, v84\n"
    "v_mfma_f32_16x16x32_bf16 a[100:103], a[188:191], v[96:99], a[100:103]\n"
    "v_exp_f32 v85, v85\n"
    "v_mfma_f32_16x16x32_bf16 a[116:119], a[188:191], v[112:115], a[116:119]\n"
    "v_exp_f32 v86, v86\n"
    "v_mfma_f32_16x16x32_bf16 a[108:111], a[188:191], v[104:107], a[108:111]\n"
    "v_exp_f32 v87, v87\n"
    "v_mfma_f32_16x16x32_bf16 a[124:127], a[188:191], v[120:123], a[124:127]\n"
    "s_waitcnt vmcnt(19) lgkmcnt(1)\n"
    "v_cvt_pk_bf16_f32 v82, v84, v85\n"
    "v_cvt_pk_bf16_f32 v83, v86, v87\n"
    "v_mfma_f32_16x16x32_bf16 a[192:195], v[164:167], v[228:231], a[192:195]\n"
    "s_waitcnt vmcnt(17)\n"
    "v_exp_f32 v72, v72\n"
    "v_mfma_f32_16x16x32_bf16 a[200:203], v[164:167], v[244:247], a[200:203]\n"
    "v_exp_f32 v73, v73\n"
    "v_mfma_f32_16x16x32_bf16 a[208:211], v[164:167], v[236:239], a[208:211]\n"
    "s_waitcnt vmcnt(16)\n"
    "v_exp_f32 v74, v74\n"
    "v_mfma_f32_16x16x32_bf16 a[216:219], v[164:167], v[252:255], a[216:219]\n"
    "v_exp_f32 v75, v75\n"
    "v_mfma_f32_16x16x32_bf16 a[196:199], v[172:175], v[228:231], a[196:199]\n"
    "v_cvt_pk_bf16_f32 v72, v72, v73\n"
    "v_cvt_pk_bf16_f32 v73, v74, v75\n"
    "v_mfma_f32_16x16x32_bf16 a[204:207], v[172:175], v[244:247], a[204:207]\n"
    "v_exp_f32 v76, v76\n"
    "v_mfma_f32_16x16x32_bf16 a[212:215], v[172:175], v[236:239], a[212:215]\n"
    "v_exp_f32 v77, v77\n"
    "v_mfma_f32_16x16x32_bf16 a[220:223], v[172:175], v[252:255], a[220:223]\n"
    "v_exp_f32 v78, v78\n"
    "v_mfma_f32_16x16x32_bf16 a[224:227], v[180:183], v[228:231], a[224:227]\n"
    "v_exp_f32 v79, v79\n"
    "v_mfma_f32_16x16x32_bf16 a[232:235], v[180:183], v[244:247], a[232:235]\n"
    "v_cvt_pk_bf16_f32 v74, v76, v77\n"
    "v_cvt_pk_bf16_f32 v75, v78, v79\n"
    "v_mfma_f32_16x16x32_bf16 a[240:243], v[180:183], v[236:239], a[240:243]\n"
    "v_exp_f32 v88, v88\n"
    "v_mfma_f32_16x16x32_bf16 a[248:251], v[180:183], v[252:255], a[248:251]\n"
    "v_exp_f32 v89, v89\n"
    "s_waitcnt lgkmcnt(0)\n"
    "v_mfma_f32_16x16x32_bf16 a[228:231], v[188:191], v[228:231], a[228:231]\n"
    "v_exp_f32 v90, v90\n"
    "v_mfma_f32_16x16x32_bf16 a[236:239], v[188:191], v[244:247], a[236:239]\n"
    "v_exp_f32 v91, v91\n"
    "v_mfma_f32_16x16x32_bf16 a[244:247], v[188:191], v[236:239], a[244:247]\n"
    "v_cvt_pk_bf16_f32 v88, v88, v89\n"
    "v_cvt_pk_bf16_f32 v89, v90, v91\n"
    "v_mfma_f32_16x16x32_bf16 a[252:255], v[188:191], v[252:255], a[252:255]\n"
    "v_exp_f32 v92, v92\n"
    "v_mfma_f32_16x16x32_bf16 v[96:99], a[128:131], v[128:131], 0\n"
    "v_exp_f32 v93, v93\n"
    "v_mfma_f32_16x16x32_bf16 v[112:115], a[128:131], v[144:147], 0\n"
    "ds_read_b64_tr_b16 a[160:161], v7\n"
    "v_mfma_f32_16x16x32_bf16 v[104:107], a[128:131], v[136:139], 0\n"
    "s_add_u32 m0, s89, 0x1000\n"
    "v_exp_f32 v94, v94\n"
    "v_mfma_f32_16x16x32_bf16 v[120:123], a[128:131], v[152:155], 0\n"
    "ds_read_b64_tr_b16 a[162:163], v7 offset:4096\n"
    "v_cvt_pk_bf16_f32 v90, v92, v93\n"
    "v_mfma_f32_16x16x32_bf16 v[96:99], a[132:135], v[160:163], v[96:99]\n"
    "ds_read_b128 a[128:131], v11 offset:45056\n"
    "buffer_load_dwordx4 v29, s[68:71], s94 offen lds\n"
    "v_mfma_f32_16x16x32_bf16 v[112:115], a[132:135], v[176:179], v[112:115]\n"
    "ds_read_b64_tr_b16 a[164:165], v8\n"
    "v_mfma_f32_16x16x32_bf16 v[104:107], a[132:135], v[168:171], v[104:107]\n"
    "v_exp_f32 v95, v95\n"
    "v_mfma_f32_16x16x32_bf16 v[120:123], a[132:135], v[184:187], v[120:123]\n"
    "ds_read_b64_tr_b16 a[166:167], v8 offset:4096\n"
    "v_mfma_f32_16x16x32_bf16 v[96:99], a[136:139], v[192:195], v[96:99]\n"
    "ds_read_b128 a[132:135], v12 offset:45056\n"
    "v_mfma_f32_16x16x32_bf16 v[112:115], a[136:139], v[208:211], v[112:115]\n"
    "ds_read_b64_tr_b16 a[168:169], v7 offset:512\n"
    "v_mfma_f32_16x16x32_bf16 v[104:107], a[136:139], v[200:203], v[104:107]\n"
    "buffer_load_dwordx4 v30, s[68:71], s94 offen offset:1024 lds\n"
    "v_mfma_f32_16x16x32_bf16 v[120:123], a[136:139], v[216:219], v[120:123]\n"
    "ds_read_b64_tr_b16 a[170:171], v7 offset:4608\n"
    "v_mfma_f32_16x16x32_bf16 v[96:99], a[140:143], v[224:227], v[96:99]\n"
    "ds_read_b128 a[136:139], v13 offset:45056\n"
    "v_mfma_f32_16x16x32_bf16 v[112:115], a[140:143], v[240:243], v[112:115]\n"
    "ds_read_b64_tr_b16 a[172:173], v8 offset:512\n"
    "v_mfma_f32_16x16x32_bf16 v[104:107], a[140:143], v[232:235], v[104:107]\n"
    "v_mfma_f32_16x16x32_bf16 v[120:123], a[140:143], v[248:251], v[120:123]\n"
    "s_waitcnt lgkmcnt(5)\n"
    "ds_read_b64_tr_b16 a[174:175], v8 offset:4608\n"
    "v_mfma_f32_16x16x32_bf16 v[100:103], a[128:131], v[128:131], 0\n"
    "ds_read_b128 a[140:143], v14 offset:45056\n"
    "buffer_load_dwordx4 v31, s[68:71], s94 offen offset:2048 lds\n"
    "v_mfma_f32_16x16x32_bf16 v[116:119], a[128:131], v[144:147], 0\n"
    "ds_read_b64_tr_b16 a[176:177], v7 offset:1024\n"
    "v_mfma_f32_16x16x32_bf16 v[108:111], a[128:131], v[136:139], 0\n"
    "v_mfma_f32_16x16x32_bf16 v[124:127], a[128:131], v[152:155], 0\n"
    "ds_read_b64_tr_b16 a[178:179], v7 offset:5120\n"
    "s_waitcnt lgkmcnt(5)\n"
    "v_mfma_f32_16x16x32_bf16 v[100:103], a[132:135], v[160:163], v[100:103]\n"
    "v_mfma_f32_16x16x32_bf16 v[116:119], a[132:135], v[176:179], v[116:119]\n"
    "ds_read_b64_tr_b16 a[180:181], v8 offset:1024\n"
    "v_mfma_f32_16x16x32_bf16 v[108:111], a[132:135], v[168:171], v[108:111]\n"
    "buffer_load_dwordx4 v32, s[68:71], s94 offen offset:3072 lds\n"
    "v_mfma_f32_16x16x32_bf16 v[124:127], a[132:135], v[184:187], v[124:127]\n"
    "ds_read_b64_tr_b16 a[182:183], v8 offset:5120\n"
    "v_mfma_f32_16x16x32_bf16 v[100:103], a[136:139], v[192:195], v[100:103]\n"
    "s_add_u32 s94, s94, 0x8000\n"
    "v_mfma_f32_16x16x32_bf16 v[116:119], a[136:139], v[208:211], v[116:119]\n"
    "ds_read_b64_tr_b16 a[184:185], v7 offset:1536\n"
    "v_mfma_f32_16x16x32_bf16 v[108:111], a[136:139], v[200:203], v[108:111]\n"
    "v_mfma_f32_16x16x32_bf16 v[124:127], a[136:139], v[216:219], v[124:127]\n"
    "ds_read_b64_tr_b16 a[186:187], v7 offset:5632\n"
    "s_waitcnt lgkmcnt(4)\n"
    "v_mfma_f32_16x16x32_bf16 v[100:103], a[140:143], v[224:227], v[100:103]\n"
    "buffer_load_dwordx4 v[228:231], v56, s[72:75], s96 offen sc1\n"
    "v_mfma_f32_16x16x32_bf16 v[116:119], a[140:143], v[240:243], v[116:119]\n"
    "ds_read_b64_tr_b16 a[188:189], v8 offset:1536\n"
    "v_mfma_f32_16x16x32_bf16 v[108:111], a[140:143], v[232:235], v[108:111]\n"
    "v_mfma_f32_16x16x32_bf16 v[124:127], a[140:143], v[248:251], v[124:127]\n"
    "ds_read_b64_tr_b16 a[190:191], v8 offset:5632\n"
    "v_cvt_pk_bf16_f32 v91, v94, v95\n"
    "v_mfma_f32_16x16x32_bf16 a[144:147], v[34:37], v[64:67], a[144:147]\n"
    "v_mfma_f32_16x16x32_bf16 a[152:155], v[34:37], v[80:83], a[152:155]\n"
    "v_mfma_f32_16x16x32_bf16 a[148:151], v[34:37], v[72:75], a[148:151]\n"
    "buffer_load_dwordx4 v[236:239], v57, s[72:75], s96 offen sc1\n"
    "v_mfma_f32_16x16x32_bf16 a[156:159], v[34:37], v[88:91], a[156:159]\n"
    "v_exp_f32 v96, v96\n"
    "s_waitcnt vmcnt(18)\n"
    "s_barrier\n"
    "v_mfma_f32_16x16x32_bf16 a[0:3], a[160:163], v[64:67], a[0:3]\n"
    "v_mfma_f32_16x16x32_bf16 a[16:19], a[160:163], v[80:83], a[16:19]\n"
    "v_exp_f32 v97, v97\n"
    "v_mfma_f32_16x16x32_bf16 a[8:11], a[160:163], v[72:75], a[8:11]\n"
    "v_exp_f32 v98, v98\n"
    "v_mfma_f32_16x16x32_bf16 a[24:27], a[160:163], v[88:91], a[24:27]\n"
    "v_exp_f32 v99, v99\n"
    "v_mfma_f32_16x16x32_bf16 a[4:7], a[164:167], v[64:67], a[4:7]\n"
    "ds_read_b128 a[128:131], v11\n"
    "buffer_load_dwordx4 v[244:247], v56, s[72:75], s96 offen offset:2048 sc1\n"
    "v_mfma_f32_16x16x32_bf16 a[20:23], a[164:167], v[80:83], a[20:23]\n"
    "v_cvt_pk_bf16_f32 v96, v96, v97\n"
    "v_cvt_pk_bf16_f32 v97, v98, v99\n"
    "v_mfma_f32_16x16x32_bf16 a[12:15], a[164:167], v[72:75], a[12:15]\n"
    "v_exp_f32 v100, v100\n"
    "v_mfma_f32_16x16x32_bf16 a[28:31], a[164:167], v[88:91], a[28:31]\n"
    "ds_read_b128 a[132:135], v12\n"
    "v_mfma_f32_16x16x32_bf16 a[32:35], a[168:171], v[64:67], a[32:35]\n"
    "v_exp_f32 v101, v101\n"
    "v_mfma_f32_16x16x32_bf16 a[48:51], a[168:171], v[80:83], a[48:51]\n"
    "v_exp_f32 v102, v102\n"
    "v_mfma_f32_16x16x32_bf16 a[40:43], a[168:171], v[72:75], a[40:43]\n"
    "ds_read_b128 a[136:139], v13\n"
    "buffer_load_dwordx4 v[252:255], v57, s[72:75], s96 offen offset:2048 sc1\n"
    "s_add_u32 s96, s96, 0x1000\n"
    "v_mfma_f32_16x16x32_bf16 a[56:59], a[168:171], v[88:91], a[56:59]\n"
    "v_exp_f32 v103, v103\n"
    "v_mfma_f32_16x16x32_bf16 a[36:39], a[172:175], v[64:67], a[36:39]\n"
    "s_add_u32 m0, s91, 0x4000\n"
    "v_cvt_pk_bf16_f32 v98, v100, v101\n"
    "v_cvt_pk_bf16_f32 v99, v102, v103\n"
    "v_mfma_f32_16x16x32_bf16 a[52:55], a[172:175], v[80:83], a[52:55]\n"
    "ds_read_b128 a[140:143], v14\n"
    "s_add_u32 s90, s91, 0x14000\n"
    "v_mfma_f32_16x16x32_bf16 a[44:47], a[172:175], v[72:75], a[44:47]\n"
    "buffer_load_dwordx4 v23, s[60:63], s90 offen lds\n"
    "v_mfma_f32_16x16x32_bf16 a[60:63], a[172:175], v[88:91], a[60:63]\n"
    "v_exp_f32 v112, v112\n"
    "v_mfma_f32_16x16x32_bf16 a[64:67], a[176:179], v[64:67], a[64:67]\n"
    "v_exp_f32 v113, v113\n"
    "v_mfma_f32_16x16x32_bf16 a[80:83], a[176:179], v[80:83], a[80:83]\n"
    "v_exp_f32 v114, v114\n"
    "v_mfma_f32_16x16x32_bf16 a[72:75], a[176:179], v[72:75], a[72:75]\n"
    "buffer_load_dwordx4 v24, s[60:63], s90 offen offset:1024 lds\n"
    "ds_read_b64_tr_b16 v[132:133], v40 offset:8192\n"
    "v_mfma_f32_16x16x32_bf16 a[88:91], a[176:179], v[88:91], a[88:91]\n"
    "ds_read_b64_tr_b16 v[134:135], v40 offset:9216\n"
    "v_cvt_pk_bf16_f32 v112, v112, v113\n"
    "s_waitcnt lgkmcnt(2)\n"
    "v_mfma_f32_16x16x32_bf16 a[68:71], a[180:183], v[64:67], a[68:71]\n"
    "ds_read_b64_tr_b16 v[140:141], v41 offset:8192\n"
    "v_mfma_f32_16x16x32_bf16 a[84:87], a[180:183], v[80:83], a[84:87]\n"
    "ds_read_b64_tr_b16 v[142:143], v41 offset:9216\n"
    "v_mfma_f32_16x16x32_bf16 a[76:79], a[180:183], v[72:75], a[76:79]\n"
    "buffer_load_dwordx4 v23, s[60:63], s90 offen offset:2048 lds\n"
    "ds_read_b64_tr_b16 v[148:149], v42 offset:8192\n"
    "v_mfma_f32_16x16x32_bf16 a[92:95], a[180:183], v[88:91], a[92:95]\n"
    "ds_read_b64_tr_b16 v[150:151], v42 offset:9216\n"
    "v_mfma_f32_16x16x32_bf16 a[96:99], a[184:187], v[64:67], a[96:99]\n"
    "ds_read_b64_tr_b16 v[156:157], v43 offset:8192\n"
    "v_mfma_f32_16x16x32_bf16 a[112:115], a[184:187], v[80:83], a[112:115]\n"
    "ds_read_b64_tr_b16 v[158:159], v43 offset:9216\n"
    "v_mfma_f32_16x16x32_bf16 a[104:107], a[184:187], v[72:75], a[104:107]\n"
    "buffer_load_dwordx4 v24, s[60:63], s90 offen offset:3072 lds\n"
    "v_mfma_f32_16x16x32_bf16 a[120:123], a[184:187], v[88:91], a[120:123]\n"
    "v_exp_f32 v115, v115\n"
    "v_mfma_f32_16x16x32_bf16 a[100:103], a[188:191], v[64:67], a[100:103]\n"
    "v_exp_f32 v116, v116\n"
    "v_mfma_f32_16x16x32_bf16 a[116:119], a[188:191], v[80:83], a[116:119]\n"
    "v_cvt_pk_bf16_f32 v113, v114, v115\n"
    "v_mfma_f32_16x16x32_bf16 a[108:111], a[188:191], v[72:75], a[108:111]\n"
    "v_exp_f32 v117, v117\n"
    "v_mfma_f32_16x16x32_bf16 a[124:127], a[188:191], v[88:91], a[124:127]\n"
    "s_waitcnt vmcnt(19) lgkmcnt(1)\n"
    "v_exp_f32 v118, v118\n"
    "v_mfma_f32_16x16x32_bf16 a[192:195], v[132:135], v[196:199], a[192:195]\n"
    "s_waitcnt vmcnt(17)\n"
    "v_exp_f32 v119, v119\n"
    "v_mfma_f32_16x16x32_bf16 a[200:203], v[132:135], v[212:215], a[200:203]\n"
    "v_cvt_pk_bf16_f32 v114, v116, v117\n"
    "v_cvt_pk_bf16_f32 v115, v118, v119\n"
    "v_mfma_f32_16x16x32_bf16 a[208:211], v[132:135], v[204:207], a[208:211]\n"
    "s_waitcnt vmcnt(16)\n"
    "v_exp_f32 v104, v104\n"
    "v_mfma_f32_16x16x32_bf16 a[216:219], v[132:135], v[220:223], a[216:219]\n"
    "v_exp_f32 v105, v105\n"
    "v_mfma_f32_16x16x32_bf16 a[196:199], v[140:143], v[196:199], a[196:199]\n"
    "v_exp_f32 v106, v106\n"
    "v_mfma_f32_16x16x32_bf16 a[204:207], v[140:143], v[212:215], a[204:207]\n"
    "v_exp_f32 v107, v107\n"
    "v_mfma_f32_16x16x32_bf16 a[212:215], v[140:143], v[204:207], a[212:215]\n"
    "v_cvt_pk_bf16_f32 v104, v104, v105\n"
    "v_cvt_pk_bf16_f32 v105, v106, v107\n"
    "v_mfma_f32_16x16x32_bf16 a[220:223], v[140:143], v[220:223], a[220:223]\n"
    "v_exp_f32 v108, v108\n"
    "v_mfma_f32_16x16x32_bf16 a[224:227], v[148:151], v[196:199], a[224:227]\n"
    "v_exp_f32 v109, v109\n"
    "v_mfma_f32_16x16x32_bf16 a[232:235], v[148:151], v[212:215], a[232:235]\n"
    "v_exp_f32 v110, v110\n"
    "v_mfma_f32_16x16x32_bf16 a[240:243], v[148:151], v[204:207], a[240:243]\n"
    "v_exp_f32 v111, v111\n"
    "v_mfma_f32_16x16x32_bf16 a[248:251], v[148:151], v[220:223], a[248:251]\n"
    "s_waitcnt lgkmcnt(0)\n"
    "v_cvt_pk_bf16_f32 v106, v108, v109\n"
    "v_cvt_pk_bf16_f32 v107, v110, v111\n"
    "v_mfma_f32_16x16x32_bf16 a[228:231], v[156:159], v[196:199], a[228:231]\n"
    "v_exp_f32 v120, v120\n"
    "v_mfma_f32_16x16x32_bf16 a[236:239], v[156:159], v[212:215], a[236:239]\n"
    "v_exp_f32 v121, v121\n"
    "v_mfma_f32_16x16x32_bf16 a[244:247], v[156:159], v[204:207], a[244:247]\n"
    "v_exp_f32 v122, v122\n"
    "v_mfma_f32_16x16x32_bf16 a[252:255], v[156:159], v[220:223], a[252:255]\n"
    "v_exp_f32 v123, v123\n"
    "v_mfma_f32_16x16x32_bf16 v[64:67], a[128:131], v[128:131], 0\n"
    "v_cvt_pk_bf16_f32 v120, v120, v121\n"
    "v_cvt_pk_bf16_f32 v121, v122, v123\n"
    "v_mfma_f32_16x16x32_bf16 v[80:83], a[128:131], v[144:147], 0\n"
    "ds_read_b64_tr_b16 a[160:161], v7 offset:8192\n"
    "v_mfma_f32_16x16x32_bf16 v[72:75], a[128:131], v[136:139], 0\n"
    "s_add_u32 m0, s89, 0x2000\n"
    "v_exp_f32 v124, v124\n"
    "v_mfma_f32_16x16x32_bf16 v[88:91], a[128:131], v[152:155], 0\n"
    "ds_read_b64_tr_b16 a[162:163], v7 offset:12288\n"
    "v_mfma_f32_16x16x32_bf16 v[64:67], a[132:135], v[160:163], v[64:67]\n"
    "ds_read_b128 a[128:131], v11 offset:4096\n"
    "buffer_load_dwordx4 v29, s[68:71], s94 offen lds\n"
    "v_mfma_f32_16x16x32_bf16 v[80:83], a[132:135], v[176:179], v[80:83]\n"
    "ds_read_b64_tr_b16 a[164:165], v8 offset:8192\n"
    "v_mfma_f32_16x16x32_bf16 v[72:75], a[132:135], v[168:171], v[72:75]\n"
    "v_exp_f32 v125, v125\n"
    "v_mfma_f32_16x16x32_bf16 v[88:91], a[132:135], v[184:187], v[88:91]\n"
    "ds_read_b64_tr_b16 a[166:167], v8 offset:12288\n"
    "v_mfma_f32_16x16x32_bf16 v[64:67], a[136:139], v[192:195], v[64:67]\n"
    "ds_read_b128 a[132:135], v12 offset:4096\n"
    "v_mfma_f32_16x16x32_bf16 v[80:83], a[136:139], v[208:211], v[80:83]\n"
    "ds_read_b64_tr_b16 a[168:169], v7 offset:8704\n"
    "v_mfma_f32_16x16x32_bf16 v[72:75], a[136:139], v[200:203], v[72:75]\n"
    "buffer_load_dwordx4 v30, s[68:71], s94 offen offset:1024 lds\n"
    "v_mfma_f32_16x16x32_bf16 v[88:91], a[136:139], v[216:219], v[88:91]\n"
    "ds_read_b64_tr_b16 a[170:171], v7 offset:12800\n"
    "v_mfma_f32_16x16x32_bf16 v[64:67], a[140:143], v[224:227], v[64:67]\n"
    "ds_read_b128 a[136:139], v13 offset:4096\n"
    "v_mfma_f32_16x16x32_bf16 v[80:83], a[140:143], v[240:243], v[80:83]\n"
    "ds_read_b64_tr_b16 a[172:173], v8 offset:8704\n"
    "v_mfma_f32_16x16x32_bf16 v[72:75], a[140:143], v[232:235], v[72:75]\n"
    "v_exp_f32 v126, v126\n"
    "v_mfma_f32_16x16x32_bf16 v[88:91], a[140:143], v[248:251], v[88:91]\n"
    "s_waitcnt lgkmcnt(4)\n"
    "ds_read_b64_tr_b16 a[174:175], v8 offset:12800\n"
    "v_cvt_pk_bf16_f32 v122, v124, v125\n"
    "v_mfma_f32_16x16x32_bf16 v[68:71], a[128:131], v[128:131], 0\n"
    "ds_read_b128 a[140:143], v14 offset:4096\n"
    "buffer_load_dwordx4 v31, s[68:71], s94 offen offset:2048 lds\n"
    "v_mfma_f32_16x16x32_bf16 v[84:87], a[128:131], v[144:147], 0\n"
    "ds_read_b64_tr_b16 a[176:177], v7 offset:9216\n"
    "v_mfma_f32_16x16x32_bf16 v[76:79], a[128:131], v[136:139], 0\n"
    "v_exp_f32 v127, v127\n"
    "v_mfma_f32_16x16x32_bf16 v[92:95], a[128:131], v[152:155], 0\n"
    "ds_read_b64_tr_b16 a[178:179], v7 offset:13312\n"
    "v_mfma_f32_16x16x32_bf16 v[68:71], a[132:135], v[160:163], v[68:71]\n"
    "v_mfma_f32_16x16x32_bf16 v[84:87], a[132:135], v[176:179], v[84:87]\n"
    "ds_read_b64_tr_b16 a[180:181], v8 offset:9216\n"
    "v_mfma_f32_16x16x32_bf16 v[76:79], a[132:135], v[168:171], v[76:79]\n"
    "buffer_load_dwordx4 v32, s[68:71], s94 offen offset:3072 lds\n"
    "v_mfma_f32_16x16x32_bf16 v[92:95], a[132:135], v[184:187], v[92:95]\n"
    "s_waitcnt lgkmcnt(3)\n"
    "ds_read_b64_tr_b16 a[182:183], v8 offset:13312\n"
    "v_mfma_f32_16x16x32_bf16 v[68:71], a[136:139], v[192:195], v[68:71]\n"
    "s_add_u32 s94, s94, 0x8000\n"
    "s_lshl_b32 s39, s33, 19\n"
    "v_mfma_f32_16x16x32_bf16 v[84:87], a[136:139], v[208:211], v[84:87]\n"
    "ds_read_b64_tr_b16 a[184:185], v7 offset:9728\n"
    "s_lshl_b32 s97, s34, 14\n"
    "v_mfma_f32_16x16x32_bf16 v[76:79], a[136:139], v[200:203], v[76:79]\n"
    "s_add_u32 s39, s39, s97\n"
    "v_mfma_f32_16x16x32_bf16 v[92:95], a[136:139], v[216:219], v[92:95]\n"
    "ds_read_b64_tr_b16 a[186:187], v7 offset:13824\n"
    "v_mfma_f32_16x16x32_bf16 v[68:71], a[140:143], v[224:227], v[68:71]\n"
    "buffer_load_dwordx4 v[196:199], v56, s[72:75], s96 offen sc1\n"
    "s_lshl_b32 s97, s32, 12\n"
    "v_mfma_f32_16x16x32_bf16 v[84:87], a[140:143], v[240:243], v[84:87]\n"
    "ds_read_b64_tr_b16 a[188:189], v8 offset:9728\n"
    "s_add_u32 s39, s39, s97\n"
    "v_mfma_f32_16x16x32_bf16 v[76:79], a[140:143], v[232:235], v[76:79]\n"
    "buffer_load_dword v58, v62, s[64:67], s39 offen\n"
    "v_mfma_f32_16x16x32_bf16 v[92:95], a[140:143], v[248:251], v[92:95]\n"
    "ds_read_b64_tr_b16 a[190:191], v8 offset:13824\n"
    "v_cvt_pk_bf16_f32 v123, v126, v127\n"
    "v_mfma_f32_16x16x32_bf16 a[144:147], v[34:37], v[96:99], a[144:147]\n"
    "v_mfma_f32_16x16x32_bf16 a[152:155], v[34:37], v[112:115], a[152:155]\n"
    "v_mfma_f32_16x16x32_bf16 a[148:151], v[34:37], v[104:107], a[148:151]\n"
    "buffer_load_dwordx4 v[204:207], v57, s[72:75], s96 offen sc1\n"
    "v_mfma_f32_16x16x32_bf16 a[156:159], v[34:37], v[120:123], a[156:159]\n"
    "v_exp_f32 v64, v64\n"
    "v_mfma_f32_16x16x32_bf16 a[0:3], a[160:163], v[96:99], a[0:3]\n"
    "v_exp_f32 v65, v65\n"
    "v_mfma_f32_16x16x32_bf16 a[16:19], a[160:163], v[112:115], a[16:19]\n"
    "v_exp_f32 v66, v66\n"
    "v_mfma_f32_16x16x32_bf16 a[8:11], a[160:163], v[104:107], a[8:11]\n"
    "v_exp_f32 v67, v67\n"
    "v_mfma_f32_16x16x32_bf16 a[24:27], a[160:163], v[120:123], a[24:27]\n"
    "v_cvt_pk_bf16_f32 v64, v64, v65\n"
    "v_cvt_pk_bf16_f32 v65, v66, v67\n"
    "v_mfma_f32_16x16x32_bf16 a[4:7], a[164:167], v[96:99], a[4:7]\n"
    "ds_read_b128 a[128:131], v11 offset:8192\n"
    "buffer_load_dwordx4 v[212:215], v56, s[72:75], s96 offen offset:2048 sc1\n"
    "v_mfma_f32_16x16x32_bf16 a[20:23], a[164:167], v[112:115], a[20:23]\n"
    "v_exp_f32 v68, v68\n"
    "v_mfma_f32_16x16x32_bf16 a[12:15], a[164:167], v[104:107], a[12:15]\n"
    "v_exp_f32 v69, v69\n"
    "v_mfma_f32_16x16x32_bf16 a[28:31], a[164:167], v[120:123], a[28:31]\n"
    "ds_read_b128 a[132:135], v12 offset:8192\n"
    "v_mfma_f32_16x16x32_bf16 a[32:35], a[168:171], v[96:99], a[32:35]\n"
    "v_exp_f32 v70, v70\n"
    "v_mfma_f32_16x16x32_bf16 a[48:51], a[168:171], v[112:115], a[48:51]\n"
    "v_exp_f32 v71, v71\n"
    "v_mfma_f32_16x16x32_bf16 a[40:43], a[168:171], v[104:107], a[40:43]\n"
    "ds_read_b128 a[136:139], v13 offset:8192\n"
    "buffer_load_dwordx4 v[220:223], v57, s[72:75], s96 offen offset:2048 sc1\n"
    "s_add_u32 s96, s96, 0x1000\n"
    "v_mfma_f32_16x16x32_bf16 a[56:59], a[168:171], v[120:123], a[56:59]\n"
    "v_cvt_pk_bf16_f32 v66, v68, v69\n"
    "v_cvt_pk_bf16_f32 v67, v70, v71\n"
    "v_mfma_f32_16x16x32_bf16 a[36:39], a[172:175], v[96:99], a[36:39]\n"
    "s_add_u32 m0, s91, 0x10000\n"
    "v_exp_f32 v80, v80\n"
    "v_mfma_f32_16x16x32_bf16 a[52:55], a[172:175], v[112:115], a[52:55]\n"
    "ds_read_b128 a[140:143], v14 offset:8192\n"
    "s_add_u32 s90, s91, 0x18000\n"
    "v_mfma_f32_16x16x32_bf16 a[44:47], a[172:175], v[104:107], a[44:47]\n"
    "buffer_load_dwordx4 v19, s[56:59], s90 offen lds\n"
    "v_mfma_f32_16x16x32_bf16 a[60:63], a[172:175], v[120:123], a[60:63]\n"
    "s_waitcnt lgkmcnt(1)\n"
    "v_exp_f32 v81, v81\n"
    "v_mfma_f32_16x16x32_bf16 a[64:67], a[176:179], v[96:99], a[64:67]\n"
    "v_exp_f32 v82, v82\n"
    "v_mfma_f32_16x16x32_bf16 a[80:83], a[176:179], v[112:115], a[80:83]\n"
    "v_exp_f32 v83, v83\n"
    "v_mfma_f32_16x16x32_bf16 a[72:75], a[176:179], v[104:107], a[72:75]\n"
    "buffer_load_dwordx4 v20, s[56:59], s90 offen offset:1024 lds\n"
    "ds_read_b64_tr_b16 v[164:165], v40 offset:12288\n"
    "v_mfma_f32_16x16x32_bf16 a[88:91], a[176:179], v[120:123], a[88:91]\n"
    "ds_read_b64_tr_b16 v[166:167], v40 offset:13312\n"
    "v_cvt_pk_bf16_f32 v80, v80, v81\n"
    "v_mfma_f32_16x16x32_bf16 a[68:71], a[180:183], v[96:99], a[68:71]\n"
    "ds_read_b64_tr_b16 v[172:173], v41 offset:12288\n"
    "v_cvt_pk_bf16_f32 v81, v82, v83\n"
    "v_mfma_f32_16x16x32_bf16 a[84:87], a[180:183], v[112:115], a[84:87]\n"
    "ds_read_b64_tr_b16 v[174:175], v41 offset:13312\n"
    "v_mfma_f32_16x16x32_bf16 a[76:79], a[180:183], v[104:107], a[76:79]\n"
    "buffer_load_dwordx4 v21, s[56:59], s90 offen offset:2048 lds\n"
    "ds_read_b64_tr_b16 v[180:181], v42 offset:12288\n"
    "v_mfma_f32_16x16x32_bf16 a[92:95], a[180:183], v[120:123], a[92:95]\n"
    "ds_read_b64_tr_b16 v[182:183], v42 offset:13312\n"
    "v_mfma_f32_16x16x32_bf16 a[96:99], a[184:187], v[96:99], a[96:99]\n"
    "ds_read_b64_tr_b16 v[188:189], v43 offset:12288\n"
    "v_mfma_f32_16x16x32_bf16 a[112:115], a[184:187], v[112:115], a[112:115]\n"
    "ds_read_b64_tr_b16 v[190:191], v43 offset:13312\n"
    "v_mfma_f32_16x16x32_bf16 a[104:107], a[184:187], v[104:107], a[104:107]\n"
    "buffer_load_dwordx4 v22, s[56:59], s90 offen offset:3072 lds\n"
    "v_mfma_f32_16x16x32_bf16 a[120:123], a[184:187], v[120:123], a[120:123]\n"
    "v_exp_f32 v84, v84\n"
    "v_mfma_f32_16x16x32_bf16 a[100:103], a[188:191], v[96:99], a[100:103]\n"
    "v_exp_f32 v85, v85\n"
    "v_mfma_f32_16x16x32_bf16 a[116:119], a[188:191], v[112:115], a[116:119]\n"
    "v_exp_f32 v86, v86\n"
    "v_mfma_f32_16x16x32_bf16 a[108:111], a[188:191], v[104:107], a[108:111]\n"
    "v_exp_f32 v87, v87\n"
    "v_mfma_f32_16x16x32_bf16 a[124:127], a[188:191], v[120:123], a[124:127]\n"
    "s_waitcnt vmcnt(20) lgkmcnt(1)\n"
    "v_cvt_pk_bf16_f32 v82, v84, v85\n"
    "v_cvt_pk_bf16_f32 v83, v86, v87\n"
    "v_mfma_f32_16x16x32_bf16 a[192:195], v[164:167], v[228:231], a[192:195]\n"
    "s_waitcnt vmcnt(18)\n"
    "v_exp_f32 v72, v72\n"
    "v_mfma_f32_16x16x32_bf16 a[200:203], v[164:167], v[244:247], a[200:203]\n"
    "v_exp_f32 v73, v73\n"
    "v_mfma_f32_16x16x32_bf16 a[208:211], v[164:167], v[236:239], a[208:211]\n"
    "s_waitcnt vmcnt(17)\n"
    "v_exp_f32 v74, v74\n"
    "v_mfma_f32_16x16x32_bf16 a[216:219], v[164:167], v[252:255], a[216:219]\n"
    "v_exp_f32 v75, v75\n"
    "v_mfma_f32_16x16x32_bf16 a[196:199], v[172:175], v[228:231], a[196:199]\n"
    "v_cvt_pk_bf16_f32 v72, v72, v73\n"
    "v_cvt_pk_bf16_f32 v73, v74, v75\n"
    "v_mfma_f32_16x16x32_bf16 a[204:207], v[172:175], v[244:247], a[204:207]\n"
    "v_exp_f32 v76, v76\n"
    "v_mfma_f32_16x16x32_bf16 a[212:215], v[172:175], v[236:239], a[212:215]\n"
    "v_exp_f32 v77, v77\n"
    "v_mfma_f32_16x16x32_bf16 a[220:223], v[172:175], v[252:255], a[220:223]\n"
    "v_exp_f32 v78, v78\n"
    "v_mfma_f32_16x16x32_bf16 a[224:227], v[180:183], v[228:231], a[224:227]\n"
    "v_exp_f32 v79, v79\n"
    "v_mfma_f32_16x16x32_bf16 a[232:235], v[180:183], v[244:247], a[232:235]\n"
    "v_cvt_pk_bf16_f32 v74, v76, v77\n"
    "v_cvt_pk_bf16_f32 v75, v78, v79\n"
    "v_mfma_f32_16x16x32_bf16 a[240:243], v[180:183], v[236:239], a[240:243]\n"
    "v_exp_f32 v88, v88\n"
    "v_mfma_f32_16x16x32_bf16 a[248:251], v[180:183], v[252:255], a[248:251]\n"
    "v_exp_f32 v89, v89\n"
    "s_waitcnt lgkmcnt(0)\n"
    "v_mfma_f32_16x16x32_bf16 a[228:231], v[188:191], v[228:231], a[228:231]\n"
    "v_exp_f32 v90, v90\n"
    "v_mfma_f32_16x16x32_bf16 a[236:239], v[188:191], v[244:247], a[236:239]\n"
    "v_exp_f32 v91, v91\n"
    "v_mfma_f32_16x16x32_bf16 a[244:247], v[188:191], v[236:239], a[244:247]\n"
    "v_cvt_pk_bf16_f32 v88, v88, v89\n"
    "v_cvt_pk_bf16_f32 v89, v90, v91\n"
    "v_mfma_f32_16x16x32_bf16 a[252:255], v[188:191], v[252:255], a[252:255]\n"
    "v_exp_f32 v92, v92\n"
    "v_mfma_f32_16x16x32_bf16 v[96:99], a[128:131], v[128:131], 0\n"
    "v_exp_f32 v93, v93\n"
    "v_mfma_f32_16x16x32_bf16 v[112:115], a[128:131], v[144:147], 0\n"
    "ds_read_b64_tr_b16 a[160:161], v9\n"
    "v_mfma_f32_16x16x32_bf16 v[104:107], a[128:131], v[136:139], 0\n"
    "s_add_u32 m0, s89, 0x3000\n"
    "v_exp_f32 v94, v94\n"
    "v_mfma_f32_16x16x32_bf16 v[120:123], a[128:131], v[152:155], 0\n"
    "ds_read_b64_tr_b16 a[162:163], v9 offset:4096\n"
    "v_cvt_pk_bf16_f32 v90, v92, v93\n"
    "v_mfma_f32_16x16x32_bf16 v[96:99], a[132:135], v[160:163], v[96:99]\n"
    "ds_read_b128 a[128:131], v11 offset:12288\n"
    "buffer_load_dwordx4 v29, s[68:71], s94 offen lds\n"
    "v_mfma_f32_16x16x32_bf16 v[112:115], a[132:135], v[176:179], v[112:115]\n"
    "ds_read_b64_tr_b16 a[164:165], v10\n"
    "v_mfma_f32_16x16x32_bf16 v[104:107], a[132:135], v[168:171], v[104:107]\n"
    "v_exp_f32 v95, v95\n"
    "v_mfma_f32_16x16x32_bf16 v[120:123], a[132:135], v[184:187], v[120:123]\n"
    "ds_read_b64_tr_b16 a[166:167], v10 offset:4096\n"
    "v_mfma_f32_16x16x32_bf16 v[96:99], a[136:139], v[192:195], v[96:99]\n"
    "ds_read_b128 a[132:135], v12 offset:12288\n"
    "v_mfma_f32_16x16x32_bf16 v[112:115], a[136:139], v[208:211], v[112:115]\n"
    "ds_read_b64_tr_b16 a[168:169], v9 offset:512\n"
    "v_mfma_f32_16x16x32_bf16 v[104:107], a[136:139], v[200:203], v[104:107]\n"
    "buffer_load_dwordx4 v30, s[68:71], s94 offen offset:1024 lds\n"
    "v_mfma_f32_16x16x32_bf16 v[120:123], a[136:139], v[216:219], v[120:123]\n"
    "ds_read_b64_tr_b16 a[170:171], v9 offset:4608\n"
    "v_mfma_f32_16x16x32_bf16 v[96:99], a[140:143], v[224:227], v[96:99]\n"
    "ds_read_b128 a[136:139], v13 offset:12288\n"
    "v_mfma_f32_16x16x32_bf16 v[112:115], a[140:143], v[240:243], v[112:115]\n"
    "ds_read_b64_tr_b16 a[172:173], v10 offset:512\n"
    "v_mfma_f32_16x16x32_bf16 v[104:107], a[140:143], v[232:235], v[104:107]\n"
    "v_mfma_f32_16x16x32_bf16 v[120:123], a[140:143], v[248:251], v[120:123]\n"
    "s_waitcnt lgkmcnt(5)\n"
    "ds_read_b64_tr_b16 a[174:175], v10 offset:4608\n"
    "v_mfma_f32_16x16x32_bf16 v[100:103], a[128:131], v[128:131], 0\n"
    "ds_read_b128 a[140:143], v14 offset:12288\n"
    "buffer_load_dwordx4 v31, s[68:71], s94 offen offset:2048 lds\n"
    "v_mfma_f32_16x16x32_bf16 v[116:119], a[128:131], v[144:147], 0\n"
    "ds_read_b64_tr_b16 a[176:177], v9 offset:1024\n"
    "v_mfma_f32_16x16x32_bf16 v[108:111], a[128:131], v[136:139], 0\n"
    "v_mfma_f32_16x16x32_bf16 v[124:127], a[128:131], v[152:155], 0\n"
    "ds_read_b64_tr_b16 a[178:179], v9 offset:5120\n"
    "s_waitcnt lgkmcnt(5)\n"
    "v_mfma_f32_16x16x32_bf16 v[100:103], a[132:135], v[160:163], v[100:103]\n"
    "v_mfma_f32_16x16x32_bf16 v[116:119], a[132:135], v[176:179], v[116:119]\n"
    "ds_read_b64_tr_b16 a[180:181], v10 offset:1024\n"
    "v_mfma_f32_16x16x32_bf16 v[108:111], a[132:135], v[168:171], v[108:111]\n"
    "buffer_load_dwordx4 v32, s[68:71], s94 offen offset:3072 lds\n"
    "v_mfma_f32_16x16x32_bf16 v[124:127], a[132:135], v[184:187], v[124:127]\n"
    "ds_read_b64_tr_b16 a[182:183], v10 offset:5120\n"
    "v_mfma_f32_16x16x32_bf16 v[100:103], a[136:139], v[192:195], v[100:103]\n"
    "s_add_u32 s94, s94, 0x8000\n"
    "v_mfma_f32_16x16x32_bf16 v[116:119], a[136:139], v[208:211], v[116:119]\n"
    "ds_read_b64_tr_b16 a[184:185], v9 offset:1536\n"
    "v_mfma_f32_16x16x32_bf16 v[108:111], a[136:139], v[200:203], v[108:111]\n"
    "v_mfma_f32_16x16x32_bf16 v[124:127], a[136:139], v[216:219], v[124:127]\n"
    "ds_read_b64_tr_b16 a[186:187], v9 offset:5632\n"
    "s_waitcnt lgkmcnt(4)\n"
    "v_mfma_f32_16x16x32_bf16 v[100:103], a[140:143], v[224:227], v[100:103]\n"
    "buffer_load_dwordx4 v[228:231], v56, s[72:75], s96 offen sc1\n"
    "v_mfma_f32_16x16x32_bf16 v[116:119], a[140:143], v[240:243], v[116:119]\n"
    "ds_read_b64_tr_b16 a[188:189], v10 offset:1536\n"
    "v_mfma_f32_16x16x32_bf16 v[108:111], a[140:143], v[232:235], v[108:111]\n"
    "v_mfma_f32_16x16x32_bf16 v[124:127], a[140:143], v[248:251], v[124:127]\n"
    "ds_read_b64_tr_b16 a[190:191], v10 offset:5632\n"
    "v_cvt_pk_bf16_f32 v91, v94, v95\n"
    "v_mfma_f32_16x16x32_bf16 a[144:147], v[34:37], v[64:67], a[144:147]\n"
    "v_mfma_f32_16x16x32_bf16 a[152:155], v[34:37], v[80:83], a[152:155]\n"
    "v_mfma_f32_16x16x32_bf16 a[148:151], v[34:37], v[72:75], a[148:151]\n"
    "buffer_load_dwordx4 v[236:239], v57, s[72:75], s96 offen sc1\n"
    "v_mfma_f32_16x16x32_bf16 a[156:159], v[34:37], v[88:91], a[156:159]\n"
    "v_exp_f32 v96, v96\n"
    "s_waitcnt vmcnt(19)\n"
    "s_barrier\n"
    "v_mfma_f32_16x16x32_bf16 a[0:3], a[160:163], v[64:67], a[0:3]\n"
    "v_mfma_f32_16x16x32_bf16 a[16:19], a[160:163], v[80:83], a[16:19]\n"
    "v_exp_f32 v97, v97\n"
    "v_mfma_f32_16x16x32_bf16 a[8:11], a[160:163], v[72:75], a[8:11]\n"
    "v_exp_f32 v98, v98\n"
    "v_mfma_f32_16x16x32_bf16 a[24:27], a[160:163], v[88:91], a[24:27]\n"
    "v_exp_f32 v99, v99\n"
    "v_mfma_f32_16x16x32_bf16 a[4:7], a[164:167], v[64:67], a[4:7]\n"
    "ds_read_b128 a[128:131], v11 offset:16384\n"
    "buffer_load_dwordx4 v[244:247], v56, s[72:75], s96 offen offset:2048 sc1\n"
    "v_mfma_f32_16x16x32_bf16 a[20:23], a[164:167], v[80:83], a[20:23]\n"
    "v_cvt_pk_bf16_f32 v96, v96, v97\n"
    "v_cvt_pk_bf16_f32 v97, v98, v99\n"
    "v_mfma_f32_16x16x32_bf16 a[12:15], a[164:167], v[72:75], a[12:15]\n"
    "v_exp_f32 v100, v100\n"
    "v_mfma_f32_16x16x32_bf16 a[28:31], a[164:167], v[88:91], a[28:31]\n"
    "ds_read_b128 a[132:135], v12 offset:16384\n"
    "v_mfma_f32_16x16x32_bf16 a[32:35], a[168:171], v[64:67], a[32:35]\n"
    "v_exp_f32 v101, v101\n"
    "v_mfma_f32_16x16x32_bf16 a[48:51], a[168:171], v[80:83], a[48:51]\n"
    "v_exp_f32 v102, v102\n"
    "v_mfma_f32_16x16x32_bf16 a[40:43], a[168:171], v[72:75], a[40:43]\n"
    "ds_read_b128 a[136:139], v13 offset:16384\n"
    "buffer_load_dwordx4 v[252:255], v57, s[72:75], s96 offen offset:2048 sc1\n"
    "s_add_u32 s96, s96, 0x1000\n"
    "v_mfma_f32_16x16x32_bf16 a[56:59], a[168:171], v[88:91], a[56:59]\n"
    "v_exp_f32 v103, v103\n"
    "v_mfma_f32_16x16x32_bf16 a[36:39], a[172:175], v[64:67], a[36:39]\n"
    "s_add_u32 m0, s91, 0x14000\n"
    "v_cvt_pk_bf16_f32 v98, v100, v101\n"
    "v_cvt_pk_bf16_f32 v99, v102, v103\n"
    "v_mfma_f32_16x16x32_bf16 a[52:55], a[172:175], v[80:83], a[52:55]\n"
    "ds_read_b128 a[140:143], v14 offset:16384\n"
    "s_add_u32 s90, s91, 0x18000\n"
    "v_mfma_f32_16x16x32_bf16 a[44:47], a[172:175], v[72:75], a[44:47]\n"
    "buffer_load_dwordx4 v23, s[60:63], s90 offen lds\n"
    "v_mfma_f32_16x16x32_bf16 a[60:63], a[172:175], v[88:91], a[60:63]\n"
    "v_exp_f32 v112, v112\n"
    "v_mfma_f32_16x16x32_bf16 a[64:67], a[176:179], v[64:67], a[64:67]\n"
    "v_exp_f32 v113, v113\n"
    "v_mfma_f32_16x16x32_bf16 a[80:83], a[176:179], v[80:83], a[80:83]\n"
    "v_exp_f32 v114, v114\n"
    "v_mfma_f32_16x16x32_bf16 a[72:75], a[176:179], v[72:75], a[72:75]\n"
    "buffer_load_dwordx4 v24, s[60:63], s90 offen offset:1024 lds\n"
    "ds_read_b64_tr_b16 v[132:133], v40\n"
    "v_mfma_f32_16x16x32_bf16 a[88:91], a[176:179], v[88:91], a[88:91]\n"
    "ds_read_b64_tr_b16 v[134:135], v40 offset:1024\n"
    "v_cvt_pk_bf16_f32 v112, v112, v113\n"
    "s_waitcnt lgkmcnt(2)\n"
    "v_mfma_f32_16x16x32_bf16 a[68:71], a[180:183], v[64:67], a[68:71]\n"
    "ds_read_b64_tr_b16 v[140:141], v41\n"
    "v_mfma_f32_16x16x32_bf16 a[84:87], a[180:183], v[80:83], a[84:87]\n"
    "ds_read_b64_tr_b16 v[142:143], v41 offset:1024\n"
    "v_mfma_f32_16x16x32_bf16 a[76:79], a[180:183], v[72:75], a[76:79]\n"
    "buffer_load_dwordx4 v23, s[60:63], s90 offen offset:2048 lds\n"
    "ds_read_b64_tr_b16 v[148:149], v42\n"
    "v_mfma_f32_16x16x32_bf16 a[92:95], a[180:183], v[88:91], a[92:95]\n"
    "ds_read_b64_tr_b16 v[150:151], v42 offset:1024\n"
    "v_mfma_f32_16x16x32_bf16 a[96:99], a[184:187], v[64:67], a[96:99]\n"
    "ds_read_b64_tr_b16 v[156:157], v43\n"
    "v_mfma_f32_16x16x32_bf16 a[112:115], a[184:187], v[80:83], a[112:115]\n"
    "ds_read_b64_tr_b16 v[158:159], v43 offset:1024\n"
    "v_mfma_f32_16x16x32_bf16 a[104:107], a[184:187], v[72:75], a[104:107]\n"
    "buffer_load_dwordx4 v24, s[60:63], s90 offen offset:3072 lds\n"
    "v_mfma_f32_16x16x32_bf16 a[120:123], a[184:187], v[88:91], a[120:123]\n"
    "v_exp_f32 v115, v115\n"
    "v_mfma_f32_16x16x32_bf16 a[100:103], a[188:191], v[64:67], a[100:103]\n"
    "v_exp_f32 v116, v116\n"
    "v_mfma_f32_16x16x32_bf16 a[116:119], a[188:191], v[80:83], a[116:119]\n"
    "v_cvt_pk_bf16_f32 v113, v114, v115\n"
    "v_mfma_f32_16x16x32_bf16 a[108:111], a[188:191], v[72:75], a[108:111]\n"
    "v_exp_f32 v117, v117\n"
    "v_mfma_f32_16x16x32_bf16 a[124:127], a[188:191], v[88:91], a[124:127]\n"
    "s_waitcnt vmcnt(20) lgkmcnt(1)\n"
    "v_exp_f32 v118, v118\n"
    "v_mfma_f32_16x16x32_bf16 a[192:195], v[132:135], v[196:199], a[192:195]\n"
    "s_waitcnt vmcnt(17)\n"
    "v_exp_f32 v119, v119\n"
    "v_mfma_f32_16x16x32_bf16 a[200:203], v[132:135], v[212:215], a[200:203]\n"
    "v_cvt_pk_bf16_f32 v114, v116, v117\n"
    "v_cvt_pk_bf16_f32 v115, v118, v119\n"
    "v_mfma_f32_16x16x32_bf16 a[208:211], v[132:135], v[204:207], a[208:211]\n"
    "s_waitcnt vmcnt(16)\n"
    "v_exp_f32 v104, v104\n"
    "v_mfma_f32_16x16x32_bf16 a[216:219], v[132:135], v[220:223], a[216:219]\n"
    "v_exp_f32 v105, v105\n"
    "v_mfma_f32_16x16x32_bf16 a[196:199], v[140:143], v[196:199], a[196:199]\n"
    "v_exp_f32 v106, v106\n"
    "v_mfma_f32_16x16x32_bf16 a[204:207], v[140:143], v[212:215], a[204:207]\n"
    "v_exp_f32 v107, v107\n"
    "v_mfma_f32_16x16x32_bf16 a[212:215], v[140:143], v[204:207], a[212:215]\n"
    "v_cvt_pk_bf16_f32 v104, v104, v105\n"
    "v_cvt_pk_bf16_f32 v105, v106, v107\n"
    "v_mfma_f32_16x16x32_bf16 a[220:223], v[140:143], v[220:223], a[220:223]\n"
    "v_exp_f32 v108, v108\n"
    "v_mfma_f32_16x16x32_bf16 a[224:227], v[148:151], v[196:199], a[224:227]\n"
    "v_exp_f32 v109, v109\n"
    "v_mfma_f32_16x16x32_bf16 a[232:235], v[148:151], v[212:215], a[232:235]\n"
    "v_exp_f32 v110, v110\n"
    "v_mfma_f32_16x16x32_bf16 a[240:243], v[148:151], v[204:207], a[240:243]\n"
    "v_exp_f32 v111, v111\n"
    "v_mfma_f32_16x16x32_bf16 a[248:251], v[148:151], v[220:223], a[248:251]\n"
    "s_waitcnt lgkmcnt(0)\n"
    "v_cvt_pk_bf16_f32 v106, v108, v109\n"
    "v_cvt_pk_bf16_f32 v107, v110, v111\n"
    "v_mfma_f32_16x16x32_bf16 a[228:231], v[156:159], v[196:199], a[228:231]\n"
    "v_exp_f32 v120, v120\n"
    "v_mfma_f32_16x16x32_bf16 a[236:239], v[156:159], v[212:215], a[236:239]\n"
    "v_exp_f32 v121, v121\n"
    "v_mfma_f32_16x16x32_bf16 a[244:247], v[156:159], v[204:207], a[244:247]\n"
    "v_exp_f32 v122, v122\n"
    "v_mfma_f32_16x16x32_bf16 a[252:255], v[156:159], v[220:223], a[252:255]\n"
    "v_exp_f32 v123, v123\n"
    "v_mfma_f32_16x16x32_bf16 v[64:67], a[128:131], v[128:131], 0\n"
    "v_cvt_pk_bf16_f32 v120, v120, v121\n"
    "v_cvt_pk_bf16_f32 v121, v122, v123\n"
    "v_mfma_f32_16x16x32_bf16 v[80:83], a[128:131], v[144:147], 0\n"
    "ds_read_b64_tr_b16 a[160:161], v9 offset:8192\n"
    "v_mfma_f32_16x16x32_bf16 v[72:75], a[128:131], v[136:139], 0\n"
    "s_add_u32 m0, s89, 0\n"
    "v_exp_f32 v124, v124\n"
    "v_mfma_f32_16x16x32_bf16 v[88:91], a[128:131], v[152:155], 0\n"
    "ds_read_b64_tr_b16 a[162:163], v9 offset:12288\n"
    "v_mfma_f32_16x16x32_bf16 v[64:67], a[132:135], v[160:163], v[64:67]\n"
    "ds_read_b128 a[128:131], v11 offset:20480\n"
    "buffer_load_dwordx4 v29, s[68:71], s94 offen lds\n"
    "v_mfma_f32_16x16x32_bf16 v[80:83], a[132:135], v[176:179], v[80:83]\n"
    "ds_read_b64_tr_b16 a[164:165], v10 offset:8192\n"
    "v_mfma_f32_16x16x32_bf16 v[72:75], a[132:135], v[168:171], v[72:75]\n"
    "v_exp_f32 v125, v125\n"
    "v_mfma_f32_16x16x32_bf16 v[88:91], a[132:135], v[184:187], v[88:91]\n"
    "ds_read_b64_tr_b16 a[166:167], v10 offset:12288\n"
    "v_mfma_f32_16x16x32_bf16 v[64:67], a[136:139], v[192:195], v[64:67]\n"
    "ds_read_b128 a[132:135], v12 offset:20480\n"
    "v_mfma_f32_16x16x32_bf16 v[80:83], a[136:139], v[208:211], v[80:83]\n"
    "ds_read_b64_tr_b16 a[168:169], v9 offset:8704\n"
    "v_mfma_f32_16x16x32_bf16 v[72:75], a[136:139], v[200:203], v[72:75]\n"
    "buffer_load_dwordx4 v30, s[68:71], s94 offen offset:1024 lds\n"
    "v_mfma_f32_16x16x32_bf16 v[88:91], a[136:139], v[216:219], v[88:91]\n"
    "ds_read_b64_tr_b16 a[170:171], v9 offset:12800\n"
    "v_mfma_f32_16x16x32_bf16 v[64:67], a[140:143], v[224:227], v[64:67]\n"
    "ds_read_b128 a[136:139], v13 offset:20480\n"
    "v_mfma_f32_16x16x32_bf16 v[80:83], a[140:143], v[240:243], v[80:83]\n"
    "ds_read_b64_tr_b16 a[172:173], v10 offset:8704\n"
    "v_mfma_f32_16x16x32_bf16 v[72:75], a[140:143], v[232:235], v[72:75]\n"
    "v_exp_f32 v126, v126\n"
    "v_mfma_f32_16x16x32_bf16 v[88:91], a[140:143], v[248:251], v[88:91]\n"
    "s_waitcnt lgkmcnt(4)\n"
    "ds_read_b64_tr_b16 a[174:175], v10 offset:12800\n"
    "v_cvt_pk_bf16_f32 v122, v124, v125\n"
    "v_mfma_f32_16x16x32_bf16 v[68:71], a[128:131], v[128:131], 0\n"
    "ds_read_b128 a[140:143], v14 offset:20480\n"
    "buffer_load_dwordx4 v31, s[68:71], s94 offen offset:2048 lds\n"
    "v_mfma_f32_16x16x32_bf16 v[84:87], a[128:131], v[144:147], 0\n"
    "ds_read_b64_tr_b16 a[176:177], v9 offset:9216\n"
    "v_mfma_f32_16x16x32_bf16 v[76:79], a[128:131], v[136:139], 0\n"
    "v_exp_f32 v127, v127\n"
    "v_mfma_f32_16x16x32_bf16 v[92:95], a[128:131], v[152:155], 0\n"
    "ds_read_b64_tr_b16 a[178:179], v9 offset:13312\n"
    "v_mfma_f32_16x16x32_bf16 v[68:71], a[132:135], v[160:163], v[68:71]\n"
    "v_mfma_f32_16x16x32_bf16 v[84:87], a[132:135], v[176:179], v[84:87]\n"
    "ds_read_b64_tr_b16 a[180:181], v10 offset:9216\n"
    "v_mfma_f32_16x16x32_bf16 v[76:79], a[132:135], v[168:171], v[76:79]\n"
    "buffer_load_dwordx4 v32, s[68:71], s94 offen offset:3072 lds\n"
    "v_mfma_f32_16x16x32_bf16 v[92:95], a[132:135], v[184:187], v[92:95]\n"
    "s_waitcnt lgkmcnt(3)\n"
    "ds_read_b64_tr_b16 a[182:183], v10 offset:13312\n"
    "v_mfma_f32_16x16x32_bf16 v[68:71], a[136:139], v[192:195], v[68:71]\n"
    "s_add_u32 s94, s94, 0x8000\n"
    "v_mfma_f32_16x16x32_bf16 v[84:87], a[136:139], v[208:211], v[84:87]\n"
    "ds_read_b64_tr_b16 a[184:185], v9 offset:9728\n"
    "v_mfma_f32_16x16x32_bf16 v[76:79], a[136:139], v[200:203], v[76:79]\n"
    "v_mfma_f32_16x16x32_bf16 v[92:95], a[136:139], v[216:219], v[92:95]\n"
    "ds_read_b64_tr_b16 a[186:187], v9 offset:13824\n"
    "v_mfma_f32_16x16x32_bf16 v[68:71], a[140:143], v[224:227], v[68:71]\n"
    "buffer_load_dwordx4 v[196:199], v56, s[72:75], s96 offen sc1\n"
    "v_mfma_f32_16x16x32_bf16 v[84:87], a[140:143], v[240:243], v[84:87]\n"
    "ds_read_b64_tr_b16 a[188:189], v10 offset:9728\n"
    "v_mfma_f32_16x16x32_bf16 v[76:79], a[140:143], v[232:235], v[76:79]\n"
    "v_mfma_f32_16x16x32_bf16 v[92:95], a[140:143], v[248:251], v[92:95]\n"
    "ds_read_b64_tr_b16 a[190:191], v10 offset:13824\n"
    "v_cvt_pk_bf16_f32 v123, v126, v127\n"
    "v_mfma_f32_16x16x32_bf16 a[144:147], v[34:37], v[96:99], a[144:147]\n"
    "v_mfma_f32_16x16x32_bf16 a[152:155], v[34:37], v[112:115], a[152:155]\n"
    "v_mfma_f32_16x16x32_bf16 a[148:151], v[34:37], v[104:107], a[148:151]\n"
    "buffer_load_dwordx4 v[204:207], v57, s[72:75], s96 offen sc1\n"
    "v_mfma_f32_16x16x32_bf16 a[156:159], v[34:37], v[120:123], a[156:159]\n"
    "v_exp_f32 v64, v64\n"
    "v_mfma_f32_16x16x32_bf16 a[0:3], a[160:163], v[96:99], a[0:3]\n"
    "v_exp_f32 v65, v65\n"
    "v_mfma_f32_16x16x32_bf16 a[16:19], a[160:163], v[112:115], a[16:19]\n"
    "v_exp_f32 v66, v66\n"
    "v_mfma_f32_16x16x32_bf16 a[8:11], a[160:163], v[104:107], a[8:11]\n"
    "v_exp_f32 v67, v67\n"
    "v_mfma_f32_16x16x32_bf16 a[24:27], a[160:163], v[120:123], a[24:27]\n"
    "v_cvt_pk_bf16_f32 v64, v64, v65\n"
    "v_cvt_pk_bf16_f32 v65, v66, v67\n"
    "v_mfma_f32_16x16x32_bf16 a[4:7], a[164:167], v[96:99], a[4:7]\n"
    "ds_read_b128 a[128:131], v11 offset:24576\n"
    "buffer_load_dwordx4 v[212:215], v56, s[72:75], s96 offen offset:2048 sc1\n"
    "v_mfma_f32_16x16x32_bf16 a[20:23], a[164:167], v[112:115], a[20:23]\n"
    "v_exp_f32 v68, v68\n"
    "v_mfma_f32_16x16x32_bf16 a[12:15], a[164:167], v[104:107], a[12:15]\n"
    "v_exp_f32 v69, v69\n"
    "v_mfma_f32_16x16x32_bf16 a[28:31], a[164:167], v[120:123], a[28:31]\n"
    "ds_read_b128 a[132:135], v12 offset:24576\n"
    "v_mfma_f32_16x16x32_bf16 a[32:35], a[168:171], v[96:99], a[32:35]\n"
    "v_exp_f32 v70, v70\n"
    "v_mfma_f32_16x16x32_bf16 a[48:51], a[168:171], v[112:115], a[48:51]\n"
    "v_exp_f32 v71, v71\n"
    "v_mfma_f32_16x16x32_bf16 a[40:43], a[168:171], v[104:107], a[40:43]\n"
    "ds_read_b128 a[136:139], v13 offset:24576\n"
    "buffer_load_dwordx4 v[220:223], v57, s[72:75], s96 offen offset:2048 sc1\n"
    "s_add_u32 s96, s96, 0x1000\n"
    "v_mfma_f32_16x16x32_bf16 a[56:59], a[168:171], v[120:123], a[56:59]\n"
    "v_cvt_pk_bf16_f32 v66, v68, v69\n"
    "v_cvt_pk_bf16_f32 v67, v70, v71\n"
    "v_mfma_f32_16x16x32_bf16 a[36:39], a[172:175], v[96:99], a[36:39]\n"
    "s_add_u32 m0, s91, 0x8000\n"
    "v_exp_f32 v80, v80\n"
    "v_mfma_f32_16x16x32_bf16 a[52:55], a[172:175], v[112:115], a[52:55]\n"
    "ds_read_b128 a[140:143], v14 offset:24576\n"
    "s_add_u32 s90, s91, 0x1c000\n"
    "v_mfma_f32_16x16x32_bf16 a[44:47], a[172:175], v[104:107], a[44:47]\n"
    "buffer_load_dwordx4 v19, s[56:59], s90 offen lds\n"
    "v_mfma_f32_16x16x32_bf16 a[60:63], a[172:175], v[120:123], a[60:63]\n"
    "s_waitcnt lgkmcnt(1)\n"
    "v_exp_f32 v81, v81\n"
    "v_mfma_f32_16x16x32_bf16 a[64:67], a[176:179], v[96:99], a[64:67]\n"
    "v_exp_f32 v82, v82\n"
    "v_mfma_f32_16x16x32_bf16 a[80:83], a[176:179], v[112:115], a[80:83]\n"
    "v_exp_f32 v83, v83\n"
    "v_mfma_f32_16x16x32_bf16 a[72:75], a[176:179], v[104:107], a[72:75]\n"
    "buffer_load_dwordx4 v20, s[56:59], s90 offen offset:1024 lds\n"
    "ds_read_b64_tr_b16 v[164:165], v40 offset:4096\n"
    "v_mfma_f32_16x16x32_bf16 a[88:91], a[176:179], v[120:123], a[88:91]\n"
    "ds_read_b64_tr_b16 v[166:167], v40 offset:5120\n"
    "v_cvt_pk_bf16_f32 v80, v80, v81\n"
    "v_mfma_f32_16x16x32_bf16 a[68:71], a[180:183], v[96:99], a[68:71]\n"
    "ds_read_b64_tr_b16 v[172:173], v41 offset:4096\n"
    "v_cvt_pk_bf16_f32 v81, v82, v83\n"
    "v_mfma_f32_16x16x32_bf16 a[84:87], a[180:183], v[112:115], a[84:87]\n"
    "ds_read_b64_tr_b16 v[174:175], v41 offset:5120\n"
    "v_mfma_f32_16x16x32_bf16 a[76:79], a[180:183], v[104:107], a[76:79]\n"
    "buffer_load_dwordx4 v21, s[56:59], s90 offen offset:2048 lds\n"
    "ds_read_b64_tr_b16 v[180:181], v42 offset:4096\n"
    "v_mfma_f32_16x16x32_bf16 a[92:95], a[180:183], v[120:123], a[92:95]\n"
    "ds_read_b64_tr_b16 v[182:183], v42 offset:5120\n"
    "v_mfma_f32_16x16x32_bf16 a[96:99], a[184:187], v[96:99], a[96:99]\n"
    "ds_read_b64_tr_b16 v[188:189], v43 offset:4096\n"
    "v_mfma_f32_16x16x32_bf16 a[112:115], a[184:187], v[112:115], a[112:115]\n"
    "ds_read_b64_tr_b16 v[190:191], v43 offset:5120\n"
    "v_mfma_f32_16x16x32_bf16 a[104:107], a[184:187], v[104:107], a[104:107]\n"
    "buffer_load_dwordx4 v22, s[56:59], s90 offen offset:3072 lds\n"
    "v_mfma_f32_16x16x32_bf16 a[120:123], a[184:187], v[120:123], a[120:123]\n"
    "v_exp_f32 v84, v84\n"
    "v_mfma_f32_16x16x32_bf16 a[100:103], a[188:191], v[96:99], a[100:103]\n"
    "v_exp_f32 v85, v85\n"
    "v_mfma_f32_16x16x32_bf16 a[116:119], a[188:191], v[112:115], a[116:119]\n"
    "v_exp_f32 v86, v86\n"
    "v_mfma_f32_16x16x32_bf16 a[108:111], a[188:191], v[104:107], a[108:111]\n"
    "v_exp_f32 v87, v87\n"
    "v_mfma_f32_16x16x32_bf16 a[124:127], a[188:191], v[120:123], a[124:127]\n"
    "s_waitcnt vmcnt(19) lgkmcnt(1)\n"
    "v_cvt_pk_bf16_f32 v82, v84, v85\n"
    "v_cvt_pk_bf16_f32 v83, v86, v87\n"
    "v_mfma_f32_16x16x32_bf16 a[192:195], v[164:167], v[228:231], a[192:195]\n"
    "s_waitcnt vmcnt(17)\n"
    "v_exp_f32 v72, v72\n"
    "v_mfma_f32_16x16x32_bf16 a[200:203], v[164:167], v[244:247], a[200:203]\n"
    "v_exp_f32 v73, v73\n"
    "v_mfma_f32_16x16x32_bf16 a[208:211], v[164:167], v[236:239], a[208:211]\n"
    "s_waitcnt vmcnt(16)\n"
    "v_exp_f32 v74, v74\n"
    "v_mfma_f32_16x16x32_bf16 a[216:219], v[164:167], v[252:255], a[216:219]\n"
    "v_exp_f32 v75, v75\n"
    "v_mfma_f32_16x16x32_bf16 a[196:199], v[172:175], v[228:231], a[196:199]\n"
    "v_cvt_pk_bf16_f32 v72, v72, v73\n"
    "v_cvt_pk_bf16_f32 v73, v74, v75\n"
    "v_mfma_f32_16x16x32_bf16 a[204:207], v[172:175], v[244:247], a[204:207]\n"
    "v_exp_f32 v76, v76\n"
    "v_mfma_f32_16x16x32_bf16 a[212:215], v[172:175], v[236:239], a[212:215]\n"
    "v_exp_f32 v77, v77\n"
    "v_mfma_f32_16x16x32_bf16 a[220:223], v[172:175], v[252:255], a[220:223]\n"
    "v_exp_f32 v78, v78\n"
    "v_mfma_f32_16x16x32_bf16 a[224:227], v[180:183], v[228:231], a[224:227]\n"
    "v_exp_f32 v79, v79\n"
    "v_mfma_f32_16x16x32_bf16 a[232:235], v[180:183], v[244:247], a[232:235]\n"
    "v_cvt_pk_bf16_f32 v74, v76, v77\n"
    "v_cvt_pk_bf16_f32 v75, v78, v79\n"
    "v_mfma_f32_16x16x32_bf16 a[240:243], v[180:183], v[236:239], a[240:243]\n"
    "v_exp_f32 v88, v88\n"
    "v_mfma_f32_16x16x32_bf16 a[248:251], v[180:183], v[252:255], a[248:251]\n"
    "v_exp_f32 v89, v89\n"
    "s_waitcnt lgkmcnt(0)\n"
    "v_mfma_f32_16x16x32_bf16 a[228:231], v[188:191], v[228:231], a[228:231]\n"
    "v_exp_f32 v90, v90\n"
    "v_mfma_f32_16x16x32_bf16 a[236:239], v[188:191], v[244:247], a[236:239]\n"
    "v_exp_f32 v91, v91\n"
    "v_mfma_f32_16x16x32_bf16 a[244:247], v[188:191], v[236:239], a[244:247]\n"
    "v_cvt_pk_bf16_f32 v88, v88, v89\n"
    "v_cvt_pk_bf16_f32 v89, v90, v91\n"
    "v_mfma_f32_16x16x32_bf16 a[252:255], v[188:191], v[252:255], a[252:255]\n"
    "v_exp_f32 v92, v92\n"
    "v_mfma_f32_16x16x32_bf16 v[96:99], a[128:131], v[128:131], 0\n"
    "v_exp_f32 v93, v93\n"
    "v_mfma_f32_16x16x32_bf16 v[112:115], a[128:131], v[144:147], 0\n"
    "ds_read_b64_tr_b16 a[160:161], v9 offset:16384\n"
    "v_mfma_f32_16x16x32_bf16 v[104:107], a[128:131], v[136:139], 0\n"
    "s_add_u32 m0, s89, 0x1000\n"
    "v_exp_f32 v94, v94\n"
    "v_mfma_f32_16x16x32_bf16 v[120:123], a[128:131], v[152:155], 0\n"
    "ds_read_b64_tr_b16 a[162:163], v9 offset:20480\n"
    "v_cvt_pk_bf16_f32 v90, v92, v93\n"
    "v_mfma_f32_16x16x32_bf16 v[96:99], a[132:135], v[160:163], v[96:99]\n"
    "ds_read_b128 a[128:131], v11 offset:28672\n"
    "buffer_load_dwordx4 v29, s[68:71], s94 offen lds\n"
    "v_mfma_f32_16x16x32_bf16 v[112:115], a[132:135], v[176:179], v[112:115]\n"
    "ds_read_b64_tr_b16 a[164:165], v10 offset:16384\n"
    "v_mfma_f32_16x16x32_bf16 v[104:107], a[132:135], v[168:171], v[104:107]\n"
    "v_exp_f32 v95, v95\n"
    "v_mfma_f32_16x16x32_bf16 v[120:123], a[132:135], v[184:187], v[120:123]\n"
    "ds_read_b64_tr_b16 a[166:167], v10 offset:20480\n"
    "v_mfma_f32_16x16x32_bf16 v[96:99], a[136:139], v[192:195], v[96:99]\n"
    "ds_read_b128 a[132:135], v12 offset:28672\n"
    "v_mfma_f32_16x16x32_bf16 v[112:115], a[136:139], v[208:211], v[112:115]\n"
    "ds_read_b64_tr_b16 a[168:169], v9 offset:16896\n"
    "v_mfma_f32_16x16x32_bf16 v[104:107], a[136:139], v[200:203], v[104:107]\n"
    "buffer_load_dwordx4 v30, s[68:71], s94 offen offset:1024 lds\n"
    "v_mfma_f32_16x16x32_bf16 v[120:123], a[136:139], v[216:219], v[120:123]\n"
    "ds_read_b64_tr_b16 a[170:171], v9 offset:20992\n"
    "v_mfma_f32_16x16x32_bf16 v[96:99], a[140:143], v[224:227], v[96:99]\n"
    "ds_read_b128 a[136:139], v13 offset:28672\n"
    "v_mfma_f32_16x16x32_bf16 v[112:115], a[140:143], v[240:243], v[112:115]\n"
    "ds_read_b64_tr_b16 a[172:173], v10 offset:16896\n"
    "v_mfma_f32_16x16x32_bf16 v[104:107], a[140:143], v[232:235], v[104:107]\n"
    "v_mfma_f32_16x16x32_bf16 v[120:123], a[140:143], v[248:251], v[120:123]\n"
    "s_waitcnt lgkmcnt(5)\n"
    "ds_read_b64_tr_b16 a[174:175], v10 offset:20992\n"
    "v_mfma_f32_16x16x32_bf16 v[100:103], a[128:131], v[128:131], 0\n"
    "ds_read_b128 a[140:143], v14 offset:28672\n"
    "buffer_load_dwordx4 v31, s[68:71], s94 offen offset:2048 lds\n"
    "v_mfma_f32_16x16x32_bf16 v[116:119], a[128:131], v[144:147], 0\n"
    "ds_read_b64_tr_b16 a[176:177], v9 offset:17408\n"
    "v_mfma_f32_16x16x32_bf16 v[108:111], a[128:131], v[136:139], 0\n"
    "v_mfma_f32_16x16x32_bf16 v[124:127], a[128:131], v[152:155], 0\n"
    "ds_read_b64_tr_b16 a[178:179], v9 offset:21504\n"
    "s_waitcnt lgkmcnt(5)\n"
    "v_mfma_f32_16x16x32_bf16 v[100:103], a[132:135], v[160:163], v[100:103]\n"
    "v_mfma_f32_16x16x32_bf16 v[116:119], a[132:135], v[176:179], v[116:119]\n"
    "ds_read_b64_tr_b16 a[180:181], v10 offset:17408\n"
    "v_mfma_f32_16x16x32_bf16 v[108:111], a[132:135], v[168:171], v[108:111]\n"
    "buffer_load_dwordx4 v32, s[68:71], s94 offen offset:3072 lds\n"
    "v_mfma_f32_16x16x32_bf16 v[124:127], a[132:135], v[184:187], v[124:127]\n"
    "ds_read_b64_tr_b16 a[182:183], v10 offset:21504\n"
    "v_mfma_f32_16x16x32_bf16 v[100:103], a[136:139], v[192:195], v[100:103]\n"
    "s_add_u32 s94, s94, 0x8000\n"
    "v_mfma_f32_16x16x32_bf16 v[116:119], a[136:139], v[208:211], v[116:119]\n"
    "ds_read_b64_tr_b16 a[184:185], v9 offset:17920\n"
    "v_mfma_f32_16x16x32_bf16 v[108:111], a[136:139], v[200:203], v[108:111]\n"
    "v_mfma_f32_16x16x32_bf16 v[124:127], a[136:139], v[216:219], v[124:127]\n"
    "ds_read_b64_tr_b16 a[186:187], v9 offset:22016\n"
    "s_waitcnt lgkmcnt(4)\n"
    "v_mfma_f32_16x16x32_bf16 v[100:103], a[140:143], v[224:227], v[100:103]\n"
    "buffer_load_dwordx4 v[228:231], v56, s[72:75], s96 offen sc1\n"
    "v_mfma_f32_16x16x32_bf16 v[116:119], a[140:143], v[240:243], v[116:119]\n"
    "ds_read_b64_tr_b16 a[188:189], v10 offset:17920\n"
    "v_mfma_f32_16x16x32_bf16 v[108:111], a[140:143], v[232:235], v[108:111]\n"
    "v_mfma_f32_16x16x32_bf16 v[124:127], a[140:143], v[248:251], v[124:127]\n"
    "ds_read_b64_tr_b16 a[190:191], v10 offset:22016\n"
    "v_cvt_pk_bf16_f32 v91, v94, v95\n"
    "v_mfma_f32_16x16x32_bf16 a[144:147], v[34:37], v[64:67], a[144:147]\n"
    "v_mfma_f32_16x16x32_bf16 a[152:155], v[34:37], v[80:83], a[152:155]\n"
    "v_mfma_f32_16x16x32_bf16 a[148:151], v[34:37], v[72:75], a[148:151]\n"
    "buffer_load_dwordx4 v[236:239], v57, s[72:75], s96 offen sc1\n"
    "v_mfma_f32_16x16x32_bf16 a[156:159], v[34:37], v[88:91], a[156:159]\n"
    "v_exp_f32 v96, v96\n"
    "s_waitcnt vmcnt(18)\n"
    "s_barrier\n"
    "v_mfma_f32_16x16x32_bf16 a[0:3], a[160:163], v[64:67], a[0:3]\n"
    "v_mfma_f32_16x16x32_bf16 a[16:19], a[160:163], v[80:83], a[16:19]\n"
    "v_exp_f32 v97, v97\n"
    "v_mfma_f32_16x16x32_bf16 a[8:11], a[160:163], v[72:75], a[8:11]\n"
    "v_exp_f32 v98, v98\n"
    "v_mfma_f32_16x16x32_bf16 a[24:27], a[160:163], v[88:91], a[24:27]\n"
    "v_exp_f32 v99, v99\n"
    "v_mfma_f32_16x16x32_bf16 a[4:7], a[164:167], v[64:67], a[4:7]\n"
    "ds_read_b128 a[128:131], v11 offset:32768\n"
    "buffer_load_dwordx4 v[244:247], v56, s[72:75], s96 offen offset:2048 sc1\n"
    "v_mfma_f32_16x16x32_bf16 a[20:23], a[164:167], v[80:83], a[20:23]\n"
    "v_cvt_pk_bf16_f32 v96, v96, v97\n"
    "v_cvt_pk_bf16_f32 v97, v98, v99\n"
    "v_mfma_f32_16x16x32_bf16 a[12:15], a[164:167], v[72:75], a[12:15]\n"
    "v_exp_f32 v100, v100\n"
    "v_mfma_f32_16x16x32_bf16 a[28:31], a[164:167], v[88:91], a[28:31]\n"
    "ds_read_b128 a[132:135], v12 offset:32768\n"
    "v_mfma_f32_16x16x32_bf16 a[32:35], a[168:171], v[64:67], a[32:35]\n"
    "v_exp_f32 v101, v101\n"
    "v_mfma_f32_16x16x32_bf16 a[48:51], a[168:171], v[80:83], a[48:51]\n"
    "v_exp_f32 v102, v102\n"
    "v_mfma_f32_16x16x32_bf16 a[40:43], a[168:171], v[72:75], a[40:43]\n"
    "ds_read_b128 a[136:139], v13 offset:32768\n"
    "buffer_load_dwordx4 v[252:255], v57, s[72:75], s96 offen offset:2048 sc1\n"
    "s_add_u32 s96, s96, 0x1000\n"
    "v_mfma_f32_16x16x32_bf16 a[56:59], a[168:171], v[88:91], a[56:59]\n"
    "v_exp_f32 v103, v103\n"
    "v_mfma_f32_16x16x32_bf16 a[36:39], a[172:175], v[64:67], a[36:39]\n"
    "s_add_u32 m0, s91, 0\n"
    "v_cvt_pk_bf16_f32 v98, v100, v101\n"
    "v_cvt_pk_bf16_f32 v99, v102, v103\n"
    "v_mfma_f32_16x16x32_bf16 a[52:55], a[172:175], v[80:83], a[52:55]\n"
    "ds_read_b128 a[140:143], v14 offset:32768\n"
    "s_add_u32 s90, s91, 0x1c000\n"
    "v_mfma_f32_16x16x32_bf16 a[44:47], a[172:175], v[72:75], a[44:47]\n"
    "buffer_load_dwordx4 v23, s[60:63], s90 offen lds\n"
    "v_mfma_f32_16x16x32_bf16 a[60:63], a[172:175], v[88:91], a[60:63]\n"
    "v_exp_f32 v112, v112\n"
    "v_mfma_f32_16x16x32_bf16 a[64:67], a[176:179], v[64:67], a[64:67]\n"
    "v_exp_f32 v113, v113\n"
    "v_mfma_f32_16x16x32_bf16 a[80:83], a[176:179], v[80:83], a[80:83]\n"
    "v_exp_f32 v114, v114\n"
    "v_mfma_f32_16x16x32_bf16 a[72:75], a[176:179], v[72:75], a[72:75]\n"
    "buffer_load_dwordx4 v24, s[60:63], s90 offen offset:1024 lds\n"
    "ds_read_b64_tr_b16 v[132:133], v40 offset:8192\n"
    "v_mfma_f32_16x16x32_bf16 a[88:91], a[176:179], v[88:91], a[88:91]\n"
    "ds_read_b64_tr_b16 v[134:135], v40 offset:9216\n"
    "v_cvt_pk_bf16_f32 v112, v112, v113\n"
    "s_waitcnt lgkmcnt(2)\n"
    "v_mfma_f32_16x16x32_bf16 a[68:71], a[180:183], v[64:67], a[68:71]\n"
    "ds_read_b64_tr_b16 v[140:141], v41 offset:8192\n"
    "v_mfma_f32_16x16x32_bf16 a[84:87], a[180:183], v[80:83], a[84:87]\n"
    "ds_read_b64_tr_b16 v[142:143], v41 offset:9216\n"
    "v_mfma_f32_16x16x32_bf16 a[76:79], a[180:183], v[72:75], a[76:79]\n"
    "buffer_load_dwordx4 v23, s[60:63], s90 offen offset:2048 lds\n"
    "ds_read_b64_tr_b16 v[148:149], v42 offset:8192\n"
    "v_mfma_f32_16x16x32_bf16 a[92:95], a[180:183], v[88:91], a[92:95]\n"
    "ds_read_b64_tr_b16 v[150:151], v42 offset:9216\n"
    "v_mfma_f32_16x16x32_bf16 a[96:99], a[184:187], v[64:67], a[96:99]\n"
    "ds_read_b64_tr_b16 v[156:157], v43 offset:8192\n"
    "v_mfma_f32_16x16x32_bf16 a[112:115], a[184:187], v[80:83], a[112:115]\n"
    "ds_read_b64_tr_b16 v[158:159], v43 offset:9216\n"
    "v_mfma_f32_16x16x32_bf16 a[104:107], a[184:187], v[72:75], a[104:107]\n"
    "buffer_load_dwordx4 v24, s[60:63], s90 offen offset:3072 lds\n"
    "v_mfma_f32_16x16x32_bf16 a[120:123], a[184:187], v[88:91], a[120:123]\n"
    "v_exp_f32 v115, v115\n"
    "v_mfma_f32_16x16x32_bf16 a[100:103], a[188:191], v[64:67], a[100:103]\n"
    "v_exp_f32 v116, v116\n"
    "v_mfma_f32_16x16x32_bf16 a[116:119], a[188:191], v[80:83], a[116:119]\n"
    "v_cvt_pk_bf16_f32 v113, v114, v115\n"
    "v_mfma_f32_16x16x32_bf16 a[108:111], a[188:191], v[72:75], a[108:111]\n"
    "v_exp_f32 v117, v117\n"
    "v_mfma_f32_16x16x32_bf16 a[124:127], a[188:191], v[88:91], a[124:127]\n"
    "s_waitcnt vmcnt(19) lgkmcnt(1)\n"
    "v_exp_f32 v118, v118\n"
    "v_mfma_f32_16x16x32_bf16 a[192:195], v[132:135], v[196:199], a[192:195]\n"
    "s_waitcnt vmcnt(17)\n"
    "v_exp_f32 v119, v119\n"
    "v_mfma_f32_16x16x32_bf16 a[200:203], v[132:135], v[212:215], a[200:203]\n"
    "v_cvt_pk_bf16_f32 v114, v116, v117\n"
    "v_cvt_pk_bf16_f32 v115, v118, v119\n"
    "v_mfma_f32_16x16x32_bf16 a[208:211], v[132:135], v[204:207], a[208:211]\n"
    "s_waitcnt vmcnt(16)\n"
    "v_exp_f32 v104, v104\n"
    "v_mfma_f32_16x16x32_bf16 a[216:219], v[132:135], v[220:223], a[216:219]\n"
    "v_exp_f32 v105, v105\n"
    "v_mfma_f32_16x16x32_bf16 a[196:199], v[140:143], v[196:199], a[196:199]\n"
    "v_exp_f32 v106, v106\n"
    "v_mfma_f32_16x16x32_bf16 a[204:207], v[140:143], v[212:215], a[204:207]\n"
    "v_exp_f32 v107, v107\n"
    "v_mfma_f32_16x16x32_bf16 a[212:215], v[140:143], v[204:207], a[212:215]\n"
    "v_cvt_pk_bf16_f32 v104, v104, v105\n"
    "v_cvt_pk_bf16_f32 v105, v106, v107\n"
    "v_mfma_f32_16x16x32_bf16 a[220:223], v[140:143], v[220:223], a[220:223]\n"
    "v_exp_f32 v108, v108\n"
    "v_mfma_f32_16x16x32_bf16 a[224:227], v[148:151], v[196:199], a[224:227]\n"
    "v_exp_f32 v109, v109\n"
    "v_mfma_f32_16x16x32_bf16 a[232:235], v[148:151], v[212:215], a[232:235]\n"
    "v_exp_f32 v110, v110\n"
    "v_mfma_f32_16x16x32_bf16 a[240:243], v[148:151], v[204:207], a[240:243]\n"
    "v_exp_f32 v111, v111\n"
    "v_mfma_f32_16x16x32_bf16 a[248:251], v[148:151], v[220:223], a[248:251]\n"
    "s_waitcnt lgkmcnt(0)\n"
    "v_cvt_pk_bf16_f32 v106, v108, v109\n"
    "v_cvt_pk_bf16_f32 v107, v110, v111\n"
    "v_mfma_f32_16x16x32_bf16 a[228:231], v[156:159], v[196:199], a[228:231]\n"
    "v_exp_f32 v120, v120\n"
    "v_mfma_f32_16x16x32_bf16 a[236:239], v[156:159], v[212:215], a[236:239]\n"
    "v_exp_f32 v121, v121\n"
    "v_mfma_f32_16x16x32_bf16 a[244:247], v[156:159], v[204:207], a[244:247]\n"
    "v_exp_f32 v122, v122\n"
    "v_mfma_f32_16x16x32_bf16 a[252:255], v[156:159], v[220:223], a[252:255]\n"
    "v_exp_f32 v123, v123\n"
    "v_mfma_f32_16x16x32_bf16 v[64:67], a[128:131], v[128:131], 0\n"
    "v_cvt_pk_bf16_f32 v120, v120, v121\n"
    "v_cvt_pk_bf16_f32 v121, v122, v123\n"
    "v_mfma_f32_16x16x32_bf16 v[80:83], a[128:131], v[144:147], 0\n"
    "ds_read_b64_tr_b16 a[160:161], v9 offset:24576\n"
    "v_mfma_f32_16x16x32_bf16 v[72:75], a[128:131], v[136:139], 0\n"
    "s_add_u32 m0, s89, 0x2000\n"
    "v_exp_f32 v124, v124\n"
    "v_mfma_f32_16x16x32_bf16 v[88:91], a[128:131], v[152:155], 0\n"
    "ds_read_b64_tr_b16 a[162:163], v9 offset:28672\n"
    "v_mfma_f32_16x16x32_bf16 v[64:67], a[132:135], v[160:163], v[64:67]\n"
    "ds_read_b128 a[128:131], v11 offset:36864\n"
    "buffer_load_dwordx4 v29, s[68:71], s94 offen lds\n"
    "v_mfma_f32_16x16x32_bf16 v[80:83], a[132:135], v[176:179], v[80:83]\n"
    "ds_read_b64_tr_b16 a[164:165], v10 offset:24576\n"
    "v_mfma_f32_16x16x32_bf16 v[72:75], a[132:135], v[168:171], v[72:75]\n"
    "v_exp_f32 v125, v125\n"
    "v_mfma_f32_16x16x32_bf16 v[88:91], a[132:135], v[184:187], v[88:91]\n"
    "ds_read_b64_tr_b16 a[166:167], v10 offset:28672\n"
    "v_mfma_f32_16x16x32_bf16 v[64:67], a[136:139], v[192:195], v[64:67]\n"
    "ds_read_b128 a[132:135], v12 offset:36864\n"
    "v_mfma_f32_16x16x32_bf16 v[80:83], a[136:139], v[208:211], v[80:83]\n"
    "ds_read_b64_tr_b16 a[168:169], v9 offset:25088\n"
    "v_mfma_f32_16x16x32_bf16 v[72:75], a[136:139], v[200:203], v[72:75]\n"
    "buffer_load_dwordx4 v30, s[68:71], s94 offen offset:1024 lds\n"
    "v_mfma_f32_16x16x32_bf16 v[88:91], a[136:139], v[216:219], v[88:91]\n"
    "ds_read_b64_tr_b16 a[170:171], v9 offset:29184\n"
    "v_mfma_f32_16x16x32_bf16 v[64:67], a[140:143], v[224:227], v[64:67]\n"
    "ds_read_b128 a[136:139], v13 offset:36864\n"
    "v_mfma_f32_16x16x32_bf16 v[80:83], a[140:143], v[240:243], v[80:83]\n"
    "ds_read_b64_tr_b16 a[172:173], v10 offset:25088\n"
    "v_mfma_f32_16x16x32_bf16 v[72:75], a[140:143], v[232:235], v[72:75]\n"
    "v_exp_f32 v126, v126\n"
    "v_mfma_f32_16x16x32_bf16 v[88:91], a[140:143], v[248:251], v[88:91]\n"
    "s_waitcnt lgkmcnt(4)\n"
    "ds_read_b64_tr_b16 a[174:175], v10 offset:29184\n"
    "v_cvt_pk_bf16_f32 v122, v124, v125\n"
    "v_mfma_f32_16x16x32_bf16 v[68:71], a[128:131], v[128:131], 0\n"
    "ds_read_b128 a[140:143], v14 offset:36864\n"
    "buffer_load_dwordx4 v31, s[68:71], s94 offen offset:2048 lds\n"
    "v_mfma_f32_16x16x32_bf16 v[84:87], a[128:131], v[144:147], 0\n"
    "ds_read_b64_tr_b16 a[176:177], v9 offset:25600\n"
    "v_mfma_f32_16x16x32_bf16 v[76:79], a[128:131], v[136:139], 0\n"
    "v_exp_f32 v127, v127\n"
    "v_mfma_f32_16x16x32_bf16 v[92:95], a[128:131], v[152:155], 0\n"
    "ds_read_b64_tr_b16 a[178:179], v9 offset:29696\n"
    "v_mfma_f32_16x16x32_bf16 v[68:71], a[132:135], v[160:163], v[68:71]\n"
    "v_mfma_f32_16x16x32_bf16 v[84:87], a[132:135], v[176:179], v[84:87]\n"
    "ds_read_b64_tr_b16 a[180:181], v10 offset:25600\n"
    "v_mfma_f32_16x16x32_bf16 v[76:79], a[132:135], v[168:171], v[76:79]\n"
    "buffer_load_dwordx4 v32, s[68:71], s94 offen offset:3072 lds\n"
    "v_mfma_f32_16x16x32_bf16 v[92:95], a[132:135], v[184:187], v[92:95]\n"
    "s_waitcnt lgkmcnt(3)\n"
    "ds_read_b64_tr_b16 a[182:183], v10 offset:29696\n"
    "v_mfma_f32_16x16x32_bf16 v[68:71], a[136:139], v[192:195], v[68:71]\n"
    "s_add_u32 s94, s94, 0x8000\n"
    "v_mfma_f32_16x16x32_bf16 v[84:87], a[136:139], v[208:211], v[84:87]\n"
    "ds_read_b64_tr_b16 a[184:185], v9 offset:26112\n"
    "v_mfma_f32_16x16x32_bf16 v[76:79], a[136:139], v[200:203], v[76:79]\n"
    "v_mfma_f32_16x16x32_bf16 v[92:95], a[136:139], v[216:219], v[92:95]\n"
    "ds_read_b64_tr_b16 a[186:187], v9 offset:30208\n"
    "v_mfma_f32_16x16x32_bf16 v[68:71], a[140:143], v[224:227], v[68:71]\n"
    "buffer_load_dwordx4 v[196:199], v56, s[72:75], s96 offen sc1\n"
    "v_mfma_f32_16x16x32_bf16 v[84:87], a[140:143], v[240:243], v[84:87]\n"
    "ds_read_b64_tr_b16 a[188:189], v10 offset:26112\n"
    "v_mfma_f32_16x16x32_bf16 v[76:79], a[140:143], v[232:235], v[76:79]\n"
    "v_mfma_f32_16x16x32_bf16 v[92:95], a[140:143], v[248:251], v[92:95]\n"
    "ds_read_b64_tr_b16 a[190:191], v10 offset:30208\n"
    "v_cvt_pk_bf16_f32 v123, v126, v127\n"
    "v_mfma_f32_16x16x32_bf16 a[144:147], v[34:37], v[96:99], a[144:147]\n"
    "v_mfma_f32_16x16x32_bf16 a[152:155], v[34:37], v[112:115], a[152:155]\n"
    "v_mfma_f32_16x16x32_bf16 a[148:151], v[34:37], v[104:107], a[148:151]\n"
    "buffer_load_dwordx4 v[204:207], v57, s[72:75], s96 offen sc1\n"
    "v_mfma_f32_16x16x32_bf16 a[156:159], v[34:37], v[120:123], a[156:159]\n"
    "v_exp_f32 v64, v64\n"
    "v_mfma_f32_16x16x32_bf16 a[0:3], a[160:163], v[96:99], a[0:3]\n"
    "v_exp_f32 v65, v65\n"
    "v_mfma_f32_16x16x32_bf16 a[16:19], a[160:163], v[112:115], a[16:19]\n"
    "v_exp_f32 v66, v66\n"
    "v_mfma_f32_16x16x32_bf16 a[8:11], a[160:163], v[104:107], a[8:11]\n"
    "v_exp_f32 v67, v67\n"
    "v_mfma_f32_16x16x32_bf16 a[24:27], a[160:163], v[120:123], a[24:27]\n"
    "v_cvt_pk_bf16_f32 v64, v64, v65\n"
    "v_cvt_pk_bf16_f32 v65, v66, v67\n"
    "v_mfma_f32_16x16x32_bf16 a[4:7], a[164:167], v[96:99], a[4:7]\n"
    "ds_read_b128 a[128:131], v11 offset:40960\n"
    "buffer_load_dwordx4 v[212:215], v56, s[72:75], s96 offen offset:2048 sc1\n"
    "v_mfma_f32_16x16x32_bf16 a[20:23], a[164:167], v[112:115], a[20:23]\n"
    "v_exp_f32 v68, v68\n"
    "v_mfma_f32_16x16x32_bf16 a[12:15], a[164:167], v[104:107], a[12:15]\n"
    "v_exp_f32 v69, v69\n"
    "v_mfma_f32_16x16x32_bf16 a[28:31], a[164:167], v[120:123], a[28:31]\n"
    "ds_read_b128 a[132:135], v12 offset:40960\n"
    "v_mfma_f32_16x16x32_bf16 a[32:35], a[168:171], v[96:99], a[32:35]\n"
    "v_exp_f32 v70, v70\n"
    "v_mfma_f32_16x16x32_bf16 a[48:51], a[168:171], v[112:115], a[48:51]\n"
    "v_exp_f32 v71, v71\n"
    "v_mfma_f32_16x16x32_bf16 a[40:43], a[168:171], v[104:107], a[40:43]\n"
    "ds_read_b128 a[136:139], v13 offset:40960\n"
    "buffer_load_dwordx4 v[220:223], v57, s[72:75], s96 offen offset:2048 sc1\n"
    "s_add_u32 s96, s96, 0x1000\n"
    "v_mfma_f32_16x16x32_bf16 a[56:59], a[168:171], v[120:123], a[56:59]\n"
    "v_cvt_pk_bf16_f32 v66, v68, v69\n"
    "v_cvt_pk_bf16_f32 v67, v70, v71\n"
    "v_mfma_f32_16x16x32_bf16 a[36:39], a[172:175], v[96:99], a[36:39]\n"
    "v_exp_f32 v80, v80\n"
    "v_mfma_f32_16x16x32_bf16 a[52:55], a[172:175], v[112:115], a[52:55]\n"
    "ds_read_b128 a[140:143], v14 offset:40960\n"
    "v_mfma_f32_16x16x32_bf16 a[44:47], a[172:175], v[104:107], a[44:47]\n"
    "v_exp_f32 v81, v81\n"
    "v_mfma_f32_16x16x32_bf16 a[60:63], a[172:175], v[120:123], a[60:63]\n"
    "s_waitcnt lgkmcnt(2)\n"
    "v_exp_f32 v82, v82\n"
    "v_mfma_f32_16x16x32_bf16 a[64:67], a[176:179], v[96:99], a[64:67]\n"
    "v_exp_f32 v83, v83\n"
    "v_mfma_f32_16x16x32_bf16 a[80:83], a[176:179], v[112:115], a[80:83]\n"
    "v_cvt_pk_bf16_f32 v80, v80, v81\n"
    "v_cvt_pk_bf16_f32 v81, v82, v83\n"
    "v_mfma_f32_16x16x32_bf16 a[72:75], a[176:179], v[104:107], a[72:75]\n"
    "ds_read_b64_tr_b16 v[164:165], v40 offset:12288\n"
    "v_mfma_f32_16x16x32_bf16 a[88:91], a[176:179], v[120:123], a[88:91]\n"
    "ds_read_b64_tr_b16 v[166:167], v40 offset:13312\n"
    "v_mfma_f32_16x16x32_bf16 a[68:71], a[180:183], v[96:99], a[68:71]\n"
    "ds_read_b64_tr_b16 v[172:173], v41 offset:12288\n"
    "v_mfma_f32_16x16x32_bf16 a[84:87], a[180:183], v[112:115], a[84:87]\n"
    "ds_read_b64_tr_b16 v[174:175], v41 offset:13312\n"
    "v_mfma_f32_16x16x32_bf16 a[76:79], a[180:183], v[104:107], a[76:79]\n"
    "ds_read_b64_tr_b16 v[180:181], v42 offset:12288\n"
    "v_mfma_f32_16x16x32_bf16 a[92:95], a[180:183], v[120:123], a[92:95]\n"
    "ds_read_b64_tr_b16 v[182:183], v42 offset:13312\n"
    "v_mfma_f32_16x16x32_bf16 a[96:99], a[184:187], v[96:99], a[96:99]\n"
    "ds_read_b64_tr_b16 v[188:189], v43 offset:12288\n"
    "v_mfma_f32_16x16x32_bf16 a[112:115], a[184:187], v[112:115], a[112:115]\n"
    "ds_read_b64_tr_b16 v[190:191], v43 offset:13312\n"
    "v_mfma_f32_16x16x32_bf16 a[104:107], a[184:187], v[104:107], a[104:107]\n"
    "v_exp_f32 v84, v84\n"
    "v_mfma_f32_16x16x32_bf16 a[120:123], a[184:187], v[120:123], a[120:123]\n"
    "v_exp_f32 v85, v85\n"
    "v_mfma_f32_16x16x32_bf16 a[100:103], a[188:191], v[96:99], a[100:103]\n"
    "v_exp_f32 v86, v86\n"
    "v_mfma_f32_16x16x32_bf16 a[116:119], a[188:191], v[112:115], a[116:119]\n"
    "v_exp_f32 v87, v87\n"
    "v_mfma_f32_16x16x32_bf16 a[108:111], a[188:191], v[104:107], a[108:111]\n"
    "v_cvt_pk_bf16_f32 v82, v84, v85\n"
    "v_cvt_pk_bf16_f32 v83, v86, v87\n"
    "v_mfma_f32_16x16x32_bf16 a[124:127], a[188:191], v[120:123], a[124:127]\n"
    "s_waitcnt vmcnt(15) lgkmcnt(1)\n"
    "v_exp_f32 v72, v72\n"
    "v_mfma_f32_16x16x32_bf16 a[192:195], v[164:167], v[228:231], a[192:195]\n"
    "s_waitcnt vmcnt(13)\n"
    "v_exp_f32 v73, v73\n"
    "v_mfma_f32_16x16x32_bf16 a[200:203], v[164:167], v[244:247], a[200:203]\n"
    "v_exp_f32 v74, v74\n"
    "v_mfma_f32_16x16x32_bf16 a[208:211], v[164:167], v[236:239], a[208:211]\n"
    "s_waitcnt vmcnt(12)\n"
    "v_exp_f32 v75, v75\n"
    "v_mfma_f32_16x16x32_bf16 a[216:219], v[164:167], v[252:255], a[216:219]\n"
    "v_cvt_pk_bf16_f32 v72, v72, v73\n"
    "v_cvt_pk_bf16_f32 v73, v74, v75\n"
    "v_mfma_f32_16x16x32_bf16 a[196:199], v[172:175], v[228:231], a[196:199]\n"
    "v_exp_f32 v76, v76\n"
    "v_mfma_f32_16x16x32_bf16 a[204:207], v[172:175], v[244:247], a[204:207]\n"
    "v_exp_f32 v77, v77\n"
    "v_mfma_f32_16x16x32_bf16 a[212:215], v[172:175], v[236:239], a[212:215]\n"
    "v_exp_f32 v78, v78\n"
    "v_mfma_f32_16x16x32_bf16 a[220:223], v[172:175], v[252:255], a[220:223]\n"
    "v_exp_f32 v79, v79\n"
    "v_mfma_f32_16x16x32_bf16 a[224:227], v[180:183], v[228:231], a[224:227]\n"
    "v_cvt_pk_bf16_f32 v74, v76, v77\n"
    "v_cvt_pk_bf16_f32 v75, v78, v79\n"
    "v_mfma_f32_16x16x32_bf16 a[232:235], v[180:183], v[244:247], a[232:235]\n"
    "v_exp_f32 v88, v88\n"
    "v_mfma_f32_16x16x32_bf16 a[240:243], v[180:183], v[236:239], a[240:243]\n"
    "v_exp_f32 v89, v89\n"
    "v_mfma_f32_16x16x32_bf16 a[248:251], v[180:183], v[252:255], a[248:251]\n"
    "v_exp_f32 v90, v90\n"
    "s_waitcnt lgkmcnt(0)\n"
    "v_mfma_f32_16x16x32_bf16 a[228:231], v[188:191], v[228:231], a[228:231]\n"
    "v_exp_f32 v91, v91\n"
    "v_mfma_f32_16x16x32_bf16 a[236:239], v[188:191], v[244:247], a[236:239]\n"
    "v_cvt_pk_bf16_f32 v88, v88, v89\n"
    "v_cvt_pk_bf16_f32 v89, v90, v91\n"
    "v_mfma_f32_16x16x32_bf16 a[244:247], v[188:191], v[236:239], a[244:247]\n"
    "v_exp_f32 v92, v92\n"
    "v_mfma_f32_16x16x32_bf16 a[252:255], v[188:191], v[252:255], a[252:255]\n"
    "v_exp_f32 v93, v93\n"
    "v_mfma_f32_16x16x32_bf16 v[96:99], a[128:131], v[128:131], 0\n"
    "v_exp_f32 v94, v94\n"
    "v_mfma_f32_16x16x32_bf16 v[112:115], a[128:131], v[144:147], 0\n"
    "ds_read_b64_tr_b16 a[160:161], v7\n"
    "v_cvt_pk_bf16_f32 v90, v92, v93\n"
    "v_mfma_f32_16x16x32_bf16 v[104:107], a[128:131], v[136:139], 0\n"
    "s_add_u32 m0, s89, 0x3000\n"
    "v_exp_f32 v95, v95\n"
    "v_mfma_f32_16x16x32_bf16 v[120:123], a[128:131], v[152:155], 0\n"
    "ds_read_b64_tr_b16 a[162:163], v7 offset:4096\n"
    "v_mfma_f32_16x16x32_bf16 v[96:99], a[132:135], v[160:163], v[96:99]\n"
    "ds_read_b128 a[128:131], v11 offset:45056\n"
    "buffer_load_dwordx4 v29, s[68:71], s94 offen lds\n"
    "v_mfma_f32_16x16x32_bf16 v[112:115], a[132:135], v[176:179], v[112:115]\n"
    "ds_read_b64_tr_b16 a[164:165], v8\n"
    "v_mfma_f32_16x16x32_bf16 v[104:107], a[132:135], v[168:171], v[104:107]\n"
    "v_mfma_f32_16x16x32_bf16 v[120:123], a[132:135], v[184:187], v[120:123]\n"
    "ds_read_b64_tr_b16 a[166:167], v8 offset:4096\n"
    "v_mfma_f32_16x16x32_bf16 v[96:99], a[136:139], v[192:195], v[96:99]\n"
    "ds_read_b128 a[132:135], v12 offset:45056\n"
    "v_mfma_f32_16x16x32_bf16 v[112:115], a[136:139], v[208:211], v[112:115]\n"
    "ds_read_b64_tr_b16 a[168:169], v7 offset:512\n"
    "v_mfma_f32_16x16x32_bf16 v[104:107], a[136:139], v[200:203], v[104:107]\n"
    "buffer_load_dwordx4 v30, s[68:71], s94 offen offset:1024 lds\n"
    "v_mfma_f32_16x16x32_bf16 v[120:123], a[136:139], v[216:219], v[120:123]\n"
    "ds_read_b64_tr_b16 a[170:171], v7 offset:4608\n"
    "v_mfma_f32_16x16x32_bf16 v[96:99], a[140:143], v[224:227], v[96:99]\n"
    "ds_read_b128 a[136:139], v13 offset:45056\n"
    "v_mfma_f32_16x16x32_bf16 v[112:115], a[140:143], v[240:243], v[112:115]\n"
    "ds_read_b64_tr_b16 a[172:173], v8 offset:512\n"
    "v_mfma_f32_16x16x32_bf16 v[104:107], a[140:143], v[232:235], v[104:107]\n"
    "v_mfma_f32_16x16x32_bf16 v[120:123], a[140:143], v[248:251], v[120:123]\n"
    "s_waitcnt lgkmcnt(5)\n"
    "ds_read_b64_tr_b16 a[174:175], v8 offset:4608\n"
    "v_mfma_f32_16x16x32_bf16 v[100:103], a[128:131], v[128:131], 0\n"
    "ds_read_b128 a[140:143], v14 offset:45056\n"
    "buffer_load_dwordx4 v31, s[68:71], s94 offen offset:2048 lds\n"
    "v_mfma_f32_16x16x32_bf16 v[116:119], a[128:131], v[144:147], 0\n"
    "ds_read_b64_tr_b16 a[176:177], v7 offset:1024\n"
    "v_mfma_f32_16x16x32_bf16 v[108:111], a[128:131], v[136:139], 0\n"
    "v_mfma_f32_16x16x32_bf16 v[124:127], a[128:131], v[152:155], 0\n"
    "ds_read_b64_tr_b16 a[178:179], v7 offset:5120\n"
    "s_waitcnt lgkmcnt(5)\n"
    "v_mfma_f32_16x16x32_bf16 v[100:103], a[132:135], v[160:163], v[100:103]\n"
    "v_mfma_f32_16x16x32_bf16 v[116:119], a[132:135], v[176:179], v[116:119]\n"
    "ds_read_b64_tr_b16 a[180:181], v8 offset:1024\n"
    "v_mfma_f32_16x16x32_bf16 v[108:111], a[132:135], v[168:171], v[108:111]\n"
    "buffer_load_dwordx4 v32, s[68:71], s94 offen offset:3072 lds\n"
    "v_mfma_f32_16x16x32_bf16 v[124:127], a[132:135], v[184:187], v[124:127]\n"
    "ds_read_b64_tr_b16 a[182:183], v8 offset:5120\n"
    "v_mfma_f32_16x16x32_bf16 v[100:103], a[136:139], v[192:195], v[100:103]\n"
    "s_add_u32 s94, s94, 0x8000\n"
    "v_mfma_f32_16x16x32_bf16 v[116:119], a[136:139], v[208:211], v[116:119]\n"
    "ds_read_b64_tr_b16 a[184:185], v7 offset:1536\n"
    "v_mfma_f32_16x16x32_bf16 v[108:111], a[136:139], v[200:203], v[108:111]\n"
    "v_mfma_f32_16x16x32_bf16 v[124:127], a[136:139], v[216:219], v[124:127]\n"
    "ds_read_b64_tr_b16 a[186:187], v7 offset:5632\n"
    "s_waitcnt lgkmcnt(4)\n"
    "v_mfma_f32_16x16x32_bf16 v[100:103], a[140:143], v[224:227], v[100:103]\n"
    "buffer_load_dwordx4 v[228:231], v56, s[72:75], s96 offen sc1\n"
    "v_mfma_f32_16x16x32_bf16 v[116:119], a[140:143], v[240:243], v[116:119]\n"
    "ds_read_b64_tr_b16 a[188:189], v8 offset:1536\n"
    "v_mfma_f32_16x16x32_bf16 v[108:111], a[140:143], v[232:235], v[108:111]\n"
    "v_mfma_f32_16x16x32_bf16 v[124:127], a[140:143], v[248:251], v[124:127]\n"
    "ds_read_b64_tr_b16 a[190:191], v8 offset:5632\n"
    "v_cvt_pk_bf16_f32 v91, v94, v95\n"
    "v_mfma_f32_16x16x32_bf16 a[144:147], v[34:37], v[64:67], a[144:147]\n"
    "v_mfma_f32_16x16x32_bf16 a[152:155], v[34:37], v[80:83], a[152:155]\n"
    "v_mfma_f32_16x16x32_bf16 a[148:151], v[34:37], v[72:75], a[148:151]\n"
    "buffer_load_dwordx4 v[236:239], v57, s[72:75], s96 offen sc1\n"
    "v_mfma_f32_16x16x32_bf16 a[156:159], v[34:37], v[88:91], a[156:159]\n"
    "v_exp_f32 v96, v96\n"
    "s_waitcnt vmcnt(14)\n"
    "s_barrier\n"
    "v_mfma_f32_16x16x32_bf16 a[0:3], a[160:163], v[64:67], a[0:3]\n"
    "v_mfma_f32_16x16x32_bf16 a[16:19], a[160:163], v[80:83], a[16:19]\n"
    "v_exp_f32 v97, v97\n"
    "v_mfma_f32_16x16x32_bf16 a[8:11], a[160:163], v[72:75], a[8:11]\n"
    "v_exp_f32 v98, v98\n"
    "v_mfma_f32_16x16x32_bf16 a[24:27], a[160:163], v[88:91], a[24:27]\n"
    "v_exp_f32 v99, v99\n"
    "v_mfma_f32_16x16x32_bf16 a[4:7], a[164:167], v[64:67], a[4:7]\n"
    "ds_read_b128 a[128:131], v11\n"
    "buffer_load_dwordx4 v[244:247], v56, s[72:75], s96 offen offset:2048 sc1\n"
    "v_mfma_f32_16x16x32_bf16 a[20:23], a[164:167], v[80:83], a[20:23]\n"
    "v_cvt_pk_bf16_f32 v96, v96, v97\n"
    "v_cvt_pk_bf16_f32 v97, v98, v99\n"
    "v_mfma_f32_16x16x32_bf16 a[12:15], a[164:167], v[72:75], a[12:15]\n"
    "v_exp_f32 v100, v100\n"
    "v_mfma_f32_16x16x32_bf16 a[28:31], a[164:167], v[88:91], a[28:31]\n"
    "ds_read_b128 a[132:135], v12\n"
    "v_mfma_f32_16x16x32_bf16 a[32:35], a[168:171], v[64:67], a[32:35]\n"
    "v_exp_f32 v101, v101\n"
    "v_mfma_f32_16x16x32_bf16 a[48:51], a[168:171], v[80:83], a[48:51]\n"
    "v_exp_f32 v102, v102\n"
    "v_mfma_f32_16x16x32_bf16 a[40:43], a[168:171], v[72:75], a[40:43]\n"
    "ds_read_b128 a[136:139], v13\n"
    "buffer_load_dwordx4 v[252:255], v57, s[72:75], s96 offen offset:2048 sc1\n"
    "s_add_u32 s96, s96, 0x1000\n"
    "v_mfma_f32_16x16x32_bf16 a[56:59], a[168:171], v[88:91], a[56:59]\n"
    "v_exp_f32 v103, v103\n"
    "v_mfma_f32_16x16x32_bf16 a[36:39], a[172:175], v[64:67], a[36:39]\n"
    "v_cvt_pk_bf16_f32 v98, v100, v101\n"
    "v_cvt_pk_bf16_f32 v99, v102, v103\n"
    "v_mfma_f32_16x16x32_bf16 a[52:55], a[172:175], v[80:83], a[52:55]\n"
    "ds_read_b128 a[140:143], v14\n"
    "v_mfma_f32_16x16x32_bf16 a[44:47], a[172:175], v[72:75], a[44:47]\n"
    "v_exp_f32 v112, v112\n"
    "v_mfma_f32_16x16x32_bf16 a[60:63], a[172:175], v[88:91], a[60:63]\n"
    "v_exp_f32 v113, v113\n"
    "v_mfma_f32_16x16x32_bf16 a[64:67], a[176:179], v[64:67], a[64:67]\n"
    "v_exp_f32 v114, v114\n"
    "v_mfma_f32_16x16x32_bf16 a[80:83], a[176:179], v[80:83], a[80:83]\n"
    "v_exp_f32 v115, v115\n"
    "v_mfma_f32_16x16x32_bf16 a[72:75], a[176:179], v[72:75], a[72:75]\n"
    "ds_read_b64_tr_b16 v[132:133], v40\n"
    "v_cvt_pk_bf16_f32 v112, v112, v113\n"
    "v_mfma_f32_16x16x32_bf16 a[88:91], a[176:179], v[88:91], a[88:91]\n"
    "ds_read_b64_tr_b16 v[134:135], v40 offset:1024\n"
    "v_cvt_pk_bf16_f32 v113, v114, v115\n"
    "s_waitcnt lgkmcnt(2)\n"
    "v_mfma_f32_16x16x32_bf16 a[68:71], a[180:183], v[64:67], a[68:71]\n"
    "ds_read_b64_tr_b16 v[140:141], v41\n"
    "v_mfma_f32_16x16x32_bf16 a[84:87], a[180:183], v[80:83], a[84:87]\n"
    "ds_read_b64_tr_b16 v[142:143], v41 offset:1024\n"
    "v_mfma_f32_16x16x32_bf16 a[76:79], a[180:183], v[72:75], a[76:79]\n"
    "ds_read_b64_tr_b16 v[148:149], v42\n"
    "v_mfma_f32_16x16x32_bf16 a[92:95], a[180:183], v[88:91], a[92:95]\n"
    "ds_read_b64_tr_b16 v[150:151], v42 offset:1024\n"
    "v_mfma_f32_16x16x32_bf16 a[96:99], a[184:187], v[64:67], a[96:99]\n"
    "ds_read_b64_tr_b16 v[156:157], v43\n"
    "v_mfma_f32_16x16x32_bf16 a[112:115], a[184:187], v[80:83], a[112:115]\n"
    "ds_read_b64_tr_b16 v[158:159], v43 offset:1024\n"
    "v_mfma_f32_16x16x32_bf16 a[104:107], a[184:187], v[72:75], a[104:107]\n"
    "v_exp_f32 v116, v116\n"
    "v_mfma_f32_16x16x32_bf16 a[120:123], a[184:187], v[88:91], a[120:123]\n"
    "v_exp_f32 v117, v117\n"
    "v_mfma_f32_16x16x32_bf16 a[100:103], a[188:191], v[64:67], a[100:103]\n"
    "v_exp_f32 v118, v118\n"
    "v_mfma_f32_16x16x32_bf16 a[116:119], a[188:191], v[80:83], a[116:119]\n"
    "v_exp_f32 v119, v119\n"
    "v_mfma_f32_16x16x32_bf16 a[108:111], a[188:191], v[72:75], a[108:111]\n"
    "v_cvt_pk_bf16_f32 v114, v116, v117\n"
    "v_cvt_pk_bf16_f32 v115, v118, v119\n"
    "v_mfma_f32_16x16x32_bf16 a[124:127], a[188:191], v[88:91], a[124:127]\n"
    "s_waitcnt vmcnt(11) lgkmcnt(1)\n"
    "v_exp_f32 v104, v104\n"
    "v_mfma_f32_16x16x32_bf16 a[192:195], v[132:135], v[196:199], a[192:195]\n"
    "s_waitcnt vmcnt(9)\n"
    "v_exp_f32 v105, v105\n"
    "v_mfma_f32_16x16x32_bf16 a[200:203], v[132:135], v[212:215], a[200:203]\n"
    "v_exp_f32 v106, v106\n"
    "v_mfma_f32_16x16x32_bf16 a[208:211], v[132:135], v[204:207], a[208:211]\n"
    "s_waitcnt vmcnt(8)\n"
    "v_exp_f32 v107, v107\n"
    "v_mfma_f32_16x16x32_bf16 a[216:219], v[132:135], v[220:223], a[216:219]\n"
    "v_cvt_pk_bf16_f32 v104, v104, v105\n"
    "v_cvt_pk_bf16_f32 v105, v106, v107\n"
    "v_mfma_f32_16x16x32_bf16 a[196:199], v[140:143], v[196:199], a[196:199]\n"
    "v_exp_f32 v108, v108\n"
    "v_mfma_f32_16x16x32_bf16 a[204:207], v[140:143], v[212:215], a[204:207]\n"
    "v_exp_f32 v109, v109\n"
    "v_mfma_f32_16x16x32_bf16 a[212:215], v[140:143], v[204:207], a[212:215]\n"
    "v_exp_f32 v110, v110\n"
    "v_mfma_f32_16x16x32_bf16 a[220:223], v[140:143], v[220:223], a[220:223]\n"
    "v_exp_f32 v111, v111\n"
    "v_mfma_f32_16x16x32_bf16 a[224:227], v[148:151], v[196:199], a[224:227]\n"
    "v_cvt_pk_bf16_f32 v106, v108, v109\n"
    "v_cvt_pk_bf16_f32 v107, v110, v111\n"
    "v_mfma_f32_16x16x32_bf16 a[232:235], v[148:151], v[212:215], a[232:235]\n"
    "v_exp_f32 v120, v120\n"
    "v_mfma_f32_16x16x32_bf16 a[240:243], v[148:151], v[204:207], a[240:243]\n"
    "v_exp_f32 v121, v121\n"
    "v_mfma_f32_16x16x32_bf16 a[248:251], v[148:151], v[220:223], a[248:251]\n"
    "v_exp_f32 v122, v122\n"
    "s_waitcnt lgkmcnt(0)\n"
    "v_mfma_f32_16x16x32_bf16 a[228:231], v[156:159], v[196:199], a[228:231]\n"
    "v_exp_f32 v123, v123\n"
    "v_mfma_f32_16x16x32_bf16 a[236:239], v[156:159], v[212:215], a[236:239]\n"
    "v_cvt_pk_bf16_f32 v120, v120, v121\n"
    "v_cvt_pk_bf16_f32 v121, v122, v123\n"
    "v_mfma_f32_16x16x32_bf16 a[244:247], v[156:159], v[204:207], a[244:247]\n"
    "v_exp_f32 v124, v124\n"
    "v_mfma_f32_16x16x32_bf16 a[252:255], v[156:159], v[220:223], a[252:255]\n"
    "v_exp_f32 v125, v125\n"
    "v_mfma_f32_16x16x32_bf16 v[64:67], a[128:131], v[128:131], 0\n"
    "v_exp_f32 v126, v126\n"
    "v_mfma_f32_16x16x32_bf16 v[80:83], a[128:131], v[144:147], 0\n"
    "ds_read_b64_tr_b16 a[160:161], v7 offset:8192\n"
    "v_cvt_pk_bf16_f32 v122, v124, v125\n"
    "v_mfma_f32_16x16x32_bf16 v[72:75], a[128:131], v[136:139], 0\n"
    "s_add_u32 m0, s89, 0\n"
    "v_exp_f32 v127, v127\n"
    "v_mfma_f32_16x16x32_bf16 v[88:91], a[128:131], v[152:155], 0\n"
    "ds_read_b64_tr_b16 a[162:163], v7 offset:12288\n"
    "v_mfma_f32_16x16x32_bf16 v[64:67], a[132:135], v[160:163], v[64:67]\n"
    "ds_read_b128 a[128:131], v11 offset:4096\n"
    "buffer_load_dwordx4 v29, s[68:71], s94 offen lds\n"
    "v_mfma_f32_16x16x32_bf16 v[80:83], a[132:135], v[176:179], v[80:83]\n"
    "ds_read_b64_tr_b16 a[164:165], v8 offset:8192\n"
    "v_mfma_f32_16x16x32_bf16 v[72:75], a[132:135], v[168:171], v[72:75]\n"
    "v_mfma_f32_16x16x32_bf16 v[88:91], a[132:135], v[184:187], v[88:91]\n"
    "ds_read_b64_tr_b16 a[166:167], v8 offset:12288\n"
    "v_mfma_f32_16x16x32_bf16 v[64:67], a[136:139], v[192:195], v[64:67]\n"
    "ds_read_b128 a[132:135], v12 offset:4096\n"
    "v_mfma_f32_16x16x32_bf16 v[80:83], a[136:139], v[208:211], v[80:83]\n"
    "ds_read_b64_tr_b16 a[168:169], v7 offset:8704\n"
    "v_mfma_f32_16x16x32_bf16 v[72:75], a[136:139], v[200:203], v[72:75]\n"
    "buffer_load_dwordx4 v30, s[68:71], s94 offen offset:1024 lds\n"
    "v_mfma_f32_16x16x32_bf16 v[88:91], a[136:139], v[216:219], v[88:91]\n"
    "ds_read_b64_tr_b16 a[170:171], v7 offset:12800\n"
    "v_mfma_f32_16x16x32_bf16 v[64:67], a[140:143], v[224:227], v[64:67]\n"
    "ds_read_b128 a[136:139], v13 offset:4096\n"
    "v_mfma_f32_16x16x32_bf16 v[80:83], a[140:143], v[240:243], v[80:83]\n"
    "ds_read_b64_tr_b16 a[172:173], v8 offset:8704\n"
    "v_mfma_f32_16x16x32_bf16 v[72:75], a[140:143], v[232:235], v[72:75]\n"
    "v_mfma_f32_16x16x32_bf16 v[88:91], a[140:143], v[248:251], v[88:91]\n"
    "s_waitcnt lgkmcnt(5)\n"
    "ds_read_b64_tr_b16 a[174:175], v8 offset:12800\n"
    "v_mfma_f32_16x16x32_bf16 v[68:71], a[128:131], v[128:131], 0\n"
    "ds_read_b128 a[140:143], v14 offset:4096\n"
    "buffer_load_dwordx4 v31, s[68:71], s94 offen offset:2048 lds\n"
    "v_mfma_f32_16x16x32_bf16 v[84:87], a[128:131], v[144:147], 0\n"
    "ds_read_b64_tr_b16 a[176:177], v7 offset:9216\n"
    "v_mfma_f32_16x16x32_bf16 v[76:79], a[128:131], v[136:139], 0\n"
    "v_mfma_f32_16x16x32_bf16 v[92:95], a[128:131], v[152:155], 0\n"
    "ds_read_b64_tr_b16 a[178:179], v7 offset:13312\n"
    "s_waitcnt lgkmcnt(5)\n"
    "v_mfma_f32_16x16x32_bf16 v[68:71], a[132:135], v[160:163], v[68:71]\n"
    "v_mfma_f32_16x16x32_bf16 v[84:87], a[132:135], v[176:179], v[84:87]\n"
    "ds_read_b64_tr_b16 a[180:181], v8 offset:9216\n"
    "v_mfma_f32_16x16x32_bf16 v[76:79], a[132:135], v[168:171], v[76:79]\n"
    "buffer_load_dwordx4 v32, s[68:71], s94 offen offset:3072 lds\n"
    "v_mfma_f32_16x16x32_bf16 v[92:95], a[132:135], v[184:187], v[92:95]\n"
    "ds_read_b64_tr_b16 a[182:183], v8 offset:13312\n"
    "v_mfma_f32_16x16x32_bf16 v[68:71], a[136:139], v[192:195], v[68:71]\n"
    "s_add_u32 s94, s94, 0x8000\n"
    "v_mfma_f32_16x16x32_bf16 v[84:87], a[136:139], v[208:211], v[84:87]\n"
    "ds_read_b64_tr_b16 a[184:185], v7 offset:9728\n"
    "v_mfma_f32_16x16x32_bf16 v[76:79], a[136:139], v[200:203], v[76:79]\n"
    "v_mfma_f32_16x16x32_bf16 v[92:95], a[136:139], v[216:219], v[92:95]\n"
    "ds_read_b64_tr_b16 a[186:187], v7 offset:13824\n"
    "s_waitcnt lgkmcnt(4)\n"
    "v_mfma_f32_16x16x32_bf16 v[68:71], a[140:143], v[224:227], v[68:71]\n"
    "buffer_load_dwordx4 v[196:199], v56, s[72:75], s96 offen sc1\n"
    "v_mfma_f32_16x16x32_bf16 v[84:87], a[140:143], v[240:243], v[84:87]\n"
    "ds_read_b64_tr_b16 a[188:189], v8 offset:9728\n"
    "v_mfma_f32_16x16x32_bf16 v[76:79], a[140:143], v[232:235], v[76:79]\n"
    "v_mfma_f32_16x16x32_bf16 v[92:95], a[140:143], v[248:251], v[92:95]\n"
    "ds_read_b64_tr_b16 a[190:191], v8 offset:13824\n"
    "v_cvt_pk_bf16_f32 v123, v126, v127\n"
    "v_mfma_f32_16x16x32_bf16 a[144:147], v[34:37], v[96:99], a[144:147]\n"
    "v_mfma_f32_16x16x32_bf16 a[152:155], v[34:37], v[112:115], a[152:155]\n"
    "v_mfma_f32_16x16x32_bf16 a[148:151], v[34:37], v[104:107], a[148:151]\n"
    "buffer_load_dwordx4 v[204:207], v57, s[72:75], s96 offen sc1\n"
    "v_mfma_f32_16x16x32_bf16 a[156:159], v[34:37], v[120:123], a[156:159]\n"
    "v_exp_f32 v64, v64\n"
    "v_mfma_f32_16x16x32_bf16 a[0:3], a[160:163], v[96:99], a[0:3]\n"
    "v_exp_f32 v65, v65\n"
    "v_mfma_f32_16x16x32_bf16 a[16:19], a[160:163], v[112:115], a[16:19]\n"
    "v_exp_f32 v66, v66\n"
    "v_mfma_f32_16x16x32_bf16 a[8:11], a[160:163], v[104:107], a[8:11]\n"
    "v_exp_f32 v67, v67\n"
    "v_mfma_f32_16x16x32_bf16 a[24:27], a[160:163], v[120:123], a[24:27]\n"
    "v_cvt_pk_bf16_f32 v64, v64, v65\n"
    "v_cvt_pk_bf16_f32 v65, v66, v67\n"
    "v_mfma_f32_16x16x32_bf16 a[4:7], a[164:167], v[96:99], a[4:7]\n"
    "ds_read_b128 a[128:131], v11 offset:8192\n"
    "buffer_load_dwordx4 v[212:215], v56, s[72:75], s96 offen offset:2048 sc1\n"
    "v_mfma_f32_16x16x32_bf16 a[20:23], a[164:167], v[112:115], a[20:23]\n"
    "v_exp_f32 v68, v68\n"
    "v_mfma_f32_16x16x32_bf16 a[12:15], a[164:167], v[104:107], a[12:15]\n"
    "v_exp_f32 v69, v69\n"
    "v_mfma_f32_16x16x32_bf16 a[28:31], a[164:167], v[120:123], a[28:31]\n"
    "ds_read_b128 a[132:135], v12 offset:8192\n"
    "v_mfma_f32_16x16x32_bf16 a[32:35], a[168:171], v[96:99], a[32:35]\n"
    "v_exp_f32 v70, v70\n"
    "v_mfma_f32_16x16x32_bf16 a[48:51], a[168:171], v[112:115], a[48:51]\n"
    "v_exp_f32 v71, v71\n"
    "v_mfma_f32_16x16x32_bf16 a[40:43], a[168:171], v[104:107], a[40:43]\n"
    "ds_read_b128 a[136:139], v13 offset:8192\n"
    "buffer_load_dwordx4 v[220:223], v57, s[72:75], s96 offen offset:2048 sc1\n"
    "s_add_u32 s96, s96, 0x1000\n"
    "v_mfma_f32_16x16x32_bf16 a[56:59], a[168:171], v[120:123], a[56:59]\n"
    "v_cvt_pk_bf16_f32 v66, v68, v69\n"
    "v_cvt_pk_bf16_f32 v67, v70, v71\n"
    "v_mfma_f32_16x16x32_bf16 a[36:39], a[172:175], v[96:99], a[36:39]\n"
    "v_exp_f32 v80, v80\n"
    "v_mfma_f32_16x16x32_bf16 a[52:55], a[172:175], v[112:115], a[52:55]\n"
    "ds_read_b128 a[140:143], v14 offset:8192\n"
    "v_mfma_f32_16x16x32_bf16 a[44:47], a[172:175], v[104:107], a[44:47]\n"
    "v_exp_f32 v81, v81\n"
    "v_mfma_f32_16x16x32_bf16 a[60:63], a[172:175], v[120:123], a[60:63]\n"
    "v_exp_f32 v82, v82\n"
    "v_mfma_f32_16x16x32_bf16 a[64:67], a[176:179], v[96:99], a[64:67]\n"
    "v_exp_f32 v83, v83\n"
    "v_mfma_f32_16x16x32_bf16 a[80:83], a[176:179], v[112:115], a[80:83]\n"
    "v_cvt_pk_bf16_f32 v80, v80, v81\n"
    "v_cvt_pk_bf16_f32 v81, v82, v83\n"
    "v_mfma_f32_16x16x32_bf16 a[72:75], a[176:179], v[104:107], a[72:75]\n"
    "ds_read_b64_tr_b16 v[164:165], v40 offset:4096\n"
    "v_mfma_f32_16x16x32_bf16 a[88:91], a[176:179], v[120:123], a[88:91]\n"
    "ds_read_b64_tr_b16 v[166:167], v40 offset:5120\n"
    "s_waitcnt lgkmcnt(2)\n"
    "v_mfma_f32_16x16x32_bf16 a[68:71], a[180:183], v[96:99], a[68:71]\n"
    "ds_read_b64_tr_b16 v[172:173], v41 offset:4096\n"
    "v_mfma_f32_16x16x32_bf16 a[84:87], a[180:183], v[112:115], a[84:87]\n"
    "ds_read_b64_tr_b16 v[174:175], v41 offset:5120\n"
    "v_mfma_f32_16x16x32_bf16 a[76:79], a[180:183], v[104:107], a[76:79]\n"
    "ds_read_b64_tr_b16 v[180:181], v42 offset:4096\n"
    "v_mfma_f32_16x16x32_bf16 a[92:95], a[180:183], v[120:123], a[92:95]\n"
    "ds_read_b64_tr_b16 v[182:183], v42 offset:5120\n"
    "v_mfma_f32_16x16x32_bf16 a[96:99], a[184:187], v[96:99], a[96:99]\n"
    "ds_read_b64_tr_b16 v[188:189], v43 offset:4096\n"
    "v_mfma_f32_16x16x32_bf16 a[112:115], a[184:187], v[112:115], a[112:115]\n"
    "ds_read_b64_tr_b16 v[190:191], v43 offset:5120\n"
    "v_mfma_f32_16x16x32_bf16 a[104:107], a[184:187], v[104:107], a[104:107]\n"
    "v_exp_f32 v84, v84\n"
    "v_mfma_f32_16x16x32_bf16 a[120:123], a[184:187], v[120:123], a[120:123]\n"
    "v_exp_f32 v85, v85\n"
    "v_mfma_f32_16x16x32_bf16 a[100:103], a[188:191], v[96:99], a[100:103]\n"
    "v_exp_f32 v86, v86\n"
    "v_mfma_f32_16x16x32_bf16 a[116:119], a[188:191], v[112:115], a[116:119]\n"
    "v_exp_f32 v87, v87\n"
    "v_mfma_f32_16x16x32_bf16 a[108:111], a[188:191], v[104:107], a[108:111]\n"
    "v_cvt_pk_bf16_f32 v82, v84, v85\n"
    "v_cvt_pk_bf16_f32 v83, v86, v87\n"
    "v_mfma_f32_16x16x32_bf16 a[124:127], a[188:191], v[120:123], a[124:127]\n"
    "s_waitcnt vmcnt(11) lgkmcnt(1)\n"
    "v_exp_f32 v72, v72\n"
    "v_mfma_f32_16x16x32_bf16 a[192:195], v[164:167], v[228:231], a[192:195]\n"
    "s_waitcnt vmcnt(9)\n"
    "v_exp_f32 v73, v73\n"
    "v_mfma_f32_16x16x32_bf16 a[200:203], v[164:167], v[244:247], a[200:203]\n"
    "v_exp_f32 v74, v74\n"
    "v_mfma_f32_16x16x32_bf16 a[208:211], v[164:167], v[236:239], a[208:211]\n"
    "s_waitcnt vmcnt(8)\n"
    "v_exp_f32 v75, v75\n"
    "v_mfma_f32_16x16x32_bf16 a[216:219], v[164:167], v[252:255], a[216:219]\n"
    "v_cvt_pk_bf16_f32 v72, v72, v73\n"
    "v_cvt_pk_bf16_f32 v73, v74, v75\n"
    "v_mfma_f32_16x16x32_bf16 a[196:199], v[172:175], v[228:231], a[196:199]\n"
    "v_exp_f32 v76, v76\n"
    "v_mfma_f32_16x16x32_bf16 a[204:207], v[172:175], v[244:247], a[204:207]\n"
    "v_exp_f32 v77, v77\n"
    "v_mfma_f32_16x16x32_bf16 a[212:215], v[172:175], v[236:239], a[212:215]\n"
    "v_exp_f32 v78, v78\n"
    "v_mfma_f32_16x16x32_bf16 a[220:223], v[172:175], v[252:255], a[220:223]\n"
    "v_exp_f32 v79, v79\n"
    "v_mfma_f32_16x16x32_bf16 a[224:227], v[180:183], v[228:231], a[224:227]\n"
    "v_cvt_pk_bf16_f32 v74, v76, v77\n"
    "v_cvt_pk_bf16_f32 v75, v78, v79\n"
    "v_mfma_f32_16x16x32_bf16 a[232:235], v[180:183], v[244:247], a[232:235]\n"
    "v_exp_f32 v88, v88\n"
    "v_mfma_f32_16x16x32_bf16 a[240:243], v[180:183], v[236:239], a[240:243]\n"
    "v_exp_f32 v89, v89\n"
    "v_mfma_f32_16x16x32_bf16 a[248:251], v[180:183], v[252:255], a[248:251]\n"
    "v_exp_f32 v90, v90\n"
    "s_waitcnt lgkmcnt(0)\n"
    "v_mfma_f32_16x16x32_bf16 a[228:231], v[188:191], v[228:231], a[228:231]\n"
    "v_exp_f32 v91, v91\n"
    "v_mfma_f32_16x16x32_bf16 a[236:239], v[188:191], v[244:247], a[236:239]\n"
    "v_cvt_pk_bf16_f32 v88, v88, v89\n"
    "v_cvt_pk_bf16_f32 v89, v90, v91\n"
    "v_mfma_f32_16x16x32_bf16 a[244:247], v[188:191], v[236:239], a[244:247]\n"
    "v_exp_f32 v92, v92\n"
    "v_mfma_f32_16x16x32_bf16 a[252:255], v[188:191], v[252:255], a[252:255]\n"
    "v_exp_f32 v93, v93\n"
    "v_mfma_f32_16x16x32_bf16 v[96:99], a[128:131], v[128:131], 0\n"
    "v_exp_f32 v94, v94\n"
    "v_mfma_f32_16x16x32_bf16 v[112:115], a[128:131], v[144:147], 0\n"
    "ds_read_b64_tr_b16 a[160:161], v9\n"
    "v_cvt_pk_bf16_f32 v90, v92, v93\n"
    "v_mfma_f32_16x16x32_bf16 v[104:107], a[128:131], v[136:139], 0\n"
    "s_add_u32 m0, s89, 0x1000\n"
    "v_exp_f32 v95, v95\n"
    "v_mfma_f32_16x16x32_bf16 v[120:123], a[128:131], v[152:155], 0\n"
    "ds_read_b64_tr_b16 a[162:163], v9 offset:4096\n"
    "v_mfma_f32_16x16x32_bf16 v[96:99], a[132:135], v[160:163], v[96:99]\n"
    "ds_read_b128 a[128:131], v11 offset:12288\n"
    "buffer_load_dwordx4 v29, s[68:71], s94 offen lds\n"
    "v_mfma_f32_16x16x32_bf16 v[112:115], a[132:135], v[176:179], v[112:115]\n"
    "ds_read_b64_tr_b16 a[164:165], v10\n"
    "v_mfma_f32_16x16x32_bf16 v[104:107], a[132:135], v[168:171], v[104:107]\n"
    "v_mfma_f32_16x16x32_bf16 v[120:123], a[132:135], v[184:187], v[120:123]\n"
    "ds_read_b64_tr_b16 a[166:167], v10 offset:4096\n"
    "v_mfma_f32_16x16x32_bf16 v[96:99], a[136:139], v[192:195], v[96:99]\n"
    "ds_read_b128 a[132:135], v12 offset:12288\n"
    "v_mfma_f32_16x16x32_bf16 v[112:115], a[136:139], v[208:211], v[112:115]\n"
    "ds_read_b64_tr_b16 a[168:169], v9 offset:512\n"
    "v_mfma_f32_16x16x32_bf16 v[104:107], a[136:139], v[200:203], v[104:107]\n"
    "buffer_load_dwordx4 v30, s[68:71], s94 offen offset:1024 lds\n"
    "v_mfma_f32_16x16x32_bf16 v[120:123], a[136:139], v[216:219], v[120:123]\n"
    "ds_read_b64_tr_b16 a[170:171], v9 offset:4608\n"
    "v_mfma_f32_16x16x32_bf16 v[96:99], a[140:143], v[224:227], v[96:99]\n"
    "ds_read_b128 a[136:139], v13 offset:12288\n"
    "v_mfma_f32_16x16x32_bf16 v[112:115], a[140:143], v[240:243], v[112:115]\n"
    "ds_read_b64_tr_b16 a[172:173], v10 offset:512\n"
    "v_mfma_f32_16x16x32_bf16 v[104:107], a[140:143], v[232:235], v[104:107]\n"
    "v_mfma_f32_16x16x32_bf16 v[120:123], a[140:143], v[248:251], v[120:123]\n"
    "s_waitcnt lgkmcnt(5)\n"
    "ds_read_b64_tr_b16 a[174:175], v10 offset:4608\n"
    "v_mfma_f32_16x16x32_bf16 v[100:103], a[128:131], v[128:131], 0\n"
    "ds_read_b128 a[140:143], v14 offset:12288\n"
    "buffer_load_dwordx4 v31, s[68:71], s94 offen offset:2048 lds\n"
    "v_mfma_f32_16x16x32_bf16 v[116:119], a[128:131], v[144:147], 0\n"
    "ds_read_b64_tr_b16 a[176:177], v9 offset:1024\n"
    "v_mfma_f32_16x16x32_bf16 v[108:111], a[128:131], v[136:139], 0\n"
    "v_mfma_f32_16x16x32_bf16 v[124:127], a[128:131], v[152:155], 0\n"
    "ds_read_b64_tr_b16 a[178:179], v9 offset:5120\n"
    "s_waitcnt lgkmcnt(5)\n"
    "v_mfma_f32_16x16x32_bf16 v[100:103], a[132:135], v[160:163], v[100:103]\n"
    "v_mfma_f32_16x16x32_bf16 v[116:119], a[132:135], v[176:179], v[116:119]\n"
    "ds_read_b64_tr_b16 a[180:181], v10 offset:1024\n"
    "v_mfma_f32_16x16x32_bf16 v[108:111], a[132:135], v[168:171], v[108:111]\n"
    "buffer_load_dwordx4 v32, s[68:71], s94 offen offset:3072 lds\n"
    "v_mfma_f32_16x16x32_bf16 v[124:127], a[132:135], v[184:187], v[124:127]\n"
    "ds_read_b64_tr_b16 a[182:183], v10 offset:5120\n"
    "v_mfma_f32_16x16x32_bf16 v[100:103], a[136:139], v[192:195], v[100:103]\n"
    "s_add_u32 s94, s94, 0x8000\n"
    "v_mfma_f32_16x16x32_bf16 v[116:119], a[136:139], v[208:211], v[116:119]\n"
    "ds_read_b64_tr_b16 a[184:185], v9 offset:1536\n"
    "v_mfma_f32_16x16x32_bf16 v[108:111], a[136:139], v[200:203], v[108:111]\n"
    "v_mfma_f32_16x16x32_bf16 v[124:127], a[136:139], v[216:219], v[124:127]\n"
    "ds_read_b64_tr_b16 a[186:187], v9 offset:5632\n"
    "s_waitcnt lgkmcnt(4)\n"
    "v_mfma_f32_16x16x32_bf16 v[100:103], a[140:143], v[224:227], v[100:103]\n"
    "buffer_load_dwordx4 v[228:231], v56, s[72:75], s96 offen sc1\n"
    "v_mfma_f32_16x16x32_bf16 v[116:119], a[140:143], v[240:243], v[116:119]\n"
    "ds_read_b64_tr_b16 a[188:189], v10 offset:1536\n"
    "v_mfma_f32_16x16x32_bf16 v[108:111], a[140:143], v[232:235], v[108:111]\n"
    "v_mfma_f32_16x16x32_bf16 v[124:127], a[140:143], v[248:251], v[124:127]\n"
    "ds_read_b64_tr_b16 a[190:191], v10 offset:5632\n"
    "v_cvt_pk_bf16_f32 v91, v94, v95\n"
    "v_mfma_f32_16x16x32_bf16 a[144:147], v[34:37], v[64:67], a[144:147]\n"
    "v_mfma_f32_16x16x32_bf16 a[152:155], v[34:37], v[80:83], a[152:155]\n"
    "v_mfma_f32_16x16x32_bf16 a[148:151], v[34:37], v[72:75], a[148:151]\n"
    "buffer_load_dwordx4 v[236:239], v57, s[72:75], s96 offen sc1\n"
    "v_mfma_f32_16x16x32_bf16 a[156:159], v[34:37], v[88:91], a[156:159]\n"
    "v_exp_f32 v96, v96\n"
    "v_mfma_f32_16x16x32_bf16 a[0:3], a[160:163], v[64:67], a[0:3]\n"
    "v_exp_f32 v97, v97\n"
    "v_mfma_f32_16x16x32_bf16 a[16:19], a[160:163], v[80:83], a[16:19]\n"
    "v_exp_f32 v98, v98\n"
    "v_mfma_f32_16x16x32_bf16 a[8:11], a[160:163], v[72:75], a[8:11]\n"
    "v_exp_f32 v99, v99\n"
    "v_mfma_f32_16x16x32_bf16 a[24:27], a[160:163], v[88:91], a[24:27]\n"
    "v_cvt_pk_bf16_f32 v96, v96, v97\n"
    "v_exp_f32 v100, v100\n"
    "v_mfma_f32_16x16x32_bf16 a[4:7], a[164:167], v[64:67], a[4:7]\n"
    "ds_read_b64_tr_b16 a[160:161], v9 offset:8192\n"
    "buffer_load_dwordx4 v[244:247], v56, s[72:75], s96 offen offset:2048 sc1\n"
    "v_mfma_f32_16x16x32_bf16 a[20:23], a[164:167], v[80:83], a[20:23]\n"
    "ds_read_b64_tr_b16 a[162:163], v9 offset:12288\n"
    "v_cvt_pk_bf16_f32 v97, v98, v99\n"
    "v_mfma_f32_16x16x32_bf16 a[12:15], a[164:167], v[72:75], a[12:15]\n"
    "v_exp_f32 v101, v101\n"
    "v_mfma_f32_16x16x32_bf16 a[28:31], a[164:167], v[88:91], a[28:31]\n"
    "v_exp_f32 v102, v102\n"
    "v_mfma_f32_16x16x32_bf16 a[32:35], a[168:171], v[64:67], a[32:35]\n"
    "ds_read_b64_tr_b16 a[164:165], v10 offset:8192\n"
    "v_exp_f32 v103, v103\n"
    "v_mfma_f32_16x16x32_bf16 a[48:51], a[168:171], v[80:83], a[48:51]\n"
    "ds_read_b64_tr_b16 a[166:167], v10 offset:12288\n"
    "v_cvt_pk_bf16_f32 v98, v100, v101\n"
    "v_mfma_f32_16x16x32_bf16 a[40:43], a[168:171], v[72:75], a[40:43]\n"
    "buffer_load_dwordx4 v[252:255], v57, s[72:75], s96 offen offset:2048 sc1\n"
    "s_add_u32 s96, s96, 0x1000\n"
    "v_mfma_f32_16x16x32_bf16 a[56:59], a[168:171], v[88:91], a[56:59]\n"
    "v_cvt_pk_bf16_f32 v99, v102, v103\n"
    "v_mfma_f32_16x16x32_bf16 a[36:39], a[172:175], v[64:67], a[36:39]\n"
    "ds_read_b64_tr_b16 a[168:169], v9 offset:8704\n"
    "s_add_u32 s93, s96, 0x1000\n"
    "v_mfma_f32_16x16x32_bf16 a[52:55], a[172:175], v[80:83], a[52:55]\n"
    "ds_read_b64_tr_b16 a[170:171], v9 offset:12800\n"
    "v_exp_f32 v112, v112\n"
    "v_mfma_f32_16x16x32_bf16 a[44:47], a[172:175], v[72:75], a[44:47]\n"
    "v_exp_f32 v113, v113\n"
    "v_mfma_f32_16x16x32_bf16 a[60:63], a[172:175], v[88:91], a[60:63]\n"
    "v_exp_f32 v114, v114\n"
    "v_mfma_f32_16x16x32_bf16 a[64:67], a[176:179], v[64:67], a[64:67]\n"
    "ds_read_b64_tr_b16 a[172:173], v10 offset:8704\n"
    "v_cvt_pk_bf16_f32 v112, v112, v113\n"
    "v_mfma_f32_16x16x32_bf16 a[80:83], a[176:179], v[80:83], a[80:83]\n"
    "ds_read_b64_tr_b16 a[174:175], v10 offset:12800\n"
    "v_mfma_f32_16x16x32_bf16 a[72:75], a[176:179], v[72:75], a[72:75]\n"
    "ds_read_b64_tr_b16 v[132:133], v40 offset:8192\n"
    "v_exp_f32 v115, v115\n"
    "v_mfma_f32_16x16x32_bf16 a[88:91], a[176:179], v[88:91], a[88:91]\n"
    "s_waitcnt lgkmcnt(3)\n"
    "ds_read_b64_tr_b16 v[134:135], v40 offset:9216\n"
    "v_mfma_f32_16x16x32_bf16 a[68:71], a[180:183], v[64:67], a[68:71]\n"
    "ds_read_b64_tr_b16 a[176:177], v9 offset:9216\n"
    "ds_read_b64_tr_b16 v[140:141], v41 offset:8192\n"
    "v_mfma_f32_16x16x32_bf16 a[84:87], a[180:183], v[80:83], a[84:87]\n"
    "ds_read_b64_tr_b16 a[178:179], v9 offset:13312\n"
    "ds_read_b64_tr_b16 v[142:143], v41 offset:9216\n"
    "v_mfma_f32_16x16x32_bf16 a[76:79], a[180:183], v[72:75], a[76:79]\n"
    "ds_read_b64_tr_b16 v[148:149], v42 offset:8192\n"
    "v_mfma_f32_16x16x32_bf16 a[92:95], a[180:183], v[88:91], a[92:95]\n"
    "ds_read_b64_tr_b16 v[150:151], v42 offset:9216\n"
    "v_exp_f32 v116, v116\n"
    "v_mfma_f32_16x16x32_bf16 a[96:99], a[184:187], v[64:67], a[96:99]\n"
    "ds_read_b64_tr_b16 a[180:181], v10 offset:9216\n"
    "ds_read_b64_tr_b16 v[156:157], v43 offset:8192\n"
    "v_mfma_f32_16x16x32_bf16 a[112:115], a[184:187], v[80:83], a[112:115]\n"
    "ds_read_b64_tr_b16 a[182:183], v10 offset:13312\n"
    "ds_read_b64_tr_b16 v[158:159], v43 offset:9216\n"
    "v_mfma_f32_16x16x32_bf16 a[104:107], a[184:187], v[72:75], a[104:107]\n"
    "v_cvt_pk_bf16_f32 v113, v114, v115\n"
    "v_mfma_f32_16x16x32_bf16 a[120:123], a[184:187], v[88:91], a[120:123]\n"
    "v_exp_f32 v117, v117\n"
    "v_mfma_f32_16x16x32_bf16 a[100:103], a[188:191], v[64:67], a[100:103]\n"
    "ds_read_b64_tr_b16 a[184:185], v9 offset:9728\n"
    "v_exp_f32 v118, v118\n"
    "v_mfma_f32_16x16x32_bf16 a[116:119], a[188:191], v[80:83], a[116:119]\n"
    "s_waitcnt lgkmcnt(5)\n"
    "ds_read_b64_tr_b16 a[186:187], v9 offset:13824\n"
    "v_cvt_pk_bf16_f32 v114, v116, v117\n"
    "v_mfma_f32_16x16x32_bf16 a[108:111], a[188:191], v[72:75], a[108:111]\n"
    "v_exp_f32 v119, v119\n"
    "v_mfma_f32_16x16x32_bf16 a[124:127], a[188:191], v[88:91], a[124:127]\n"
    "s_waitcnt vmcnt(11)\n"
    "v_exp_f32 v104, v104\n"
    "v_mfma_f32_16x16x32_bf16 a[192:195], v[132:135], v[196:199], a[192:195]\n"
    "s_waitcnt vmcnt(9)\n"
    "ds_read_b64_tr_b16 a[188:189], v10 offset:9728\n"
    "v_cvt_pk_bf16_f32 v115, v118, v119\n"
    "v_mfma_f32_16x16x32_bf16 a[200:203], v[132:135], v[212:215], a[200:203]\n"
    "ds_read_b64_tr_b16 a[190:191], v10 offset:13824\n"
    "v_exp_f32 v105, v105\n"
    "v_mfma_f32_16x16x32_bf16 a[208:211], v[132:135], v[204:207], a[208:211]\n"
    "s_waitcnt vmcnt(8)\n"
    "v_exp_f32 v106, v106\n"
    "v_mfma_f32_16x16x32_bf16 a[216:219], v[132:135], v[220:223], a[216:219]\n"
    "v_exp_f32 v107, v107\n"
    "v_mfma_f32_16x16x32_bf16 a[196:199], v[140:143], v[196:199], a[196:199]\n"
    "v_cvt_pk_bf16_f32 v104, v104, v105\n"
    "v_cvt_pk_bf16_f32 v105, v106, v107\n"
    "v_mfma_f32_16x16x32_bf16 a[204:207], v[140:143], v[212:215], a[204:207]\n"
    "v_exp_f32 v108, v108\n"
    "v_mfma_f32_16x16x32_bf16 a[212:215], v[140:143], v[204:207], a[212:215]\n"
    "v_exp_f32 v109, v109\n"
    "v_mfma_f32_16x16x32_bf16 a[220:223], v[140:143], v[220:223], a[220:223]\n"
    "v_exp_f32 v110, v110\n"
    "v_mfma_f32_16x16x32_bf16 a[224:227], v[148:151], v[196:199], a[224:227]\n"
    "v_exp_f32 v111, v111\n"
    "v_mfma_f32_16x16x32_bf16 a[232:235], v[148:151], v[212:215], a[232:235]\n"
    "v_cvt_pk_bf16_f32 v106, v108, v109\n"
    "v_cvt_pk_bf16_f32 v107, v110, v111\n"
    "v_mfma_f32_16x16x32_bf16 a[240:243], v[148:151], v[204:207], a[240:243]\n"
    "v_exp_f32 v120, v120\n"
    "v_mfma_f32_16x16x32_bf16 a[248:251], v[148:151], v[220:223], a[248:251]\n"
    "v_exp_f32 v121, v121\n"
    "s_waitcnt lgkmcnt(0)\n"
    "v_mfma_f32_16x16x32_bf16 a[228:231], v[156:159], v[196:199], a[228:231]\n"
    "v_exp_f32 v122, v122\n"
    "v_mfma_f32_16x16x32_bf16 a[236:239], v[156:159], v[212:215], a[236:239]\n"
    "v_exp_f32 v123, v123\n"
    "v_mfma_f32_16x16x32_bf16 a[244:247], v[156:159], v[204:207], a[244:247]\n"
    "v_cvt_pk_bf16_f32 v120, v120, v121\n"
    "v_cvt_pk_bf16_f32 v121, v122, v123\n"
    "v_mfma_f32_16x16x32_bf16 a[252:255], v[156:159], v[220:223], a[252:255]\n"
    "v_exp_f32 v124, v124\n"
    "v_exp_f32 v125, v125\n"
    "v_exp_f32 v126, v126\n"
    "v_exp_f32 v127, v127\n"
    "v_cvt_pk_bf16_f32 v122, v124, v125\n"
    "v_cvt_pk_bf16_f32 v123, v126, v127\n"
    "v_mfma_f32_16x16x32_bf16 a[144:147], v[34:37], v[96:99], a[144:147]\n"
    "buffer_load_dwordx4 v[196:199], v56, s[72:75], s96 offen sc1\n"
    "s_lshl_b32 s37, s33, 19\n"
    "s_lshl_b32 s38, s32, 8\n"
    "s_add_u32 s88, s37, s38\n"
    "s_add_u32 s95, s88, 0x8000\n"
    "s_add_u32 m0, s89, 0x2000\n"
    "v_mfma_f32_16x16x32_bf16 a[152:155], v[34:37], v[112:115], a[152:155]\n"
    "buffer_load_dwordx4 v25, s[64:67], s95 offen lds\n"
    "v_mfma_f32_16x16x32_bf16 a[148:151], v[34:37], v[104:107], a[148:151]\n"
    "buffer_load_dwordx4 v[204:207], v57, s[72:75], s96 offen sc1\n"
    "v_mfma_f32_16x16x32_bf16 a[156:159], v[34:37], v[120:123], a[156:159]\n"
    "v_mfma_f32_16x16x32_bf16 a[0:3], a[160:163], v[96:99], a[0:3]\n"
    "buffer_load_dwordx4 v[212:215], v56, s[72:75], s96 offen offset:2048 sc1\n"
    "v_mfma_f32_16x16x32_bf16 a[16:19], a[160:163], v[112:115], a[16:19]\n"
    "buffer_load_dwordx4 v26, s[64:67], s95 offen offset:1024 lds\n"
    "v_mfma_f32_16x16x32_bf16 a[8:11], a[160:163], v[104:107], a[8:11]\n"
    "buffer_load_dwordx4 v[220:223], v57, s[72:75], s96 offen offset:2048 sc1\n"
    "s_add_u32 s96, s96, 0x1000\n"
    "v_mfma_f32_16x16x32_bf16 a[24:27], a[160:163], v[120:123], a[24:27]\n"
    "v_accvgpr_read_b32 v44, a144\n"
    "v_accvgpr_read_b32 v45, a148\n"
    "v_accvgpr_read_b32 v46, a152\n"
    "v_accvgpr_read_b32 v47, a156\n"
    "v_mfma_f32_16x16x32_bf16 a[4:7], a[164:167], v[96:99], a[4:7]\n"
    "v_and_b32 v53, 3, v1\n"
    "v_lshlrev_b32 v53, 12, v53\n"
    "s_lshl_b32 s37, s32, 2\n"
    "s_add_u32 s37, s37, 0x10000\n"
    "v_mfma_f32_16x16x32_bf16 a[20:23], a[164:167], v[112:115], a[20:23]\n"
    "buffer_load_dwordx4 v27, s[64:67], s95 offen offset:2048 lds\n"
    "v_add_u32 v53, s37, v53\n"
    "v_cmp_nle_f32 vcc, 0x2b800000, v44\n"
    "s_mov_b64 s[98:99], vcc\n"
    "v_mfma_f32_16x16x32_bf16 a[12:15], a[164:167], v[104:107], a[12:15]\n"
    "v_cmp_nge_f32 vcc, 0x53800000, v44\n"
    "s_or_b64 s[98:99], s[98:99], vcc\n"
    "v_cmp_nle_f32 vcc, 0x2b800000, v45\n"
    "s_or_b64 s[98:99], s[98:99], vcc\n"
    "v_cmp_nge_f32 vcc, 0x53800000, v45\n"
    "s_or_b64 s[98:99], s[98:99], vcc\n"
    "v_mfma_f32_16x16x32_bf16 a[28:31], a[164:167], v[120:123], a[28:31]\n"
    "v_cmp_nle_f32 vcc, 0x2b800000, v46\n"
    "s_or_b64 s[98:99], s[98:99], vcc\n"
    "v_cmp_nge_f32 vcc, 0x53800000, v46\n"
    "s_or_b64 s[98:99], s[98:99], vcc\n"
    "v_mfma_f32_16x16x32_bf16 a[32:35], a[168:171], v[96:99], a[32:35]\n"
    "v_cmp_nle_f32 vcc, 0x2b800000, v47\n"
    "s_or_b64 s[98:99], s[98:99], vcc\n"
    "v_cmp_nge_f32 vcc, 0x53800000, v47\n"
    "s_or_b64 s[98:99], s[98:99], vcc\n"
    "v_mfma_f32_16x16x32_bf16 a[48:51], a[168:171], v[112:115], a[48:51]\n"
    "buffer_load_dwordx4 v28, s[64:67], s95 offen offset:3072 lds\n"
    "s_cmp_lg_u64 s[98:99], 0\n"
    "s_cselect_b32 s37, 1, 0\n"
    "v_mov_b32 v54, s37\n"
    "ds_write_b32 v53, v54\n"
    "s_waitcnt lgkmcnt(0)\n"
    "s_barrier\n"
    "s_add_u32 s37, s91, 0x10000\n"
    "v_mov_b32 v53, s37\n"
    "ds_read_b128 v[48:51], v53\n"
    "v_mfma_f32_16x16x32_bf16 a[40:43], a[168:171], v[104:107], a[40:43]\n"
    "v_mfma_f32_16x16x32_bf16 a[56:59], a[168:171], v[120:123], a[56:59]\n"
    "v_mfma_f32_16x16x32_bf16 a[36:39], a[172:175], v[96:99], a[36:39]\n"
    "v_mfma_f32_16x16x32_bf16 a[52:55], a[172:175], v[112:115], a[52:55]\n"
    "v_mfma_f32_16x16x32_bf16 a[44:47], a[172:175], v[104:107], a[44:47]\n"
    "s_waitcnt lgkmcnt(0)\n"
    "v_or_b32 v48, v48, v49\n"
    "v_or_b32 v50, v50, v51\n"
    "v_or_b32 v48, v48, v50\n"
    "s_nop 0\n"
    "v_readfirstlane_b32 s37, v48\n"
    "s_cmp_lg_u32 s37, 0\n"
    "s_cbranch_scc1 FB_%=\n"
    "FBR_%=:\n"
    "v_mfma_f32_16x16x32_bf16 a[60:63], a[172:175], v[120:123], a[60:63]\n"
    "s_add_u32 m0, s91, 0x10000\n"
    "s_nop 0\n"
    "buffer_load_dwordx4 v25, s[64:67], s88 offen lds\n"
    "s_lshl_b32 s39, s32, 13\n"
    "v_add_u32 v53, s39, v2\n"
    "v_accvgpr_read_b32 v44, a144\n"
    "v_accvgpr_read_b32 v45, a148\n"
    "v_mfma_f32_16x16x32_bf16 a[64:67], a[176:179], v[96:99], a[64:67]\n"
    "buffer_load_dwordx4 v[128:131], v56, s[72:75], s93 offen sc1\n"
    "ds_read_b64_tr_b16 v[164:165], v40 offset:12288\n"
    "v_accvgpr_read_b32 v46, a152\n"
    "v_accvgpr_read_b32 v47, a156\n"
    "v_mfma_f32_16x16x32_bf16 a[80:83], a[176:179], v[112:115], a[80:83]\n"
    "buffer_load_dwordx4 v26, s[64:67], s88 offen offset:1024 lds\n"
    "v_rcp_f32 v44, v44\n"
    "v_rcp_f32 v45, v45\n"
    "v_mfma_f32_16x16x32_bf16 a[72:75], a[176:179], v[104:107], a[72:75]\n"
    "ds_read_b64_tr_b16 v[166:167], v40 offset:13312\n"
    "v_rcp_f32 v46, v46\n"
    "v_rcp_f32 v47, v47\n"
    "v_mfma_f32_16x16x32_bf16 a[88:91], a[176:179], v[120:123], a[88:91]\n"
    "buffer_load_dwordx4 v[136:139], v57, s[72:75], s93 offen sc1\n"
    "buffer_load_dwordx4 v27, s[64:67], s88 offen offset:2048 lds\n"
    "v_accvgpr_read_b32 v64, a0\n"
    "v_accvgpr_read_b32 v65, a1\n"
    "v_accvgpr_read_b32 v66, a2\n"
    "v_accvgpr_read_b32 v67, a3\n"
    "v_accvgpr_read_b32 v68, a4\n"
    "v_mfma_f32_16x16x32_bf16 a[68:71], a[180:183], v[96:99], a[68:71]\n"
    "ds_read_b64_tr_b16 v[172:173], v41 offset:12288\n"
    "v_accvgpr_read_b32 v69, a5\n"
    "v_accvgpr_read_b32 v70, a6\n"
    "v_accvgpr_read_b32 v71, a7\n"
    "v_mul_f32 v64, v64, v44\n"
    "v_mul_f32 v65, v65, v44\n"
    "v_mul_f32 v66, v66, v44\n"
    "v_mfma_f32_16x16x32_bf16 a[84:87], a[180:183], v[112:115], a[84:87]\n"
    "buffer_load_dwordx4 v28, s[64:67], s88 offen offset:3072 lds\n"
    "s_add_u32 s88, s88, 0x4000\n"
    "v_mul_f32 v67, v67, v44\n"
    "v_mul_f32 v68, v68, v44\n"
    "v_mul_f32 v69, v69, v44\n"
    "v_mul_f32 v70, v70, v44\n"
    "v_mul_f32 v71, v71, v44\n"
    "v_mfma_f32_16x16x32_bf16 a[76:79], a[180:183], v[104:107], a[76:79]\n"
    "buffer_load_dwordx4 v[144:147], v56, s[72:75], s93 offen offset:2048 sc1\n"
    "ds_read_b64_tr_b16 v[174:175], v41 offset:13312\n"
    "v_cvt_pk_bf16_f32 v80, v64, v65\n"
    "v_cvt_pk_bf16_f32 v81, v66, v67\n"
    "v_cvt_pk_bf16_f32 v82, v68, v69\n"
    "v_cvt_pk_bf16_f32 v83, v70, v71\n"
    "ds_write_b128 v53, v[80:83]\n"
    "v_accvgpr_read_b32 v72, a16\n"
    "v_mfma_f32_16x16x32_bf16 a[92:95], a[180:183], v[120:123], a[92:95]\n"
    "s_add_u32 m0, s91, 0x14000\n"
    "s_nop 0\n"
    "buffer_load_dwordx4 v25, s[64:67], s88 offen lds\n"
    "v_accvgpr_read_b32 v73, a17\n"
    "v_accvgpr_read_b32 v74, a18\n"
    "v_accvgpr_read_b32 v75, a19\n"
    "v_accvgpr_read_b32 v76, a20\n"
    "v_accvgpr_read_b32 v77, a21\n"
    "v_accvgpr_read_b32 v78, a22\n"
    "v_mfma_f32_16x16x32_bf16 a[96:99], a[184:187], v[96:99], a[96:99]\n"
    "ds_read_b64_tr_b16 v[180:181], v42 offset:12288\n"
    "v_accvgpr_read_b32 v79, a23\n"
    "v_mul_f32 v72, v72, v46\n"
    "v_mul_f32 v73, v73, v46\n"
    "v_mul_f32 v74, v74, v46\n"
    "v_mul_f32 v75, v75, v46\n"
    "v_mul_f32 v76, v76, v46\n"
    "v_mfma_f32_16x16x32_bf16 a[112:115], a[184:187], v[112:115], a[112:115]\n"
    "buffer_load_dwordx4 v[152:155], v57, s[72:75], s93 offen offset:2048 sc1\n"
    "buffer_load_dwordx4 v26, s[64:67], s88 offen offset:1024 lds\n"
    "v_mul_f32 v77, v77, v46\n"
    "v_mul_f32 v78, v78, v46\n"
    "v_mul_f32 v79, v79, v46\n"
    "v_cvt_pk_bf16_f32 v84, v72, v73\n"
    "v_cvt_pk_bf16_f32 v85, v74, v75\n"
    "v_mfma_f32_16x16x32_bf16 a[104:107], a[184:187], v[104:107], a[104:107]\n"
    "ds_read_b64_tr_b16 v[182:183], v42 offset:13312\n"
    "v_cvt_pk_bf16_f32 v86, v76, v77\n"
    "v_cvt_pk_bf16_f32 v87, v78, v79\n"
    "ds_write_b128 v53, v[84:87] offset:32768\n"
    "v_accvgpr_read_b32 v64, a8\n"
    "v_accvgpr_read_b32 v65, a9\n"
    "v_accvgpr_read_b32 v66, a10\n"
    "v_accvgpr_read_b32 v67, a11\n"
    "v_mfma_f32_16x16x32_bf16 a[120:123], a[184:187], v[120:123], a[120:123]\n"
    "buffer_load_dwordx4 v27, s[64:67], s88 offen offset:2048 lds\n"
    "v_accvgpr_read_b32 v68, a12\n"
    "v_accvgpr_read_b32 v69, a13\n"
    "v_accvgpr_read_b32 v70, a14\n"
    "v_accvgpr_read_b32 v71, a15\n"
    "v_mul_f32 v64, v64, v45\n"
    "v_mul_f32 v65, v65, v45\n"
    "v_mfma_f32_16x16x32_bf16 a[100:103], a[188:191], v[96:99], a[100:103]\n"
    "ds_read_b64_tr_b16 v[188:189], v43 offset:12288\n"
    "v_mul_f32 v66, v66, v45\n"
    "v_mul_f32 v67, v67, v45\n"
    "v_mul_f32 v68, v68, v45\n"
    "v_mul_f32 v69, v69, v45\n"
    "v_mul_f32 v70, v70, v45\n"
    "v_mul_f32 v71, v71, v45\n"
    "v_mfma_f32_16x16x32_bf16 a[116:119], a[188:191], v[112:115], a[116:119]\n"
    "buffer_load_dwordx4 v28, s[64:67], s88 offen offset:3072 lds\n"
    "s_add_u32 s88, s88, 0x4000\n"
    "s_add_u32 s88, s88, 0x4000\n"
    "v_cvt_pk_bf16_f32 v88, v64, v65\n"
    "v_cvt_pk_bf16_f32 v89, v66, v67\n"
    "v_cvt_pk_bf16_f32 v90, v68, v69\n"
    "v_cvt_pk_bf16_f32 v91, v70, v71\n"
    "v_mfma_f32_16x16x32_bf16 a[108:111], a[188:191], v[104:107], a[108:111]\n"
    "ds_read_b64_tr_b16 v[190:191], v43 offset:13312\n"
    "ds_write_b128 v53, v[88:91] offset:1024\n"
    "v_accvgpr_read_b32 v72, a24\n"
    "v_accvgpr_read_b32 v73, a25\n"
    "v_accvgpr_read_b32 v74, a26\n"
    "v_accvgpr_read_b32 v75, a27\n"
    "v_accvgpr_read_b32 v76, a28\n"
    "v_mfma_f32_16x16x32_bf16 a[124:127], a[188:191], v[120:123], a[124:127]\n"
    "s_waitcnt vmcnt(23) lgkmcnt(2)\n"
    "v_accvgpr_read_b32 v77, a29\n"
    "v_accvgpr_read_b32 v78, a30\n"
    "v_accvgpr_read_b32 v79, a31\n"
    "v_mul_f32 v72, v72, v47\n"
    "v_mul_f32 v73, v73, v47\n"
    "v_mul_f32 v74, v74, v47\n"
    "v_mul_f32 v75, v75, v47\n"
    "v_mul_f32 v76, v76, v47\n"
    "; PHASE DA\n"
    "v_mul_f32 v77, v77, v47\n"
    "v_mul_f32 v78, v78, v47\n"
    "v_mul_f32 v79, v79, v47\n"
    "v_cvt_pk_bf16_f32 v92, v72, v73\n"
    "v_cvt_pk_bf16_f32 v93, v74, v75\n"
    "v_cvt_pk_bf16_f32 v94, v76, v77\n"
    "v_cvt_pk_bf16_f32 v95, v78, v79\n"
    "ds_write_b128 v53, v[92:95] offset:33792\n"
    "v_accvgpr_read_b32 v64, a32\n"
    "v_accvgpr_read_b32 v65, a33\n"
    "v_mfma_f32_16x16x32_bf16 a[192:195], v[164:167], v[228:231], a[192:195]\n"
    "s_waitcnt vmcnt(21)\n"
    "ds_read_b64_tr_b16 v[132:133], v40\n"
    "v_accvgpr_read_b32 v66, a34\n"
    "v_accvgpr_read_b32 v67, a35\n"
    "v_accvgpr_read_b32 v68, a36\n"
    "v_accvgpr_read_b32 v69, a37\n"
    "v_accvgpr_read_b32 v70, a38\n"
    "v_accvgpr_read_b32 v71, a39\n"
    "v_mul_f32 v64, v64, v44\n"
    "v_mfma_f32_16x16x32_bf16 a[200:203], v[164:167], v[244:247], a[200:203]\n"
    "v_mul_f32 v65, v65, v44\n"
    "v_mul_f32 v66, v66, v44\n"
    "v_mul_f32 v67, v67, v44\n"
    "v_mul_f32 v68, v68, v44\n"
    "v_mul_f32 v69, v69, v44\n"
    "v_mul_f32 v70, v70, v44\n"
    "v_mul_f32 v71, v71, v44\n"
    "v_mfma_f32_16x16x32_bf16 a[208:211], v[164:167], v[236:239], a[208:211]\n"
    "s_add_u32 m0, s89, 0x3000\n"
    "ds_read_b64_tr_b16 v[134:135], v40 offset:1024\n"
    "v_cvt_pk_bf16_f32 v80, v64, v65\n"
    "v_cvt_pk_bf16_f32 v81, v66, v67\n"
    "v_cvt_pk_bf16_f32 v82, v68, v69\n"
    "v_cvt_pk_bf16_f32 v83, v70, v71\n"
    "ds_write_b128 v53, v[80:83] offset:2048\n"
    "v_accvgpr_read_b32 v72, a48\n"
    "v_accvgpr_read_b32 v73, a49\n"
    "s_waitcnt vmcnt(20)\n"
    "v_mfma_f32_16x16x32_bf16 a[216:219], v[164:167], v[252:255], a[216:219]\n"
    "s_waitcnt lgkmcnt(2)\n"
    "buffer_load_dwordx4 v25, s[64:67], s88 offen lds\n"
    "v_accvgpr_read_b32 v74, a50\n"
    "v_accvgpr_read_b32 v75, a51\n"
    "v_accvgpr_read_b32 v76, a52\n"
    "v_accvgpr_read_b32 v77, a53\n"
    "v_accvgpr_read_b32 v78, a54\n"
    "v_accvgpr_read_b32 v79, a55\n"
    "v_mfma_f32_16x16x32_bf16 a[196:199], v[172:175], v[228:231], a[196:199]\n"
    "ds_read_b64_tr_b16 v[140:141], v41\n"
    "v_mul_f32 v72, v72, v46\n"
    "v_mul_f32 v73, v73, v46\n"
    "v_mul_f32 v74, v74, v46\n"
    "v_mul_f32 v75, v75, v46\n"
    "v_mul_f32 v76, v76, v46\n"
    "v_mul_f32 v77, v77, v46\n"
    "v_mfma_f32_16x16x32_bf16 a[204:207], v[172:175], v[244:247], a[204:207]\n"
    "v_mul_f32 v78, v78, v46\n"
    "v_mul_f32 v79, v79, v46\n"
    "v_cvt_pk_bf16_f32 v84, v72, v73\n"
    "v_cvt_pk_bf16_f32 v85, v74, v75\n"
    "v_cvt_pk_bf16_f32 v86, v76, v77\n"
    "v_cvt_pk_bf16_f32 v87, v78, v79\n"
    "ds_write_b128 v53, v[84:87] offset:34816\n"
    "v_accvgpr_read_b32 v64, a40\n"
    "v_mfma_f32_16x16x32_bf16 a[212:215], v[172:175], v[236:239], a[212:215]\n"
    "buffer_load_dwordx4 v26, s[64:67], s88 offen offset:1024 lds\n"
    "ds_read_b64_tr_b16 v[142:143], v41 offset:1024\n"
    "v_accvgpr_read_b32 v65, a41\n"
    "v_accvgpr_read_b32 v66, a42\n"
    "v_accvgpr_read_b32 v67, a43\n"
    "v_accvgpr_read_b32 v68, a44\n"
    "v_accvgpr_read_b32 v69, a45\n"
    "v_mfma_f32_16x16x32_bf16 a[220:223], v[172:175], v[252:255], a[220:223]\n"
    "v_accvgpr_read_b32 v70, a46\n"
    "v_accvgpr_read_b32 v71, a47\n"
    "v_mul_f32 v64, v64, v45\n"
    "v_mul_f32 v65, v65, v45\n"
    "v_mul_f32 v66, v66, v45\n"
    "v_mul_f32 v67, v67, v45\n"
    "v_mul_f32 v68, v68, v45\n"
    "v_mfma_f32_16x16x32_bf16 a[224:227], v[180:183], v[228:231], a[224:227]\n"
    "ds_read_b64_tr_b16 v[148:149], v42\n"
    "v_mul_f32 v69, v69, v45\n"
    "v_mul_f32 v70, v70, v45\n"
    "v_mul_f32 v71, v71, v45\n"
    "v_cvt_pk_bf16_f32 v88, v64, v65\n"
    "v_cvt_pk_bf16_f32 v89, v66, v67\n"
    "v_cvt_pk_bf16_f32 v90, v68, v69\n"
    "v_cvt_pk_bf16_f32 v91, v70, v71\n"
    "v_mfma_f32_16x16x32_bf16 a[232:235], v[180:183], v[244:247], a[232:235]\n"
    "buffer_load_dwordx4 v27, s[64:67], s88 offen offset:2048 lds\n"
    "ds_write_b128 v53, v[88:91] offset:3072\n"
    "v_accvgpr_read_b32 v72, a56\n"
    "v_accvgpr_read_b32 v73, a57\n"
    "v_accvgpr_read_b32 v74, a58\n"
    "v_accvgpr_read_b32 v75, a59\n"
    "v_accvgpr_read_b32 v76, a60\n"
    "v_mfma_f32_16x16x32_bf16 a[240:243], v[180:183], v[236:239], a[240:243]\n"
    "ds_read_b64_tr_b16 v[150:151], v42 offset:1024\n"
    "v_accvgpr_read_b32 v77, a61\n"
    "v_accvgpr_read_b32 v78, a62\n"
    "v_accvgpr_read_b32 v79, a63\n"
    "v_mul_f32 v72, v72, v47\n"
    "v_mul_f32 v73, v73, v47\n"
    "v_mul_f32 v74, v74, v47\n"
    "v_mfma_f32_16x16x32_bf16 a[248:251], v[180:183], v[252:255], a[248:251]\n"
    "v_mul_f32 v75, v75, v47\n"
    "v_mul_f32 v76, v76, v47\n"
    "v_mul_f32 v77, v77, v47\n"
    "v_mul_f32 v78, v78, v47\n"
    "v_mul_f32 v79, v79, v47\n"
    "v_cvt_pk_bf16_f32 v92, v72, v73\n"
    "v_cvt_pk_bf16_f32 v93, v74, v75\n"
    "v_cvt_pk_bf16_f32 v94, v76, v77\n"
    "v_mfma_f32_16x16x32_bf16 a[228:231], v[188:191], v[228:231], a[228:231]\n"
    "buffer_load_dwordx4 v28, s[64:67], s88 offen offset:3072 lds\n"
    "ds_read_b64_tr_b16 v[156:157], v43\n"
    "v_and_b32 v56, 15, v1\n"
    "v_lshlrev_b32 v56, 10, v56\n"
    "v_lshrrev_b32 v55, 4, v1\n"
    "v_cvt_pk_bf16_f32 v95, v78, v79\n"
    "v_mfma_f32_16x16x32_bf16 a[236:239], v[188:191], v[244:247], a[236:239]\n"
    "s_add_u32 s88, s88, 0x4000\n"
    "v_lshl_add_u32 v56, v55, 3, v56\n"
    "s_lshl_b32 s93, s35, 16\n"
    "s_and_b32 s39, s32, 1\n"
    "s_lshl_b32 s39, s39, 15\n"
    "ds_write_b128 v53, v[92:95] offset:35840\n"
    "v_accvgpr_read_b32 v64, a64\n"
    "v_accvgpr_read_b32 v65, a65\n"
    "v_mfma_f32_16x16x32_bf16 a[244:247], v[188:191], v[236:239], a[244:247]\n"
    "ds_read_b64_tr_b16 v[158:159], v43 offset:1024\n"
    "s_add_u32 s93, s93, s39\n"
    "s_lshl_b32 s39, s36, 7\n"
    "s_add_u32 s93, s93, s39\n"
    "v_accvgpr_read_b32 v66, a66\n"
    "v_accvgpr_read_b32 v67, a67\n"
    "v_mfma_f32_16x16x32_bf16 a[252:255], v[188:191], v[252:255], a[252:255]\n"
    "s_lshr_b32 s39, s32, 1\n"
    "s_lshl_b32 s39, s39, 6\n"
    "s_add_u32 s93, s93, s39\n"
    "s_add_u32 s39, s93, 0x4000\n"
    "v_accvgpr_read_b32 v68, a68\n"
    "v_accvgpr_read_b32 v69, a69\n"
    "v_accvgpr_read_b32 v70, a70\n"
    "s_waitcnt vmcnt(23) lgkmcnt(0)\n"
    "v_mfma_f32_16x16x32_bf16 a[192:195], v[132:135], v[196:199], a[192:195]\n"
    "s_waitcnt vmcnt(20)\n"
    "ds_read_b64_tr_b16 v[164:165], v40 offset:4096\n"
    "buffer_load_dwordx2 v[112:113], v56, s[40:43], s93 offen\n"
    "v_accvgpr_read_b32 v71, a71\n"
    "v_mul_f32 v64, v64, v44\n"
    "v_mul_f32 v65, v65, v44\n"
    "v_mul_f32 v66, v66, v44\n"
    "v_mul_f32 v67, v67, v44\n"
    "v_mfma_f32_16x16x32_bf16 a[200:203], v[132:135], v[212:215], a[200:203]\n"
    "v_mul_f32 v68, v68, v44\n"
    "v_mul_f32 v69, v69, v44\n"
    "v_mul_f32 v70, v70, v44\n"
    "v_mul_f32 v71, v71, v44\n"
    "v_cvt_pk_bf16_f32 v80, v64, v65\n"
    "v_cvt_pk_bf16_f32 v81, v66, v67\n"
    "v_cvt_pk_bf16_f32 v82, v68, v69\n"
    "v_cvt_pk_bf16_f32 v83, v70, v71\n"
    "v_mfma_f32_16x16x32_bf16 a[208:211], v[132:135], v[204:207], a[208:211]\n"
    "s_add_u32 m0, s89, 0\n"
    "ds_read_b64_tr_b16 v[166:167], v40 offset:5120\n"
    "ds_write_b128 v53, v[80:83] offset:4096\n"
    "v_accvgpr_read_b32 v72, a80\n"
    "v_accvgpr_read_b32 v73, a81\n"
    "v_accvgpr_read_b32 v74, a82\n"
    "v_accvgpr_read_b32 v75, a83\n"
    "v_accvgpr_read_b32 v76, a84\n"
    "s_waitcnt vmcnt(19)\n"
    "v_mfma_f32_16x16x32_bf16 a[216:219], v[132:135], v[220:223], a[216:219]\n"
    "buffer_load_dwordx4 v25, s[64:67], s88 offen lds\n"
    "v_accvgpr_read_b32 v77, a85\n"
    "v_accvgpr_read_b32 v78, a86\n"
    "v_accvgpr_read_b32 v79, a87\n"
    "v_mul_f32 v72, v72, v46\n"
    "v_mul_f32 v73, v73, v46\n"
    "v_mul_f32 v74, v74, v46\n"
    "v_mfma_f32_16x16x32_bf16 a[196:199], v[140:143], v[196:199], a[196:199]\n"
    "ds_read_b64_tr_b16 v[172:173], v41 offset:4096\n"
    "buffer_load_dwordx2 v[114:115], v56, s[40:43], s93 offen offset:32\n"
    "v_mul_f32 v75, v75, v46\n"
    "v_mul_f32 v76, v76, v46\n"
    "v_mul_f32 v77, v77, v46\n"
    "v_mul_f32 v78, v78, v46\n"
    "v_mul_f32 v79, v79, v46\n"
    "v_mfma_f32_16x16x32_bf16 a[204:207], v[140:143], v[212:215], a[204:207]\n"
    "v_cvt_pk_bf16_f32 v84, v72, v73\n"
    "v_cvt_pk_bf16_f32 v85, v74, v75\n"
    "v_cvt_pk_bf16_f32 v86, v76, v77\n"
    "v_cvt_pk_bf16_f32 v87, v78, v79\n"
    "ds_write_b128 v53, v[84:87] offset:36864\n"
    "v_accvgpr_read_b32 v64, a72\n"
    "v_accvgpr_read_b32 v65, a73\n"
    "v_accvgpr_read_b32 v66, a74\n"
    "v_mfma_f32_16x16x32_bf16 a[212:215], v[140:143], v[204:207], a[212:215]\n"
    "buffer_load_dwordx4 v26, s[64:67], s88 offen offset:1024 lds\n"
    "ds_read_b64_tr_b16 v[174:175], v41 offset:5120\n"
    "v_accvgpr_read_b32 v67, a75\n"
    "v_accvgpr_read_b32 v68, a76\n"
    "v_accvgpr_read_b32 v69, a77\n"
    "v_accvgpr_read_b32 v70, a78\n"
    "v_accvgpr_read_b32 v71, a79\n"
    "v_mfma_f32_16x16x32_bf16 a[220:223], v[140:143], v[220:223], a[220:223]\n"
    "v_mul_f32 v64, v64, v45\n"
    "v_mul_f32 v65, v65, v45\n"
    "v_mul_f32 v66, v66, v45\n"
    "v_mul_f32 v67, v67, v45\n"
    "v_mul_f32 v68, v68, v45\n"
    "v_mul_f32 v69, v69, v45\n"
    "v_mul_f32 v70, v70, v45\n"
    "v_mul_f32 v71, v71, v45\n"
    "v_mfma_f32_16x16x32_bf16 a[224:227], v[148:151], v[196:199], a[224:227]\n"
    "ds_read_b64_tr_b16 v[180:181], v42 offset:4096\n"
    "buffer_load_dwordx2 v[116:117], v56, s[40:43], s39 offen\n"
    "v_cvt_pk_bf16_f32 v88, v64, v65\n"
    "v_cvt_pk_bf16_f32 v89, v66, v67\n"
    "v_cvt_pk_bf16_f32 v90, v68, v69\n"
    "v_cvt_pk_bf16_f32 v91, v70, v71\n"
    "ds_write_b128 v53, v[88:91] offset:5120\n"
    "v_mfma_f32_16x16x32_bf16 a[232:235], v[148:151], v[212:215], a[232:235]\n"
    "buffer_load_dwordx4 v27, s[64:67], s88 offen offset:2048 lds\n"
    "v_accvgpr_read_b32 v72, a88\n"
    "v_accvgpr_read_b32 v73, a89\n"
    "v_accvgpr_read_b32 v74, a90\n"
    "v_accvgpr_read_b32 v75, a91\n"
    "v_accvgpr_read_b32 v76, a92\n"
    "v_accvgpr_read_b32 v77, a93\n"
    "v_mfma_f32_16x16x32_bf16 a[240:243], v[148:151], v[204:207], a[240:243]\n"
    "ds_read_b64_tr_b16 v[182:183], v42 offset:5120\n"
    "v_accvgpr_read_b32 v78, a94\n"
    "v_accvgpr_read_b32 v79, a95\n"
    "v_mul_f32 v72, v72, v47\n"
    "v_mul_f32 v73, v73, v47\n"
    "v_mul_f32 v74, v74, v47\n"
    "v_mul_f32 v75, v75, v47\n"
    "v_mfma_f32_16x16x32_bf16 a[248:251], v[148:151], v[220:223], a[248:251]\n"
    "v_mul_f32 v76, v76, v47\n"
    "v_mul_f32 v77, v77, v47\n"
    "v_mul_f32 v78, v78, v47\n"
    "v_mul_f32 v79, v79, v47\n"
    "v_cvt_pk_bf16_f32 v92, v72, v73\n"
    "v_cvt_pk_bf16_f32 v93, v74, v75\n"
    "v_cvt_pk_bf16_f32 v94, v76, v77\n"
    "v_cvt_pk_bf16_f32 v95, v78, v79\n"
    "v_mfma_f32_16x16x32_bf16 a[228:231], v[156:159], v[196:199], a[228:231]\n"
    "buffer_load_dwordx4 v28, s[64:67], s88 offen offset:3072 lds\n"
    "ds_read_b64_tr_b16 v[188:189], v43 offset:4096\n"
    "buffer_load_dwordx2 v[118:119], v56, s[40:43], s39 offen offset:32\n"
    "ds_write_b128 v53, v[92:95] offset:37888\n"
    "v_accvgpr_read_b32 v64, a96\n"
    "v_accvgpr_read_b32 v65, a97\n"
    "v_accvgpr_read_b32 v66, a98\n"
    "v_mfma_f32_16x16x32_bf16 a[236:239], v[156:159], v[212:215], a[236:239]\n"
    "s_add_u32 s88, s88, 0x4000\n"
    "v_accvgpr_read_b32 v67, a99\n"
    "v_accvgpr_read_b32 v68, a100\n"
    "v_accvgpr_read_b32 v69, a101\n"
    "v_accvgpr_read_b32 v70, a102\n"
    "v_accvgpr_read_b32 v71, a103\n"
    "v_mul_f32 v64, v64, v44\n"
    "v_mul_f32 v65, v65, v44\n"
    "v_mfma_f32_16x16x32_bf16 a[244:247], v[156:159], v[204:207], a[244:247]\n"
    "ds_read_b64_tr_b16 v[190:191], v43 offset:5120\n"
    "v_mul_f32 v66, v66, v44\n"
    "v_mul_f32 v67, v67, v44\n"
    "v_mul_f32 v68, v68, v44\n"
    "v_mul_f32 v69, v69, v44\n"
    "v_mul_f32 v70, v70, v44\n"
    "v_mul_f32 v71, v71, v44\n"
    "v_cvt_pk_bf16_f32 v80, v64, v65\n"
    "v_mfma_f32_16x16x32_bf16 a[252:255], v[156:159], v[220:223], a[252:255]\n"
    "s_waitcnt vmcnt(22) lgkmcnt(1)\n"
    "v_cvt_pk_bf16_f32 v81, v66, v67\n"
    "v_cvt_pk_bf16_f32 v82, v68, v69\n"
    "v_cvt_pk_bf16_f32 v83, v70, v71\n"
    "ds_write_b128 v53, v[80:83] offset:6144\n"
    "v_accvgpr_read_b32 v72, a112\n"
    "v_accvgpr_read_b32 v73, a113\n"
    "v_accvgpr_read_b32 v74, a114\n"
    "v_mfma_f32_16x16x32_bf16 a[192:195], v[164:167], v[128:131], a[192:195]\n"
    "s_waitcnt vmcnt(17)\n"
    "ds_read_b64_tr_b16 a[128:129], v5\n"
    "v_accvgpr_read_b32 v75, a115\n"
    "v_accvgpr_read_b32 v76, a116\n"
    "v_accvgpr_read_b32 v77, a117\n"
    "v_accvgpr_read_b32 v78, a118\n"
    "v_accvgpr_read_b32 v79, a119\n"
    "v_mul_f32 v72, v72, v46\n"
    "v_mul_f32 v73, v73, v46\n"
    "v_mfma_f32_16x16x32_bf16 a[200:203], v[164:167], v[144:147], a[200:203]\n"
    "s_waitcnt vmcnt(12)\n"
    "ds_read_b64_tr_b16 a[130:131], v5 offset:16384\n"
    "v_mul_f32 v74, v74, v46\n"
    "v_mul_f32 v75, v75, v46\n"
    "v_mul_f32 v76, v76, v46\n"
    "v_mul_f32 v77, v77, v46\n"
    "v_mul_f32 v78, v78, v46\n"
    "v_mul_f32 v79, v79, v46\n"
    "v_mfma_f32_16x16x32_bf16 a[208:211], v[164:167], v[136:139], a[208:211]\n"
    "s_add_u32 m0, s89, 0x1000\n"
    "ds_read_b64_tr_b16 a[132:133], v6\n"
    "v_cvt_pk_bf16_f32 v84, v72, v73\n"
    "v_cvt_pk_bf16_f32 v85, v74, v75\n"
    "v_cvt_pk_bf16_f32 v86, v76, v77\n"
    "v_cvt_pk_bf16_f32 v87, v78, v79\n"
    "ds_write_b128 v53, v[84:87] offset:38912\n"
    "v_accvgpr_read_b32 v64, a104\n"
    "v_mfma_f32_16x16x32_bf16 a[216:219], v[164:167], v[152:155], a[216:219]\n"
    "s_waitcnt lgkmcnt(2)\n"
    "buffer_load_dwordx4 v25, s[64:67], s88 offen lds\n"
    "ds_read_b64_tr_b16 a[134:135], v6 offset:16384\n"
    "v_accvgpr_read_b32 v65, a105\n"
    "v_accvgpr_read_b32 v66, a106\n"
    "v_accvgpr_read_b32 v67, a107\n"
    "v_accvgpr_read_b32 v68, a108\n"
    "v_accvgpr_read_b32 v69, a109\n"
    "v_mfma_f32_16x16x32_bf16 a[196:199], v[172:175], v[128:131], a[196:199]\n"
    "ds_read_b64_tr_b16 a[136:137], v5 offset:512\n"
    "v_accvgpr_read_b32 v70, a110\n"
    "v_accvgpr_read_b32 v71, a111\n"
    "v_mul_f32 v64, v64, v45\n"
    "v_mul_f32 v65, v65, v45\n"
    "v_mul_f32 v66, v66, v45\n"
    "v_mul_f32 v67, v67, v45\n"
    "v_mul_f32 v68, v68, v45\n"
    "v_mfma_f32_16x16x32_bf16 a[204:207], v[172:175], v[144:147], a[204:207]\n"
    "ds_read_b64_tr_b16 a[138:139], v5 offset:16896\n"
    "v_mul_f32 v69, v69, v45\n"
    "v_mul_f32 v70, v70, v45\n"
    "v_mul_f32 v71, v71, v45\n"
    "v_cvt_pk_bf16_f32 v88, v64, v65\n"
    "v_cvt_pk_bf16_f32 v89, v66, v67\n"
    "v_cvt_pk_bf16_f32 v90, v68, v69\n"
    "v_mfma_f32_16x16x32_bf16 a[212:215], v[172:175], v[136:139], a[212:215]\n"
    "buffer_load_dwordx4 v26, s[64:67], s88 offen offset:1024 lds\n"
    "ds_read_b64_tr_b16 a[140:141], v6 offset:512\n"
    "v_cvt_pk_bf16_f32 v91, v70, v71\n"
    "ds_write_b128 v53, v[88:91] offset:7168\n"
    "v_accvgpr_read_b32 v72, a120\n"
    "v_accvgpr_read_b32 v73, a121\n"
    "v_accvgpr_read_b32 v74, a122\n"
    "v_mfma_f32_16x16x32_bf16 a[220:223], v[172:175], v[152:155], a[220:223]\n"
    "ds_read_b64_tr_b16 a[142:143], v6 offset:16896\n"
    "v_accvgpr_read_b32 v75, a123\n"
    "v_accvgpr_read_b32 v76, a124\n"
    "v_accvgpr_read_b32 v77, a125\n"
    "v_accvgpr_read_b32 v78, a126\n"
    "v_accvgpr_read_b32 v79, a127\n"
    "v_mul_f32 v72, v72, v47\n"
    "v_mul_f32 v73, v73, v47\n"
    "v_mfma_f32_16x16x32_bf16 a[224:227], v[180:183], v[128:131], a[224:227]\n"
    "ds_read_b64_tr_b16 a[144:145], v5 offset:1024\n"
    "v_mul_f32 v74, v74, v47\n"
    "v_mul_f32 v75, v75, v47\n"
    "v_mul_f32 v76, v76, v47\n"
    "v_mul_f32 v77, v77, v47\n"
    "v_mul_f32 v78, v78, v47\n"
    "v_mul_f32 v79, v79, v47\n"
    "v_mfma_f32_16x16x32_bf16 a[232:235], v[180:183], v[144:147], a[232:235]\n"
    "buffer_load_dwordx4 v27, s[64:67], s88 offen offset:2048 lds\n"
    "ds_read_b64_tr_b16 a[146:147], v5 offset:17408\n"
    "v_cvt_pk_bf16_f32 v92, v72, v73\n"
    "v_cvt_pk_bf16_f32 v93, v74, v75\n"
    "v_cvt_pk_bf16_f32 v94, v76, v77\n"
    "v_cvt_pk_bf16_f32 v95, v78, v79\n"
    "ds_write_b128 v53, v[92:95] offset:39936\n"
    "v_mfma_f32_16x16x32_bf16 a[240:243], v[180:183], v[136:139], a[240:243]\n"
    "ds_read_b64_tr_b16 a[148:149], v6 offset:1024\n"
    "v_mfma_f32_16x16x32_bf16 a[248:251], v[180:183], v[152:155], a[248:251]\n"
    "ds_read_b64_tr_b16 a[150:151], v6 offset:17408\n"
    "s_waitcnt lgkmcnt(2)\n"
    "s_barrier\n"
    "v_mfma_f32_16x16x32_bf16 a[228:231], v[188:191], v[128:131], a[228:231]\n"
    "buffer_load_dwordx4 v28, s[64:67], s88 offen offset:3072 lds\n"
    "ds_read_b64_tr_b16 a[152:153], v5 offset:1536\n"
    "ds_read_b128 a[176:179], v2\n"
    "v_mfma_f32_16x16x32_bf16 a[236:239], v[188:191], v[144:147], a[236:239]\n"
    "s_add_u32 s88, s88, 0x4000\n"
    "ds_read_b64_tr_b16 a[154:155], v5 offset:17920\n"
    "ds_read_b128 a[184:187], v2 offset:1024\n"
    "v_mfma_f32_16x16x32_bf16 a[244:247], v[188:191], v[136:139], a[244:247]\n"
    "ds_read_b64_tr_b16 a[156:157], v6 offset:1536\n"
    "ds_read_b128 a[180:183], v2 offset:32768\n"
    "v_mfma_f32_16x16x32_bf16 a[252:255], v[188:191], v[152:155], a[252:255]\n"
    "s_waitcnt lgkmcnt(4)\n"
    "ds_read_b64_tr_b16 a[158:159], v6 offset:17920\n"
    "ds_read_b128 a[188:191], v2 offset:33792\n"
    "; PHASE T4\n"
    "v_mfma_f32_16x16x32_bf16 v[128:131], a[128:131], a[176:179], 0\n"
    "s_waitcnt lgkmcnt(4)\n"
    "v_mfma_f32_16x16x32_bf16 v[136:139], a[128:131], a[184:187], 0\n"
    "s_waitcnt lgkmcnt(2)\n"
    "v_mfma_f32_16x16x32_bf16 v[144:147], a[128:131], a[180:183], 0\n"
    "s_waitcnt lgkmcnt(0)\n"
    "v_mfma_f32_16x16x32_bf16 v[152:155], a[128:131], a[188:191], 0\n"
    "v_mfma_f32_16x16x32_bf16 v[132:135], a[132:135], a[176:179], 0\n"
    "ds_read_b64_tr_b16 a[128:129], v3 offset:8192\n"
    "v_mfma_f32_16x16x32_bf16 v[140:143], a[132:135], a[184:187], 0\n"
    "s_waitcnt vmcnt(12)\n"
    "ds_read_b64_tr_b16 a[130:131], v3 offset:12288\n"
    "v_mfma_f32_16x16x32_bf16 v[148:151], a[132:135], a[180:183], 0\n"
    "ds_read_b128 a[160:163], v2 offset:2048\n"
    "s_add_u32 m0, s91, 0x10000\n"
    "v_mfma_f32_16x16x32_bf16 v[156:159], a[132:135], a[188:191], 0\n"
    "buffer_load_dwordx4 v25, s[64:67], s88 offen lds\n"
    "v_mfma_f32_16x16x32_bf16 v[160:163], a[136:139], a[176:179], 0\n"
    "ds_read_b64_tr_b16 a[132:133], v4 offset:8192\n"
    "v_mfma_f32_16x16x32_bf16 v[168:171], a[136:139], a[184:187], 0\n"
    "ds_read_b64_tr_b16 a[134:135], v4 offset:12288\n"
    "v_mfma_f32_16x16x32_bf16 v[176:179], a[136:139], a[180:183], 0\n"
    "ds_read_b128 a[168:171], v2 offset:3072\n"
    "buffer_load_dwordx4 v26, s[64:67], s88 offen offset:1024 lds\n"
    "v_mfma_f32_16x16x32_bf16 v[184:187], a[136:139], a[188:191], 0\n"
    "v_mfma_f32_16x16x32_bf16 v[164:167], a[140:143], a[176:179], 0\n"
    "ds_read_b64_tr_b16 a[136:137], v3 offset:8704\n"
    "v_mfma_f32_16x16x32_bf16 v[172:175], a[140:143], a[184:187], 0\n"
    "ds_read_b64_tr_b16 a[138:139], v3 offset:12800\n"
    "buffer_load_dwordx4 v27, s[64:67], s88 offen offset:2048 lds\n"
    "v_mfma_f32_16x16x32_bf16 v[180:183], a[140:143], a[180:183], 0\n"
    "ds_read_b128 a[164:167], v2 offset:34816\n"
    "v_mfma_f32_16x16x32_bf16 v[188:191], a[140:143], a[188:191], 0\n"
    "v_mfma_f32_16x16x32_bf16 v[192:195], a[144:147], a[176:179], 0\n"
    "ds_read_b64_tr_b16 a[140:141], v4 offset:8704\n"
    "buffer_load_dwordx4 v28, s[64:67], s88 offen offset:3072 lds\n"
    "v_mfma_f32_16x16x32_bf16 v[200:203], a[144:147], a[184:187], 0\n"
    "ds_read_b64_tr_b16 a[142:143], v4 offset:12800\n"
    "s_add_u32 s88, s88, 0x4000\n"
    "v_mfma_f32_16x16x32_bf16 v[208:211], a[144:147], a[180:183], 0\n"
    "ds_read_b128 a[172:175], v2 offset:35840\n"
    "v_mfma_f32_16x16x32_bf16 v[216:219], a[144:147], a[188:191], 0\n"
    "s_add_u32 m0, s91, 0x14000\n"
    "v_mfma_f32_16x16x32_bf16 v[196:199], a[148:151], a[176:179], 0\n"
    "ds_read_b64_tr_b16 a[144:145], v3 offset:9216\n"
    "buffer_load_dwordx4 v25, s[64:67], s88 offen lds\n"
    "v_mfma_f32_16x16x32_bf16 v[204:207], a[148:151], a[184:187], 0\n"
    "ds_read_b64_tr_b16 a[146:147], v3 offset:13312\n"
    "v_mfma_f32_16x16x32_bf16 v[212:215], a[148:151], a[180:183], 0\n"
    "v_mfma_f32_16x16x32_bf16 v[220:223], a[148:151], a[188:191], 0\n"
    "buffer_load_dwordx4 v26, s[64:67], s88 offen offset:1024 lds\n"
    "v_mfma_f32_16x16x32_bf16 v[224:227], a[152:155], a[176:179], 0\n"
    "ds_read_b64_tr_b16 a[148:149], v4 offset:9216\n"
    "v_mfma_f32_16x16x32_bf16 v[232:235], a[152:155], a[184:187], 0\n"
    "s_waitcnt lgkmcnt(3)\n"
    "ds_read_b64_tr_b16 a[150:151], v4 offset:13312\n"
    "v_mfma_f32_16x16x32_bf16 v[240:243], a[152:155], a[180:183], 0\n"
    "buffer_load_dwordx4 v27, s[64:67], s88 offen offset:2048 lds\n"
    "v_mfma_f32_16x16x32_bf16 v[248:251], a[152:155], a[188:191], 0\n"
    "v_mfma_f32_16x16x32_bf16 v[228:231], a[156:159], a[176:179], 0\n"
    "ds_read_b64_tr_b16 a[152:153], v3 offset:9728\n"
    "v_mfma_f32_16x16x32_bf16 v[236:239], a[156:159], a[184:187], 0\n"
    "ds_read_b64_tr_b16 a[154:155], v3 offset:13824\n"
    "buffer_load_dwordx4 v28, s[64:67], s88 offen offset:3072 lds\n"
    "v_mfma_f32_16x16x32_bf16 v[244:247], a[156:159], a[180:183], 0\n"
    "s_add_u32 s88, s88, 0x4000\n"
    "v_mfma_f32_16x16x32_bf16 v[252:255], a[156:159], a[188:191], 0\n"
    "v_mfma_f32_16x16x32_bf16 v[128:131], a[128:131], a[160:163], v[128:131]\n"
    "ds_read_b64_tr_b16 a[156:157], v4 offset:9728\n"
    "v_mfma_f32_16x16x32_bf16 v[136:139], a[128:131], a[168:171], v[136:139]\n"
    "ds_read_b64_tr_b16 a[158:159], v4 offset:13824\n"
    "v_mfma_f32_16x16x32_bf16 v[144:147], a[128:131], a[164:167], v[144:147]\n"
    "v_mfma_f32_16x16x32_bf16 v[152:155], a[128:131], a[172:175], v[152:155]\n"
    "v_mfma_f32_16x16x32_bf16 v[132:135], a[132:135], a[160:163], v[132:135]\n"
    "s_waitcnt vmcnt(13)\n"
    "ds_read_b64_tr_b16 a[128:129], v3\n"
    "v_mfma_f32_16x16x32_bf16 v[140:143], a[132:135], a[168:171], v[140:143]\n"
    "s_waitcnt vmcnt(8)\n"
    "ds_read_b64_tr_b16 a[130:131], v3 offset:4096\n"
    "v_mfma_f32_16x16x32_bf16 v[148:151], a[132:135], a[164:167], v[148:151]\n"
    "ds_read_b128 a[176:179], v2 offset:4096\n"
    "s_add_u32 m0, s89, 0x2000\n"
    "v_mfma_f32_16x16x32_bf16 v[156:159], a[132:135], a[172:175], v[156:159]\n"
    "s_waitcnt lgkmcnt(4)\n"
    "buffer_load_dwordx4 v25, s[64:67], s88 offen lds\n"
    "v_mfma_f32_16x16x32_bf16 v[160:163], a[136:139], a[160:163], v[160:163]\n"
    "ds_read_b64_tr_b16 a[132:133], v4\n"
    "v_mfma_f32_16x16x32_bf16 v[168:171], a[136:139], a[168:171], v[168:171]\n"
    "ds_read_b64_tr_b16 a[134:135], v4 offset:4096\n"
    "v_mfma_f32_16x16x32_bf16 v[176:179], a[136:139], a[164:167], v[176:179]\n"
    "ds_read_b128 a[184:187], v2 offset:5120\n"
    "buffer_load_dwordx4 v26, s[64:67], s88 offen offset:1024 lds\n"
    "v_mfma_f32_16x16x32_bf16 v[184:187], a[136:139], a[172:175], v[184:187]\n"
    "v_mfma_f32_16x16x32_bf16 v[164:167], a[140:143], a[160:163], v[164:167]\n"
    "ds_read_b64_tr_b16 a[136:137], v3 offset:512\n"
    "v_mfma_f32_16x16x32_bf16 v[172:175], a[140:143], a[168:171], v[172:175]\n"
    "ds_read_b64_tr_b16 a[138:139], v3 offset:4608\n"
    "buffer_load_dwordx4 v27, s[64:67], s88 offen offset:2048 lds\n"
    "v_mfma_f32_16x16x32_bf16 v[180:183], a[140:143], a[164:167], v[180:183]\n"
    "ds_read_b128 a[180:183], v2 offset:36864\n"
    "v_mfma_f32_16x16x32_bf16 v[188:191], a[140:143], a[172:175], v[188:191]\n"
    "v_mfma_f32_16x16x32_bf16 v[192:195], a[144:147], a[160:163], v[192:195]\n"
    "ds_read_b64_tr_b16 a[140:141], v4 offset:512\n"
    "buffer_load_dwordx4 v28, s[64:67], s88 offen offset:3072 lds\n"
    "v_mfma_f32_16x16x32_bf16 v[200:203], a[144:147], a[168:171], v[200:203]\n"
    "ds_read_b64_tr_b16 a[142:143], v4 offset:4608\n"
    "s_add_u32 s88, s88, 0x4000\n"
    "v_mfma_f32_16x16x32_bf16 v[208:211], a[144:147], a[164:167], v[208:211]\n"
    "ds_read_b128 a[188:191], v2 offset:37888\n"
    "v_mfma_f32_16x16x32_bf16 v[216:219], a[144:147], a[172:175], v[216:219]\n"
    "s_add_u32 m0, s89, 0x3000\n"
    "v_mfma_f32_16x16x32_bf16 v[196:199], a[148:151], a[160:163], v[196:199]\n"
    "ds_read_b64_tr_b16 a[144:145], v3 offset:1024\n"
    "s_waitcnt lgkmcnt(4)\n"
    "buffer_load_dwordx4 v25, s[64:67], s88 offen lds\n"
    "v_mfma_f32_16x16x32_bf16 v[204:207], a[148:151], a[168:171], v[204:207]\n"
    "ds_read_b64_tr_b16 a[146:147], v3 offset:5120\n"
    "v_mfma_f32_16x16x32_bf16 v[212:215], a[148:151], a[164:167], v[212:215]\n"
    "v_mfma_f32_16x16x32_bf16 v[220:223], a[148:151], a[172:175], v[220:223]\n"
    "buffer_load_dwordx4 v26, s[64:67], s88 offen offset:1024 lds\n"
    "v_mfma_f32_16x16x32_bf16 v[224:227], a[152:155], a[160:163], v[224:227]\n"
    "ds_read_b64_tr_b16 a[148:149], v4 offset:1024\n"
    "v_mfma_f32_16x16x32_bf16 v[232:235], a[152:155], a[168:171], v[232:235]\n"
    "ds_read_b64_tr_b16 a[150:151], v4 offset:5120\n"
    "v_mfma_f32_16x16x32_bf16 v[240:243], a[152:155], a[164:167], v[240:243]\n"
    "buffer_load_dwordx4 v27, s[64:67], s88 offen offset:2048 lds\n"
    "v_mfma_f32_16x16x32_bf16 v[248:251], a[152:155], a[172:175], v[248:251]\n"
    "v_mfma_f32_16x16x32_bf16 v[228:231], a[156:159], a[160:163], v[228:231]\n"
    "ds_read_b64_tr_b16 a[152:153], v3 offset:1536\n"
    "v_mfma_f32_16x16x32_bf16 v[236:239], a[156:159], a[168:171], v[236:239]\n"
    "ds_read_b64_tr_b16 a[154:155], v3 offset:5632\n"
    "buffer_load_dwordx4 v28, s[64:67], s88 offen offset:3072 lds\n"
    "v_mfma_f32_16x16x32_bf16 v[244:247], a[156:159], a[164:167], v[244:247]\n"
    "s_add_u32 s88, s88, 0x4000\n"
    "v_mfma_f32_16x16x32_bf16 v[252:255], a[156:159], a[172:175], v[252:255]\n"
    "v_mfma_f32_16x16x32_bf16 v[128:131], a[128:131], a[176:179], v[128:131]\n"
    "ds_read_b64_tr_b16 a[156:157], v4 offset:1536\n"
    "v_mfma_f32_16x16x32_bf16 v[136:139], a[128:131], a[184:187], v[136:139]\n"
    "ds_read_b64_tr_b16 a[158:159], v4 offset:5632\n"
    "v_mfma_f32_16x16x32_bf16 v[144:147], a[128:131], a[180:183], v[144:147]\n"
    "s_waitcnt lgkmcnt(4)\n"
    "v_mfma_f32_16x16x32_bf16 v[152:155], a[128:131], a[188:191], v[152:155]\n"
    "v_mfma_f32_16x16x32_bf16 v[132:135], a[132:135], a[176:179], v[132:135]\n"
    "s_waitcnt vmcnt(12)\n"
    "ds_read_b64_tr_b16 a[128:129], v5\n"
    "v_mfma_f32_16x16x32_bf16 v[140:143], a[132:135], a[184:187], v[140:143]\n"
    "s_waitcnt vmcnt(8)\n"
    "ds_read_b64_tr_b16 a[130:131], v5 offset:16384\n"
    "v_mfma_f32_16x16x32_bf16 v[148:151], a[132:135], a[180:183], v[148:151]\n"
    "ds_read_b128 a[160:163], v2 offset:6144\n"
    "s_add_u32 m0, s89, 0\n"
    "v_mfma_f32_16x16x32_bf16 v[156:159], a[132:135], a[188:191], v[156:159]\n"
    "s_waitcnt lgkmcnt(3)\n"
    "buffer_load_dwordx4 v25, s[64:67], s88 offen lds\n"
    "v_mfma_f32_16x16x32_bf16 v[160:163], a[136:139], a[176:179], v[160:163]\n"
    "ds_read_b64_tr_b16 a[132:133], v6\n"
    "v_mfma_f32_16x16x32_bf16 v[168:171], a[136:139], a[184:187], v[168:171]\n"
    "ds_read_b64_tr_b16 a[134:135], v6 offset:16384\n"
    "v_mfma_f32_16x16x32_bf16 v[176:179], a[136:139], a[180:183], v[176:179]\n"
    "ds_read_b128 a[168:171], v2 offset:7168\n"
    "buffer_load_dwordx4 v26, s[64:67], s88 offen offset:1024 lds\n"
    "v_mfma_f32_16x16x32_bf16 v[184:187], a[136:139], a[188:191], v[184:187]\n"
    "v_mfma_f32_16x16x32_bf16 v[164:167], a[140:143], a[176:179], v[164:167]\n"
    "ds_read_b64_tr_b16 a[136:137], v5 offset:512\n"
    "v_mfma_f32_16x16x32_bf16 v[172:175], a[140:143], a[184:187], v[172:175]\n"
    "ds_read_b64_tr_b16 a[138:139], v5 offset:16896\n"
    "buffer_load_dwordx4 v27, s[64:67], s88 offen offset:2048 lds\n"
    "v_mfma_f32_16x16x32_bf16 v[180:183], a[140:143], a[180:183], v[180:183]\n"
    "ds_read_b128 a[164:167], v2 offset:38912\n"
    "v_mfma_f32_16x16x32_bf16 v[188:191], a[140:143], a[188:191], v[188:191]\n"
    "v_mfma_f32_16x16x32_bf16 v[192:195], a[144:147], a[176:179], v[192:195]\n"
    "ds_read_b64_tr_b16 a[140:141], v6 offset:512\n"
    "buffer_load_dwordx4 v28, s[64:67], s88 offen offset:3072 lds\n"
    "v_mfma_f32_16x16x32_bf16 v[200:203], a[144:147], a[184:187], v[200:203]\n"
    "ds_read_b64_tr_b16 a[142:143], v6 offset:16896\n"
    "s_add_u32 s88, s88, 0x4000\n"
    "v_mfma_f32_16x16x32_bf16 v[208:211], a[144:147], a[180:183], v[208:211]\n"
    "ds_read_b128 a[172:175], v2 offset:39936\n"
    "v_mfma_f32_16x16x32_bf16 v[216:219], a[144:147], a[188:191], v[216:219]\n"
    "s_add_u32 m0, s89, 0x1000\n"
    "v_mfma_f32_16x16x32_bf16 v[196:199], a[148:151], a[176:179], v[196:199]\n"
    "ds_read_b64_tr_b16 a[144:145], v5 offset:1024\n"
    "buffer_load_dwordx4 v25, s[64:67], s88 offen lds\n"
    "v_mfma_f32_16x16x32_bf16 v[204:207], a[148:151], a[184:187], v[204:207]\n"
    "ds_read_b64_tr_b16 a[146:147], v5 offset:17408\n"
    "v_mfma_f32_16x16x32_bf16 v[212:215], a[148:151], a[180:183], v[212:215]\n"
    "v_mfma_f32_16x16x32_bf16 v[220:223], a[148:151], a[188:191], v[220:223]\n"
    "buffer_load_dwordx4 v26, s[64:67], s88 offen offset:1024 lds\n"
    "v_mfma_f32_16x16x32_bf16 v[224:227], a[152:155], a[176:179], v[224:227]\n"
    "ds_read_b64_tr_b16 a[148:149], v6 offset:1024\n"
    "v_mfma_f32_16x16x32_bf16 v[232:235], a[152:155], a[184:187], v[232:235]\n"
    "s_waitcnt lgkmcnt(3)\n"
    "ds_read_b64_tr_b16 a[150:151], v6 offset:17408\n"
    "v_mfma_f32_16x16x32_bf16 v[240:243], a[152:155], a[180:183], v[240:243]\n"
    "buffer_load_dwordx4 v27, s[64:67], s88 offen offset:2048 lds\n"
    "v_mfma_f32_16x16x32_bf16 v[248:251], a[152:155], a[188:191], v[248:251]\n"
    "v_mfma_f32_16x16x32_bf16 v[228:231], a[156:159], a[176:179], v[228:231]\n"
    "ds_read_b64_tr_b16 a[152:153], v5 offset:1536\n"
    "v_mfma_f32_16x16x32_bf16 v[236:239], a[156:159], a[184:187], v[236:239]\n"
    "ds_read_b64_tr_b16 a[154:155], v5 offset:17920\n"
    "buffer_load_dwordx4 v28, s[64:67], s88 offen offset:3072 lds\n"
    "v_mfma_f32_16x16x32_bf16 v[244:247], a[156:159], a[180:183], v[244:247]\n"
    "s_add_u32 s88, s88, 0x4000\n"
    "v_mfma_f32_16x16x32_bf16 v[252:255], a[156:159], a[188:191], v[252:255]\n"
    "v_mfma_f32_16x16x32_bf16 v[128:131], a[128:131], a[160:163], v[128:131]\n"
    "ds_read_b64_tr_b16 a[156:157], v6 offset:1536\n"
    "v_mfma_f32_16x16x32_bf16 v[136:139], a[128:131], a[168:171], v[136:139]\n"
    "ds_read_b64_tr_b16 a[158:159], v6 offset:17920\n"
    "v_mfma_f32_16x16x32_bf16 v[144:147], a[128:131], a[164:167], v[144:147]\n"
    "v_mfma_f32_16x16x32_bf16 v[152:155], a[128:131], a[172:175], v[152:155]\n"
    "v_mfma_f32_16x16x32_bf16 v[132:135], a[132:135], a[160:163], v[132:135]\n"
    "s_waitcnt vmcnt(12)\n"
    "ds_read_b64_tr_b16 a[128:129], v3 offset:8192\n"
    "v_mfma_f32_16x16x32_bf16 v[140:143], a[132:135], a[168:171], v[140:143]\n"
    "s_waitcnt vmcnt(8)\n"
    "ds_read_b64_tr_b16 a[130:131], v3 offset:12288\n"
    "v_mfma_f32_16x16x32_bf16 v[148:151], a[132:135], a[164:167], v[148:151]\n"
    "ds_read_b128 a[176:179], v2 offset:8192\n"
    "s_add_u32 m0, s91, 0x10000\n"
    "v_mfma_f32_16x16x32_bf16 v[156:159], a[132:135], a[172:175], v[156:159]\n"
    "s_waitcnt lgkmcnt(4)\n"
    "buffer_load_dwordx4 v25, s[64:67], s88 offen lds\n"
    "v_mfma_f32_16x16x32_bf16 v[160:163], a[136:139], a[160:163], v[160:163]\n"
    "ds_read_b64_tr_b16 a[132:133], v4 offset:8192\n"
    "v_mfma_f32_16x16x32_bf16 v[168:171], a[136:139], a[168:171], v[168:171]\n"
    "ds_read_b64_tr_b16 a[134:135], v4 offset:12288\n"
    "v_mfma_f32_16x16x32_bf16 v[176:179], a[136:139], a[164:167], v[176:179]\n"
    "ds_read_b128 a[184:187], v2 offset:9216\n"
    "buffer_load_dwordx4 v26, s[64:67], s88 offen offset:1024 lds\n"
    "v_mfma_f32_16x16x32_bf16 v[184:187], a[136:139], a[172:175], v[184:187]\n"
    "v_mfma_f32_16x16x32_bf16 v[164:167], a[140:143], a[160:163], v[164:167]\n"
    "ds_read_b64_tr_b16 a[136:137], v3 offset:8704\n"
    "v_mfma_f32_16x16x32_bf16 v[172:175], a[140:143], a[168:171], v[172:175]\n"
    "ds_read_b64_tr_b16 a[138:139], v3 offset:12800\n"
    "buffer_load_dwordx4 v27, s[64:67], s88 offen offset:2048 lds\n"
    "v_mfma_f32_16x16x32_bf16 v[180:183], a[140:143], a[164:167], v[180:183]\n"
    "ds_read_b128 a[180:183], v2 offset:40960\n"
    "v_mfma_f32_16x16x32_bf16 v[188:191], a[140:143], a[172:175], v[188:191]\n"
    "v_mfma_f32_16x16x32_bf16 v[192:195], a[144:147], a[160:163], v[192:195]\n"
    "ds_read_b64_tr_b16 a[140:141], v4 offset:8704\n"
    "buffer_load_dwordx4 v28, s[64:67], s88 offen offset:3072 lds\n"
    "v_mfma_f32_16x16x32_bf16 v[200:203], a[144:147], a[168:171], v[200:203]\n"
    "ds_read_b64_tr_b16 a[142:143], v4 offset:12800\n"
    "s_add_u32 s88, s88, 0x4000\n"
    "v_mfma_f32_16x16x32_bf16 v[208:211], a[144:147], a[164:167], v[208:211]\n"
    "ds_read_b128 a[188:191], v2 offset:41984\n"
    "v_mfma_f32_16x16x32_bf16 v[216:219], a[144:147], a[172:175], v[216:219]\n"
    "s_add_u32 m0, s91, 0x14000\n"
    "v_mfma_f32_16x16x32_bf16 v[196:199], a[148:151], a[160:163], v[196:199]\n"
    "ds_read_b64_tr_b16 a[144:145], v3 offset:9216\n"
    "s_waitcnt lgkmcnt(4)\n"
    "buffer_load_dwordx4 v25, s[64:67], s88 offen lds\n"
    "v_mfma_f32_16x16x32_bf16 v[204:207], a[148:151], a[168:171], v[204:207]\n"
    "ds_read_b64_tr_b16 a[146:147], v3 offset:13312\n"
    "v_mfma_f32_16x16x32_bf16 v[212:215], a[148:151], a[164:167], v[212:215]\n"
    "v_mfma_f32_16x16x32_bf16 v[220:223], a[148:151], a[172:175], v[220:223]\n"
    "buffer_load_dwordx4 v26, s[64:67], s88 offen offset:1024 lds\n"
    "v_mfma_f32_16x16x32_bf16 v[224:227], a[152:155], a[160:163], v[224:227]\n"
    "ds_read_b64_tr_b16 a[148:149], v4 offset:9216\n"
    "v_mfma_f32_16x16x32_bf16 v[232:235], a[152:155], a[168:171], v[232:235]\n"
    "ds_read_b64_tr_b16 a[150:151], v4 offset:13312\n"
    "v_mfma_f32_16x16x32_bf16 v[240:243], a[152:155], a[164:167], v[240:243]\n"
    "buffer_load_dwordx4 v27, s[64:67], s88 offen offset:2048 lds\n"
    "v_mfma_f32_16x16x32_bf16 v[248:251], a[152:155], a[172:175], v[248:251]\n"
    "v_mfma_f32_16x16x32_bf16 v[228:231], a[156:159], a[160:163], v[228:231]\n"
    "ds_read_b64_tr_b16 a[152:153], v3 offset:9728\n"
    "v_mfma_f32_16x16x32_bf16 v[236:239], a[156:159], a[168:171], v[236:239]\n"
    "ds_read_b64_tr_b16 a[154:155], v3 offset:13824\n"
    "buffer_load_dwordx4 v28, s[64:67], s88 offen offset:3072 lds\n"
    "v_mfma_f32_16x16x32_bf16 v[244:247], a[156:159], a[164:167], v[244:247]\n"
    "s_add_u32 s88, s88, 0x4000\n"
    "v_mfma_f32_16x16x32_bf16 v[252:255], a[156:159], a[172:175], v[252:255]\n"
    "v_mfma_f32_16x16x32_bf16 v[128:131], a[128:131], a[176:179], v[128:131]\n"
    "ds_read_b64_tr_b16 a[156:157], v4 offset:9728\n"
    "v_mfma_f32_16x16x32_bf16 v[136:139], a[128:131], a[184:187], v[136:139]\n"
    "ds_read_b64_tr_b16 a[158:159], v4 offset:13824\n"
    "v_mfma_f32_16x16x32_bf16 v[144:147], a[128:131], a[180:183], v[144:147]\n"
    "s_waitcnt lgkmcnt(4)\n"
    "v_mfma_f32_16x16x32_bf16 v[152:155], a[128:131], a[188:191], v[152:155]\n"
    "v_mfma_f32_16x16x32_bf16 v[132:135], a[132:135], a[176:179], v[132:135]\n"
    "s_waitcnt vmcnt(12)\n"
    "ds_read_b64_tr_b16 a[128:129], v3\n"
    "v_mfma_f32_16x16x32_bf16 v[140:143], a[132:135], a[184:187], v[140:143]\n"
    "s_waitcnt vmcnt(8)\n"
    "ds_read_b64_tr_b16 a[130:131], v3 offset:4096\n"
    "v_mfma_f32_16x16x32_bf16 v[148:151], a[132:135], a[180:183], v[148:151]\n"
    "ds_read_b128 a[160:163], v2 offset:10240\n"
    "s_add_u32 m0, s89, 0x2000\n"
    "v_mfma_f32_16x16x32_bf16 v[156:159], a[132:135], a[188:191], v[156:159]\n"
    "s_waitcnt lgkmcnt(3)\n"
    "buffer_load_dwordx4 v25, s[64:67], s88 offen lds\n"
    "v_mfma_f32_16x16x32_bf16 v[160:163], a[136:139], a[176:179], v[160:163]\n"
    "ds_read_b64_tr_b16 a[132:133], v4\n"
    "v_mfma_f32_16x16x32_bf16 v[168:171], a[136:139], a[184:187], v[168:171]\n"
    "ds_read_b64_tr_b16 a[134:135], v4 offset:4096\n"
    "v_mfma_f32_16x16x32_bf16 v[176:179], a[136:139], a[180:183], v[176:179]\n"
    "ds_read_b128 a[168:171], v2 offset:11264\n"
    "buffer_load_dwordx4 v26, s[64:67], s88 offen offset:1024 lds\n"
    "v_mfma_f32_16x16x32_bf16 v[184:187], a[136:139], a[188:191], v[184:187]\n"
    "v_mfma_f32_16x16x32_bf16 v[164:167], a[140:143], a[176:179], v[164:167]\n"
    "ds_read_b64_tr_b16 a[136:137], v3 offset:512\n"
    "v_mfma_f32_16x16x32_bf16 v[172:175], a[140:143], a[184:187], v[172:175]\n"
    "ds_read_b64_tr_b16 a[138:139], v3 offset:4608\n"
    "buffer_load_dwordx4 v27, s[64:67], s88 offen offset:2048 lds\n"
    "v_mfma_f32_16x16x32_bf16 v[180:183], a[140:143], a[180:183], v[180:183]\n"
    "ds_read_b128 a[164:167], v2 offset:43008\n"
    "v_mfma_f32_16x16x32_bf16 v[188:191], a[140:143], a[188:191], v[188:191]\n"
    "v_mfma_f32_16x16x32_bf16 v[192:195], a[144:147], a[176:179], v[192:195]\n"
    "ds_read_b64_tr_b16 a[140:141], v4 offset:512\n"
    "buffer_load_dwordx4 v28, s[64:67], s88 offen offset:3072 lds\n"
    "v_mfma_f32_16x16x32_bf16 v[200:203], a[144:147], a[184:187], v[200:203]\n"
    "ds_read_b64_tr_b16 a[142:143], v4 offset:4608\n"
    "s_add_u32 s88, s88, 0x4000\n"
    "v_mfma_f32_16x16x32_bf16 v[208:211], a[144:147], a[180:183], v[208:211]\n"
    "ds_read_b128 a[172:175], v2 offset:44032\n"
    "v_mfma_f32_16x16x32_bf16 v[216:219], a[144:147], a[188:191], v[216:219]\n"
    "s_add_u32 m0, s89, 0x3000\n"
    "v_mfma_f32_16x16x32_bf16 v[196:199], a[148:151], a[176:179], v[196:199]\n"
    "ds_read_b64_tr_b16 a[144:145], v3 offset:1024\n"
    "buffer_load_dwordx4 v25, s[64:67], s88 offen lds\n"
    "v_mfma_f32_16x16x32_bf16 v[204:207], a[148:151], a[184:187], v[204:207]\n"
    "ds_read_b64_tr_b16 a[146:147], v3 offset:5120\n"
    "v_mfma_f32_16x16x32_bf16 v[212:215], a[148:151], a[180:183], v[212:215]\n"
    "v_mfma_f32_16x16x32_bf16 v[220:223], a[148:151], a[188:191], v[220:223]\n"
    "buffer_load_dwordx4 v26, s[64:67], s88 offen offset:1024 lds\n"
    "v_mfma_f32_16x16x32_bf16 v[224:227], a[152:155], a[176:179], v[224:227]\n"
    "ds_read_b64_tr_b16 a[148:149], v4 offset:1024\n"
    "v_mfma_f32_16x16x32_bf16 v[232:235], a[152:155], a[184:187], v[232:235]\n"
    "s_waitcnt lgkmcnt(3)\n"
    "ds_read_b64_tr_b16 a[150:151], v4 offset:5120\n"
    "v_mfma_f32_16x16x32_bf16 v[240:243], a[152:155], a[180:183], v[240:243]\n"
    "buffer_load_dwordx4 v27, s[64:67], s88 offen offset:2048 lds\n"
    "v_mfma_f32_16x16x32_bf16 v[248:251], a[152:155], a[188:191], v[248:251]\n"
    "v_mfma_f32_16x16x32_bf16 v[228:231], a[156:159], a[176:179], v[228:231]\n"
    "ds_read_b64_tr_b16 a[152:153], v3 offset:1536\n"
    "v_mfma_f32_16x16x32_bf16 v[236:239], a[156:159], a[184:187], v[236:239]\n"
    "ds_read_b64_tr_b16 a[154:155], v3 offset:5632\n"
    "buffer_load_dwordx4 v28, s[64:67], s88 offen offset:3072 lds\n"
    "v_mfma_f32_16x16x32_bf16 v[244:247], a[156:159], a[180:183], v[244:247]\n"
    "s_add_u32 s88, s88, 0x4000\n"
    "v_mfma_f32_16x16x32_bf16 v[252:255], a[156:159], a[188:191], v[252:255]\n"
    "v_mfma_f32_16x16x32_bf16 v[128:131], a[128:131], a[160:163], v[128:131]\n"
    "ds_read_b64_tr_b16 a[156:157], v4 offset:1536\n"
    "v_mfma_f32_16x16x32_bf16 v[136:139], a[128:131], a[168:171], v[136:139]\n"
    "ds_read_b64_tr_b16 a[158:159], v4 offset:5632\n"
    "v_mfma_f32_16x16x32_bf16 v[144:147], a[128:131], a[164:167], v[144:147]\n"
    "v_mfma_f32_16x16x32_bf16 v[152:155], a[128:131], a[172:175], v[152:155]\n"
    "v_mfma_f32_16x16x32_bf16 v[132:135], a[132:135], a[160:163], v[132:135]\n"
    "s_waitcnt vmcnt(12)\n"
    "ds_read_b64_tr_b16 a[128:129], v5\n"
    "v_mfma_f32_16x16x32_bf16 v[140:143], a[132:135], a[168:171], v[140:143]\n"
    "s_waitcnt vmcnt(8)\n"
    "ds_read_b64_tr_b16 a[130:131], v5 offset:16384\n"
    "v_mfma_f32_16x16x32_bf16 v[148:151], a[132:135], a[164:167], v[148:151]\n"
    "ds_read_b128 a[176:179], v2 offset:12288\n"
    "s_add_u32 m0, s89, 0\n"
    "v_mfma_f32_16x16x32_bf16 v[156:159], a[132:135], a[172:175], v[156:159]\n"
    "s_waitcnt lgkmcnt(4)\n"
    "buffer_load_dwordx4 v25, s[64:67], s88 offen lds\n"
    "v_mfma_f32_16x16x32_bf16 v[160:163], a[136:139], a[160:163], v[160:163]\n"
    "ds_read_b64_tr_b16 a[132:133], v6\n"
    "v_mfma_f32_16x16x32_bf16 v[168:171], a[136:139], a[168:171], v[168:171]\n"
    "ds_read_b64_tr_b16 a[134:135], v6 offset:16384\n"
    "v_mfma_f32_16x16x32_bf16 v[176:179], a[136:139], a[164:167], v[176:179]\n"
    "ds_read_b128 a[184:187], v2 offset:13312\n"
    "buffer_load_dwordx4 v26, s[64:67], s88 offen offset:1024 lds\n"
    "v_mfma_f32_16x16x32_bf16 v[184:187], a[136:139], a[172:175], v[184:187]\n"
    "v_mfma_f32_16x16x32_bf16 v[164:167], a[140:143], a[160:163], v[164:167]\n"
    "ds_read_b64_tr_b16 a[136:137], v5 offset:512\n"
    "v_mfma_f32_16x16x32_bf16 v[172:175], a[140:143], a[168:171], v[172:175]\n"
    "ds_read_b64_tr_b16 a[138:139], v5 offset:16896\n"
    "buffer_load_dwordx4 v27, s[64:67], s88 offen offset:2048 lds\n"
    "v_mfma_f32_16x16x32_bf16 v[180:183], a[140:143], a[164:167], v[180:183]\n"
    "ds_read_b128 a[180:183], v2 offset:45056\n"
    "v_mfma_f32_16x16x32_bf16 v[188:191], a[140:143], a[172:175], v[188:191]\n"
    "v_mfma_f32_16x16x32_bf16 v[192:195], a[144:147], a[160:163], v[192:195]\n"
    "ds_read_b64_tr_b16 a[140:141], v6 offset:512\n"
    "buffer_load_dwordx4 v28, s[64:67], s88 offen offset:3072 lds\n"
    "v_mfma_f32_16x16x32_bf16 v[200:203], a[144:147], a[168:171], v[200:203]\n"
    "ds_read_b64_tr_b16 a[142:143], v6 offset:16896\n"
    "s_add_u32 s88, s88, 0x4000\n"
    "v_mfma_f32_16x16x32_bf16 v[208:211], a[144:147], a[164:167], v[208:211]\n"
    "ds_read_b128 a[188:191], v2 offset:46080\n"
    "v_mfma_f32_16x16x32_bf16 v[216:219], a[144:147], a[172:175], v[216:219]\n"
    "s_add_u32 m0, s89, 0x1000\n"
    "v_mfma_f32_16x16x32_bf16 v[196:199], a[148:151], a[160:163], v[196:199]\n"
    "ds_read_b64_tr_b16 a[144:145], v5 offset:1024\n"
    "s_waitcnt lgkmcnt(4)\n"
    "buffer_load_dwordx4 v25, s[64:67], s88 offen lds\n"
    "v_mfma_f32_16x16x32_bf16 v[204:207], a[148:151], a[168:171], v[204:207]\n"
    "ds_read_b64_tr_b16 a[146:147], v5 offset:17408\n"
    "v_mfma_f32_16x16x32_bf16 v[212:215], a[148:151], a[164:167], v[212:215]\n"
    "v_mfma_f32_16x16x32_bf16 v[220:223], a[148:151], a[172:175], v[220:223]\n"
    "buffer_load_dwordx4 v26, s[64:67], s88 offen offset:1024 lds\n"
    "v_mfma_f32_16x16x32_bf16 v[224:227], a[152:155], a[160:163], v[224:227]\n"
    "ds_read_b64_tr_b16 a[148:149], v6 offset:1024\n"
    "v_mfma_f32_16x16x32_bf16 v[232:235], a[152:155], a[168:171], v[232:235]\n"
    "ds_read_b64_tr_b16 a[150:151], v6 offset:17408\n"
    "v_mfma_f32_16x16x32_bf16 v[240:243], a[152:155], a[164:167], v[240:243]\n"
    "buffer_load_dwordx4 v27, s[64:67], s88 offen offset:2048 lds\n"
    "v_mfma_f32_16x16x32_bf16 v[248:251], a[152:155], a[172:175], v[248:251]\n"
    "v_mfma_f32_16x16x32_bf16 v[228:231], a[156:159], a[160:163], v[228:231]\n"
    "ds_read_b64_tr_b16 a[152:153], v5 offset:1536\n"
    "v_mfma_f32_16x16x32_bf16 v[236:239], a[156:159], a[168:171], v[236:239]\n"
    "ds_read_b64_tr_b16 a[154:155], v5 offset:17920\n"
    "buffer_load_dwordx4 v28, s[64:67], s88 offen offset:3072 lds\n"
    "v_mfma_f32_16x16x32_bf16 v[244:247], a[156:159], a[164:167], v[244:247]\n"
    "s_add_u32 s88, s88, 0x4000\n"
    "v_mfma_f32_16x16x32_bf16 v[252:255], a[156:159], a[172:175], v[252:255]\n"
    "v_mfma_f32_16x16x32_bf16 v[128:131], a[128:131], a[176:179], v[128:131]\n"
    "ds_read_b64_tr_b16 a[156:157], v6 offset:1536\n"
    "v_mfma_f32_16x16x32_bf16 v[136:139], a[128:131], a[184:187], v[136:139]\n"
    "ds_read_b64_tr_b16 a[158:159], v6 offset:17920\n"
    "v_mfma_f32_16x16x32_bf16 v[144:147], a[128:131], a[180:183], v[144:147]\n"
    "s_waitcnt lgkmcnt(4)\n"
    "v_mfma_f32_16x16x32_bf16 v[152:155], a[128:131], a[188:191], v[152:155]\n"
    "v_mfma_f32_16x16x32_bf16 v[132:135], a[132:135], a[176:179], v[132:135]\n"
    "s_waitcnt vmcnt(12)\n"
    "ds_read_b64_tr_b16 a[128:129], v3 offset:8192\n"
    "v_mfma_f32_16x16x32_bf16 v[140:143], a[132:135], a[184:187], v[140:143]\n"
    "s_waitcnt vmcnt(8)\n"
    "ds_read_b64_tr_b16 a[130:131], v3 offset:12288\n"
    "v_mfma_f32_16x16x32_bf16 v[148:151], a[132:135], a[180:183], v[148:151]\n"
    "ds_read_b128 a[160:163], v2 offset:14336\n"
    "s_add_u32 m0, s91, 0x10000\n"
    "v_mfma_f32_16x16x32_bf16 v[156:159], a[132:135], a[188:191], v[156:159]\n"
    "s_waitcnt lgkmcnt(3)\n"
    "buffer_load_dwordx4 v25, s[64:67], s88 offen lds\n"
    "v_mfma_f32_16x16x32_bf16 v[160:163], a[136:139], a[176:179], v[160:163]\n"
    "ds_read_b64_tr_b16 a[132:133], v4 offset:8192\n"
    "v_mfma_f32_16x16x32_bf16 v[168:171], a[136:139], a[184:187], v[168:171]\n"
    "ds_read_b64_tr_b16 a[134:135], v4 offset:12288\n"
    "v_mfma_f32_16x16x32_bf16 v[176:179], a[136:139], a[180:183], v[176:179]\n"
    "ds_read_b128 a[168:171], v2 offset:15360\n"
    "buffer_load_dwordx4 v26, s[64:67], s88 offen offset:1024 lds\n"
    "v_mfma_f32_16x16x32_bf16 v[184:187], a[136:139], a[188:191], v[184:187]\n"
    "v_mfma_f32_16x16x32_bf16 v[164:167], a[140:143], a[176:179], v[164:167]\n"
    "ds_read_b64_tr_b16 a[136:137], v3 offset:8704\n"
    "v_mfma_f32_16x16x32_bf16 v[172:175], a[140:143], a[184:187], v[172:175]\n"
    "ds_read_b64_tr_b16 a[138:139], v3 offset:12800\n"
    "buffer_load_dwordx4 v27, s[64:67], s88 offen offset:2048 lds\n"
    "v_mfma_f32_16x16x32_bf16 v[180:183], a[140:143], a[180:183], v[180:183]\n"
    "ds_read_b128 a[164:167], v2 offset:47104\n"
    "v_mfma_f32_16x16x32_bf16 v[188:191], a[140:143], a[188:191], v[188:191]\n"
    "v_mfma_f32_16x16x32_bf16 v[192:195], a[144:147], a[176:179], v[192:195]\n"
    "ds_read_b64_tr_b16 a[140:141], v4 offset:8704\n"
    "buffer_load_dwordx4 v28, s[64:67], s88 offen offset:3072 lds\n"
    "v_mfma_f32_16x16x32_bf16 v[200:203], a[144:147], a[184:187], v[200:203]\n"
    "ds_read_b64_tr_b16 a[142:143], v4 offset:12800\n"
    "s_add_u32 s88, s88, 0x4000\n"
    "v_mfma_f32_16x16x32_bf16 v[208:211], a[144:147], a[180:183], v[208:211]\n"
    "ds_read_b128 a[172:175], v2 offset:48128\n"
    "v_mfma_f32_16x16x32_bf16 v[216:219], a[144:147], a[188:191], v[216:219]\n"
    "s_add_u32 m0, s91, 0x14000\n"
    "v_mfma_f32_16x16x32_bf16 v[196:199], a[148:151], a[176:179], v[196:199]\n"
    "ds_read_b64_tr_b16 a[144:145], v3 offset:9216\n"
    "buffer_load_dwordx4 v25, s[64:67], s88 offen lds\n"
    "v_mfma_f32_16x16x32_bf16 v[204:207], a[148:151], a[184:187], v[204:207]\n"
    "ds_read_b64_tr_b16 a[146:147], v3 offset:13312\n"
    "v_mfma_f32_16x16x32_bf16 v[212:215], a[148:151], a[180:183], v[212:215]\n"
    "v_mfma_f32_16x16x32_bf16 v[220:223], a[148:151], a[188:191], v[220:223]\n"
    "buffer_load_dwordx4 v26, s[64:67], s88 offen offset:1024 lds\n"
    "v_mfma_f32_16x16x32_bf16 v[224:227], a[152:155], a[176:179], v[224:227]\n"
    "ds_read_b64_tr_b16 a[148:149], v4 offset:9216\n"
    "v_mfma_f32_16x16x32_bf16 v[232:235], a[152:155], a[184:187], v[232:235]\n"
    "s_waitcnt lgkmcnt(3)\n"
    "ds_read_b64_tr_b16 a[150:151], v4 offset:13312\n"
    "v_mfma_f32_16x16x32_bf16 v[240:243], a[152:155], a[180:183], v[240:243]\n"
    "buffer_load_dwordx4 v27, s[64:67], s88 offen offset:2048 lds\n"
    "v_mfma_f32_16x16x32_bf16 v[248:251], a[152:155], a[188:191], v[248:251]\n"
    "v_mfma_f32_16x16x32_bf16 v[228:231], a[156:159], a[176:179], v[228:231]\n"
    "ds_read_b64_tr_b16 a[152:153], v3 offset:9728\n"
    "v_mfma_f32_16x16x32_bf16 v[236:239], a[156:159], a[184:187], v[236:239]\n"
    "ds_read_b64_tr_b16 a[154:155], v3 offset:13824\n"
    "buffer_load_dwordx4 v28, s[64:67], s88 offen offset:3072 lds\n"
    "v_mfma_f32_16x16x32_bf16 v[244:247], a[156:159], a[180:183], v[244:247]\n"
    "s_add_u32 s88, s88, 0x4000\n"
    "v_mfma_f32_16x16x32_bf16 v[252:255], a[156:159], a[188:191], v[252:255]\n"
    "v_mfma_f32_16x16x32_bf16 v[128:131], a[128:131], a[160:163], v[128:131]\n"
    "ds_read_b64_tr_b16 a[156:157], v4 offset:9728\n"
    "v_mfma_f32_16x16x32_bf16 v[136:139], a[128:131], a[168:171], v[136:139]\n"
    "ds_read_b64_tr_b16 a[158:159], v4 offset:13824\n"
    "v_mfma_f32_16x16x32_bf16 v[144:147], a[128:131], a[164:167], v[144:147]\n"
    "v_mfma_f32_16x16x32_bf16 v[152:155], a[128:131], a[172:175], v[152:155]\n"
    "v_mfma_f32_16x16x32_bf16 v[132:135], a[132:135], a[160:163], v[132:135]\n"
    "s_waitcnt vmcnt(12)\n"
    "ds_read_b64_tr_b16 a[128:129], v3\n"
    "v_mfma_f32_16x16x32_bf16 v[140:143], a[132:135], a[168:171], v[140:143]\n"
    "s_waitcnt vmcnt(8)\n"
    "ds_read_b64_tr_b16 a[130:131], v3 offset:4096\n"
    "v_mfma_f32_16x16x32_bf16 v[148:151], a[132:135], a[164:167], v[148:151]\n"
    "ds_read_b128 a[176:179], v2 offset:16384\n"
    "s_add_u32 m0, s89, 0x2000\n"
    "v_mfma_f32_16x16x32_bf16 v[156:159], a[132:135], a[172:175], v[156:159]\n"
    "s_waitcnt lgkmcnt(4)\n"
    "buffer_load_dwordx4 v25, s[64:67], s88 offen lds\n"
    "v_mfma_f32_16x16x32_bf16 v[160:163], a[136:139], a[160:163], v[160:163]\n"
    "ds_read_b64_tr_b16 a[132:133], v4\n"
    "v_mfma_f32_16x16x32_bf16 v[168:171], a[136:139], a[168:171], v[168:171]\n"
    "ds_read_b64_tr_b16 a[134:135], v4 offset:4096\n"
    "v_mfma_f32_16x16x32_bf16 v[176:179], a[136:139], a[164:167], v[176:179]\n"
    "ds_read_b128 a[184:187], v2 offset:17408\n"
    "buffer_load_dwordx4 v26, s[64:67], s88 offen offset:1024 lds\n"
    "v_mfma_f32_16x16x32_bf16 v[184:187], a[136:139], a[172:175], v[184:187]\n"
    "v_mfma_f32_16x16x32_bf16 v[164:167], a[140:143], a[160:163], v[164:167]\n"
    "ds_read_b64_tr_b16 a[136:137], v3 offset:512\n"
    "v_mfma_f32_16x16x32_bf16 v[172:175], a[140:143], a[168:171], v[172:175]\n"
    "ds_read_b64_tr_b16 a[138:139], v3 offset:4608\n"
    "buffer_load_dwordx4 v27, s[64:67], s88 offen offset:2048 lds\n"
    "v_mfma_f32_16x16x32_bf16 v[180:183], a[140:143], a[164:167], v[180:183]\n"
    "ds_read_b128 a[180:183], v2 offset:49152\n"
    "v_mfma_f32_16x16x32_bf16 v[188:191], a[140:143], a[172:175], v[188:191]\n"
    "v_mfma_f32_16x16x32_bf16 v[192:195], a[144:147], a[160:163], v[192:195]\n"
    "ds_read_b64_tr_b16 a[140:141], v4 offset:512\n"
    "buffer_load_dwordx4 v28, s[64:67], s88 offen offset:3072 lds\n"
    "v_mfma_f32_16x16x32_bf16 v[200:203], a[144:147], a[168:171], v[200:203]\n"
    "ds_read_b64_tr_b16 a[142:143], v4 offset:4608\n"
    "s_add_u32 s88, s88, 0x4000\n"
    "v_mfma_f32_16x16x32_bf16 v[208:211], a[144:147], a[164:167], v[208:211]\n"
    "ds_read_b128 a[188:191], v2 offset:50176\n"
    "v_mfma_f32_16x16x32_bf16 v[216:219], a[144:147], a[172:175], v[216:219]\n"
    "s_add_u32 m0, s89, 0x3000\n"
    "v_mfma_f32_16x16x32_bf16 v[196:199], a[148:151], a[160:163], v[196:199]\n"
    "ds_read_b64_tr_b16 a[144:145], v3 offset:1024\n"
    "s_waitcnt lgkmcnt(4)\n"
    "buffer_load_dwordx4 v25, s[64:67], s88 offen lds\n"
    "v_mfma_f32_16x16x32_bf16 v[204:207], a[148:151], a[168:171], v[204:207]\n"
    "ds_read_b64_tr_b16 a[146:147], v3 offset:5120\n"
    "v_mfma_f32_16x16x32_bf16 v[212:215], a[148:151], a[164:167], v[212:215]\n"
    "v_mfma_f32_16x16x32_bf16 v[220:223], a[148:151], a[172:175], v[220:223]\n"
    "buffer_load_dwordx4 v26, s[64:67], s88 offen offset:1024 lds\n"
    "v_mfma_f32_16x16x32_bf16 v[224:227], a[152:155], a[160:163], v[224:227]\n"
    "ds_read_b64_tr_b16 a[148:149], v4 offset:1024\n"
    "v_mfma_f32_16x16x32_bf16 v[232:235], a[152:155], a[168:171], v[232:235]\n"
    "ds_read_b64_tr_b16 a[150:151], v4 offset:5120\n"
    "v_mfma_f32_16x16x32_bf16 v[240:243], a[152:155], a[164:167], v[240:243]\n"
    "buffer_load_dwordx4 v27, s[64:67], s88 offen offset:2048 lds\n"
    "v_mfma_f32_16x16x32_bf16 v[248:251], a[152:155], a[172:175], v[248:251]\n"
    "v_mfma_f32_16x16x32_bf16 v[228:231], a[156:159], a[160:163], v[228:231]\n"
    "ds_read_b64_tr_b16 a[152:153], v3 offset:1536\n"
    "v_mfma_f32_16x16x32_bf16 v[236:239], a[156:159], a[168:171], v[236:239]\n"
    "ds_read_b64_tr_b16 a[154:155], v3 offset:5632\n"
    "buffer_load_dwordx4 v28, s[64:67], s88 offen offset:3072 lds\n"
    "v_mfma_f32_16x16x32_bf16 v[244:247], a[156:159], a[164:167], v[244:247]\n"
    "s_add_u32 s88, s88, 0x4000\n"
    "v_mfma_f32_16x16x32_bf16 v[252:255], a[156:159], a[172:175], v[252:255]\n"
    "v_mfma_f32_16x16x32_bf16 v[128:131], a[128:131], a[176:179], v[128:131]\n"
    "ds_read_b64_tr_b16 a[156:157], v4 offset:1536\n"
    "v_mfma_f32_16x16x32_bf16 v[136:139], a[128:131], a[184:187], v[136:139]\n"
    "ds_read_b64_tr_b16 a[158:159], v4 offset:5632\n"
    "v_and_b32 v127, 0xffff0000, v119\n"
    "v_mfma_f32_16x16x32_bf16 v[144:147], a[128:131], a[180:183], v[144:147]\n"
    "s_waitcnt lgkmcnt(4)\n"
    "v_lshlrev_b32 v126, 16, v119\n"
    "v_mfma_f32_16x16x32_bf16 v[152:155], a[128:131], a[188:191], v[152:155]\n"
    "v_mfma_f32_16x16x32_bf16 v[132:135], a[132:135], a[176:179], v[132:135]\n"
    "s_waitcnt vmcnt(12)\n"
    "ds_read_b64_tr_b16 a[128:129], v5\n"
    "v_and_b32 v125, 0xffff0000, v118\n"
    "v_mfma_f32_16x16x32_bf16 v[140:143], a[132:135], a[184:187], v[140:143]\n"
    "s_waitcnt vmcnt(8)\n"
    "ds_read_b64_tr_b16 a[130:131], v5 offset:16384\n"
    "v_mfma_f32_16x16x32_bf16 v[148:151], a[132:135], a[180:183], v[148:151]\n"
    "ds_read_b128 a[160:163], v2 offset:18432\n"
    "s_add_u32 m0, s89, 0\n"
    "v_mfma_f32_16x16x32_bf16 v[156:159], a[132:135], a[188:191], v[156:159]\n"
    "s_waitcnt lgkmcnt(3)\n"
    "buffer_load_dwordx4 v25, s[64:67], s88 offen lds\n"
    "v_lshlrev_b32 v124, 16, v118\n"
    "v_mfma_f32_16x16x32_bf16 v[160:163], a[136:139], a[176:179], v[160:163]\n"
    "ds_read_b64_tr_b16 a[132:133], v6\n"
    "v_mfma_f32_16x16x32_bf16 v[168:171], a[136:139], a[184:187], v[168:171]\n"
    "ds_read_b64_tr_b16 a[134:135], v6 offset:16384\n"
    "v_and_b32 v123, 0xffff0000, v117\n"
    "v_mfma_f32_16x16x32_bf16 v[176:179], a[136:139], a[180:183], v[176:179]\n"
    "ds_read_b128 a[168:171], v2 offset:19456\n"
    "buffer_load_dwordx4 v26, s[64:67], s88 offen offset:1024 lds\n"
    "v_mfma_f32_16x16x32_bf16 v[184:187], a[136:139], a[188:191], v[184:187]\n"
    "v_mfma_f32_16x16x32_bf16 v[164:167], a[140:143], a[176:179], v[164:167]\n"
    "ds_read_b64_tr_b16 a[136:137], v5 offset:512\n"
    "v_lshlrev_b32 v122, 16, v117\n"
    "v_mfma_f32_16x16x32_bf16 v[172:175], a[140:143], a[184:187], v[172:175]\n"
    "ds_read_b64_tr_b16 a[138:139], v5 offset:16896\n"
    "buffer_load_dwordx4 v27, s[64:67], s88 offen offset:2048 lds\n"
    "v_mfma_f32_16x16x32_bf16 v[180:183], a[140:143], a[180:183], v[180:183]\n"
    "ds_read_b128 a[164:167], v2 offset:51200\n"
    "v_mfma_f32_16x16x32_bf16 v[188:191], a[140:143], a[188:191], v[188:191]\n"
    "v_and_b32 v121, 0xffff0000, v116\n"
    "v_mfma_f32_16x16x32_bf16 v[192:195], a[144:147], a[176:179], v[192:195]\n"
    "ds_read_b64_tr_b16 a[140:141], v6 offset:512\n"
    "buffer_load_dwordx4 v28, s[64:67], s88 offen offset:3072 lds\n"
    "v_mfma_f32_16x16x32_bf16 v[200:203], a[144:147], a[184:187], v[200:203]\n"
    "ds_read_b64_tr_b16 a[142:143], v6 offset:16896\n"
    "s_add_u32 s88, s88, 0x4000\n"
    "v_mfma_f32_16x16x32_bf16 v[208:211], a[144:147], a[180:183], v[208:211]\n"
    "ds_read_b128 a[172:175], v2 offset:52224\n"
    "v_mfma_f32_16x16x32_bf16 v[216:219], a[144:147], a[188:191], v[216:219]\n"
    "s_add_u32 m0, s89, 0x1000\n"
    "v_lshlrev_b32 v120, 16, v116\n"
    "v_mfma_f32_16x16x32_bf16 v[196:199], a[148:151], a[176:179], v[196:199]\n"
    "ds_read_b64_tr_b16 a[144:145], v5 offset:1024\n"
    "buffer_load_dwordx4 v25, s[64:67], s88 offen lds\n"
    "v_mfma_f32_16x16x32_bf16 v[204:207], a[148:151], a[184:187], v[204:207]\n"
    "ds_read_b64_tr_b16 a[146:147], v5 offset:17408\n"
    "v_and_b32 v119, 0xffff0000, v115\n"
    "v_mfma_f32_16x16x32_bf16 v[212:215], a[148:151], a[180:183], v[212:215]\n"
    "v_mfma_f32_16x16x32_bf16 v[220:223], a[148:151], a[188:191], v[220:223]\n"
    "buffer_load_dwordx4 v26, s[64:67], s88 offen offset:1024 lds\n"
    "v_lshlrev_b32 v118, 16, v115\n"
    "v_mfma_f32_16x16x32_bf16 v[224:227], a[152:155], a[176:179], v[224:227]\n"
    "ds_read_b64_tr_b16 a[148:149], v6 offset:1024\n"
    "v_mfma_f32_16x16x32_bf16 v[232:235], a[152:155], a[184:187], v[232:235]\n"
    "s_waitcnt lgkmcnt(3)\n"
    "ds_read_b64_tr_b16 a[150:151], v6 offset:17408\n"
    "v_and_b32 v117, 0xffff0000, v114\n"
    "v_mfma_f32_16x16x32_bf16 v[240:243], a[152:155], a[180:183], v[240:243]\n"
    "buffer_load_dwordx4 v27, s[64:67], s88 offen offset:2048 lds\n"
    "v_mfma_f32_16x16x32_bf16 v[248:251], a[152:155], a[188:191], v[248:251]\n"
    "v_lshlrev_b32 v116, 16, v114\n"
    "v_mfma_f32_16x16x32_bf16 v[228:231], a[156:159], a[176:179], v[228:231]\n"
    "ds_read_b64_tr_b16 a[152:153], v5 offset:1536\n"
    "v_mfma_f32_16x16x32_bf16 v[236:239], a[156:159], a[184:187], v[236:239]\n"
    "ds_read_b64_tr_b16 a[154:155], v5 offset:17920\n"
    "buffer_load_dwordx4 v28, s[64:67], s88 offen offset:3072 lds\n"
    "v_mfma_f32_16x16x32_bf16 v[244:247], a[156:159], a[180:183], v[244:247]\n"
    "s_add_u32 s88, s88, 0x4000\n"
    "v_and_b32 v115, 0xffff0000, v113\n"
    "v_mfma_f32_16x16x32_bf16 v[252:255], a[156:159], a[188:191], v[252:255]\n"
    "v_lshlrev_b32 v114, 16, v113\n"
    "v_mfma_f32_16x16x32_bf16 v[128:131], a[128:131], a[160:163], v[128:131]\n"
    "ds_read_b64_tr_b16 a[156:157], v6 offset:1536\n"
    "v_mfma_f32_16x16x32_bf16 v[136:139], a[128:131], a[168:171], v[136:139]\n"
    "ds_read_b64_tr_b16 a[158:159], v6 offset:17920\n"
    "v_and_b32 v113, 0xffff0000, v112\n"
    "v_mfma_f32_16x16x32_bf16 v[144:147], a[128:131], a[164:167], v[144:147]\n"
    "v_lshlrev_b32 v112, 16, v112\n"
    "v_mfma_f32_16x16x32_bf16 v[152:155], a[128:131], a[172:175], v[152:155]\n"
    "v_mfma_f32_16x16x32_bf16 v[132:135], a[132:135], a[160:163], v[132:135]\n"
    "s_waitcnt vmcnt(12)\n"
    "ds_read_b64_tr_b16 a[128:129], v3 offset:8192\n"
    "s_lshl_b32 s95, s34, 3\n"
    "v_mfma_f32_16x16x32_bf16 v[140:143], a[132:135], a[168:171], v[140:143]\n"
    "s_waitcnt vmcnt(8)\n"
    "ds_read_b64_tr_b16 a[130:131], v3 offset:12288\n"
    "v_mfma_f32_16x16x32_bf16 v[148:151], a[132:135], a[164:167], v[148:151]\n"
    "ds_read_b128 a[176:179], v2 offset:20480\n"
    "s_add_u32 m0, s91, 0x10000\n"
    "v_mfma_f32_16x16x32_bf16 v[156:159], a[132:135], a[172:175], v[156:159]\n"
    "s_waitcnt lgkmcnt(3)\n"
    "buffer_load_dwordx4 v25, s[64:67], s88 offen lds\n"
    "s_add_u32 s95, s95, s33\n"
    "v_mfma_f32_16x16x32_bf16 v[160:163], a[136:139], a[160:163], v[160:163]\n"
    "ds_read_b64_tr_b16 a[132:133], v4 offset:8192\n"
    "v_mfma_f32_16x16x32_bf16 v[168:171], a[136:139], a[168:171], v[168:171]\n"
    "ds_read_b64_tr_b16 a[134:135], v4 offset:12288\n"
    "s_lshl_b32 s95, s95, 16\n"
    "v_mfma_f32_16x16x32_bf16 v[176:179], a[136:139], a[164:167], v[176:179]\n"
    "ds_read_b128 a[184:187], v2 offset:21504\n"
    "buffer_load_dwordx4 v26, s[64:67], s88 offen offset:1024 lds\n"
    "v_mfma_f32_16x16x32_bf16 v[184:187], a[136:139], a[172:175], v[184:187]\n"
    "v_mfma_f32_16x16x32_bf16 v[164:167], a[140:143], a[160:163], v[164:167]\n"
    "ds_read_b64_tr_b16 a[136:137], v3 offset:8704\n"
    "s_lshl_b32 s39, s32, 14\n"
    "v_mfma_f32_16x16x32_bf16 v[172:175], a[140:143], a[168:171], v[172:175]\n"
    "ds_read_b64_tr_b16 a[138:139], v3 offset:12800\n"
    "buffer_load_dwordx4 v27, s[64:67], s88 offen offset:2048 lds\n"
    "v_mfma_f32_16x16x32_bf16 v[180:183], a[140:143], a[164:167], v[180:183]\n"
    "ds_read_b128 a[180:183], v2 offset:53248\n"
    "v_mfma_f32_16x16x32_bf16 v[188:191], a[140:143], a[172:175], v[188:191]\n"
    "s_add_u32 s95, s95, s39\n"
    "v_mfma_f32_16x16x32_bf16 v[192:195], a[144:147], a[160:163], v[192:195]\n"
    "ds_read_b64_tr_b16 a[140:141], v4 offset:8704\n"
    "buffer_load_dwordx4 v28, s[64:67], s88 offen offset:3072 lds\n"
    "v_mfma_f32_16x16x32_bf16 v[200:203], a[144:147], a[168:171], v[200:203]\n"
    "ds_read_b64_tr_b16 a[142:143], v4 offset:12800\n"
    "s_add_u32 s88, s88, 0x4000\n"
    "v_mfma_f32_16x16x32_bf16 v[208:211], a[144:147], a[164:167], v[208:211]\n"
    "ds_read_b128 a[188:191], v2 offset:54272\n"
    "v_mfma_f32_16x16x32_bf16 v[216:219], a[144:147], a[172:175], v[216:219]\n"
    "s_add_u32 m0, s91, 0x14000\n"
    "v_and_b32 v56, 15, v1\n"
    "v_mfma_f32_16x16x32_bf16 v[196:199], a[148:151], a[160:163], v[196:199]\n"
    "ds_read_b64_tr_b16 a[144:145], v3 offset:9216\n"
    "buffer_load_dwordx4 v25, s[64:67], s88 offen lds\n"
    "v_mfma_f32_16x16x32_bf16 v[204:207], a[148:151], a[168:171], v[204:207]\n"
    "ds_read_b64_tr_b16 a[146:147], v3 offset:13312\n"
    "v_lshlrev_b32 v56, 10, v56\n"
    "v_mfma_f32_16x16x32_bf16 v[212:215], a[148:151], a[164:167], v[212:215]\n"
    "v_mfma_f32_16x16x32_bf16 v[220:223], a[148:151], a[172:175], v[220:223]\n"
    "buffer_load_dwordx4 v26, s[64:67], s88 offen offset:1024 lds\n"
    "v_lshrrev_b32 v55, 4, v1\n"
    "v_mfma_f32_16x16x32_bf16 v[224:227], a[152:155], a[160:163], v[224:227]\n"
    "ds_read_b64_tr_b16 a[148:149], v4 offset:9216\n"
    "v_mfma_f32_16x16x32_bf16 v[232:235], a[152:155], a[168:171], v[232:235]\n"
    "s_waitcnt lgkmcnt(3)\n"
    "ds_read_b64_tr_b16 a[150:151], v4 offset:13312\n"
    "v_lshl_add_u32 v56, v55, 3, v56\n"
    "v_mfma_f32_16x16x32_bf16 v[240:243], a[152:155], a[164:167], v[240:243]\n"
    "buffer_load_dwordx4 v27, s[64:67], s88 offen offset:2048 lds\n"
    "v_mfma_f32_16x16x32_bf16 v[248:251], a[152:155], a[172:175], v[248:251]\n"
    "s_lshl_b32 s93, s35, 16\n"
    "v_mfma_f32_16x16x32_bf16 v[228:231], a[156:159], a[160:163], v[228:231]\n"
    "ds_read_b64_tr_b16 a[152:153], v3 offset:9728\n"
    "v_mfma_f32_16x16x32_bf16 v[236:239], a[156:159], a[168:171], v[236:239]\n"
    "ds_read_b64_tr_b16 a[154:155], v3 offset:13824\n"
    "buffer_load_dwordx4 v28, s[64:67], s88 offen offset:3072 lds\n"
    "v_mfma_f32_16x16x32_bf16 v[244:247], a[156:159], a[164:167], v[244:247]\n"
    "s_add_u32 s88, s88, 0x4000\n"
    "s_and_b32 s39, s32, 1\n"
    "v_mfma_f32_16x16x32_bf16 v[252:255], a[156:159], a[172:175], v[252:255]\n"
    "s_lshl_b32 s39, s39, 15\n"
    "v_mfma_f32_16x16x32_bf16 v[128:131], a[128:131], a[176:179], v[128:131]\n"
    "ds_read_b64_tr_b16 a[156:157], v4 offset:9728\n"
    "v_mfma_f32_16x16x32_bf16 v[136:139], a[128:131], a[184:187], v[136:139]\n"
    "ds_read_b64_tr_b16 a[158:159], v4 offset:13824\n"
    "s_add_u32 s93, s93, s39\n"
    "v_mfma_f32_16x16x32_bf16 v[144:147], a[128:131], a[180:183], v[144:147]\n"
    "s_lshl_b32 s39, s36, 7\n"
    "v_mfma_f32_16x16x32_bf16 v[152:155], a[128:131], a[188:191], v[152:155]\n"
    "v_mfma_f32_16x16x32_bf16 v[132:135], a[132:135], a[176:179], v[132:135]\n"
    "s_waitcnt vmcnt(12)\n"
    "ds_read_b64_tr_b16 a[128:129], v3\n"
    "s_add_u32 s93, s93, s39\n"
    "v_mfma_f32_16x16x32_bf16 v[140:143], a[132:135], a[184:187], v[140:143]\n"
    "s_waitcnt vmcnt(8)\n"
    "ds_read_b64_tr_b16 a[130:131], v3 offset:4096\n"
    "v_mfma_f32_16x16x32_bf16 v[148:151], a[132:135], a[180:183], v[148:151]\n"
    "ds_read_b128 a[160:163], v2 offset:22528\n"
    "s_add_u32 m0, s89, 0x2000\n"
    "v_mfma_f32_16x16x32_bf16 v[156:159], a[132:135], a[188:191], v[156:159]\n"
    "s_waitcnt lgkmcnt(3)\n"
    "buffer_load_dwordx4 v25, s[64:67], s88 offen lds\n"
    "s_lshr_b32 s39, s32, 1\n"
    "v_mfma_f32_16x16x32_bf16 v[160:163], a[136:139], a[176:179], v[160:163]\n"
    "ds_read_b64_tr_b16 a[132:133], v4\n"
    "v_mfma_f32_16x16x32_bf16 v[168:171], a[136:139], a[184:187], v[168:171]\n"
    "ds_read_b64_tr_b16 a[134:135], v4 offset:4096\n"
    "s_lshl_b32 s39, s39, 6\n"
    "v_mfma_f32_16x16x32_bf16 v[176:179], a[136:139], a[180:183], v[176:179]\n"
    "ds_read_b128 a[168:171], v2 offset:23552\n"
    "buffer_load_dwordx4 v26, s[64:67], s88 offen offset:1024 lds\n"
    "v_mfma_f32_16x16x32_bf16 v[184:187], a[136:139], a[188:191], v[184:187]\n"
    "v_mfma_f32_16x16x32_bf16 v[164:167], a[140:143], a[176:179], v[164:167]\n"
    "ds_read_b64_tr_b16 a[136:137], v3 offset:512\n"
    "s_add_u32 s93, s93, s39\n"
    "v_mfma_f32_16x16x32_bf16 v[172:175], a[140:143], a[184:187], v[172:175]\n"
    "ds_read_b64_tr_b16 a[138:139], v3 offset:4608\n"
    "buffer_load_dwordx4 v27, s[64:67], s88 offen offset:2048 lds\n"
    "v_mfma_f32_16x16x32_bf16 v[180:183], a[140:143], a[180:183], v[180:183]\n"
    "ds_read_b128 a[164:167], v2 offset:55296\n"
    "v_mfma_f32_16x16x32_bf16 v[188:191], a[140:143], a[188:191], v[188:191]\n"
    "s_add_u32 s39, s93, 0x4000\n"
    "v_mfma_f32_16x16x32_bf16 v[192:195], a[144:147], a[176:179], v[192:195]\n"
    "ds_read_b64_tr_b16 a[140:141], v4 offset:512\n"
    "buffer_load_dwordx4 v28, s[64:67], s88 offen offset:3072 lds\n"
    "v_mfma_f32_16x16x32_bf16 v[200:203], a[144:147], a[184:187], v[200:203]\n"
    "ds_read_b64_tr_b16 a[142:143], v4 offset:4608\n"
    "s_add_u32 s88, s88, 0x4000\n"
    "v_mfma_f32_16x16x32_bf16 v[208:211], a[144:147], a[180:183], v[208:211]\n"
    "ds_read_b128 a[172:175], v2 offset:56320\n"
    "v_mfma_f32_16x16x32_bf16 v[216:219], a[144:147], a[188:191], v[216:219]\n"
    "s_add_u32 m0, s89, 0x3000\n"
    "v_mfma_f32_16x16x32_bf16 v[196:199], a[148:151], a[176:179], v[196:199]\n"
    "ds_read_b64_tr_b16 a[144:145], v3 offset:1024\n"
    "buffer_load_dwordx4 v25, s[64:67], s88 offen lds\n"
    "v_mfma_f32_16x16x32_bf16 v[204:207], a[148:151], a[184:187], v[204:207]\n"
    "ds_read_b64_tr_b16 a[146:147], v3 offset:5120\n"
    "v_mfma_f32_16x16x32_bf16 v[212:215], a[148:151], a[180:183], v[212:215]\n"
    "v_mfma_f32_16x16x32_bf16 v[220:223], a[148:151], a[188:191], v[220:223]\n"
    "buffer_load_dwordx4 v26, s[64:67], s88 offen offset:1024 lds\n"
    "v_mfma_f32_16x16x32_bf16 v[224:227], a[152:155], a[176:179], v[224:227]\n"
    "ds_read_b64_tr_b16 a[148:149], v4 offset:1024\n"
    "v_mfma_f32_16x16x32_bf16 v[232:235], a[152:155], a[184:187], v[232:235]\n"
    "s_waitcnt lgkmcnt(3)\n"
    "ds_read_b64_tr_b16 a[150:151], v4 offset:5120\n"
    "v_mfma_f32_16x16x32_bf16 v[240:243], a[152:155], a[180:183], v[240:243]\n"
    "buffer_load_dwordx4 v27, s[64:67], s88 offen offset:2048 lds\n"
    "v_mfma_f32_16x16x32_bf16 v[248:251], a[152:155], a[188:191], v[248:251]\n"
    "v_mfma_f32_16x16x32_bf16 v[228:231], a[156:159], a[176:179], v[228:231]\n"
    "ds_read_b64_tr_b16 a[152:153], v3 offset:1536\n"
    "v_mfma_f32_16x16x32_bf16 v[236:239], a[156:159], a[184:187], v[236:239]\n"
    "ds_read_b64_tr_b16 a[154:155], v3 offset:5632\n"
    "buffer_load_dwordx4 v28, s[64:67], s88 offen offset:3072 lds\n"
    "v_mfma_f32_16x16x32_bf16 v[244:247], a[156:159], a[180:183], v[244:247]\n"
    "s_add_u32 s88, s88, 0x4000\n"
    "v_mfma_f32_16x16x32_bf16 v[252:255], a[156:159], a[188:191], v[252:255]\n"
    "v_mfma_f32_16x16x32_bf16 v[128:131], a[128:131], a[160:163], v[128:131]\n"
    "ds_read_b64_tr_b16 a[156:157], v4 offset:1536\n"
    "v_mfma_f32_16x16x32_bf16 v[136:139], a[128:131], a[168:171], v[136:139]\n"
    "ds_read_b64_tr_b16 a[158:159], v4 offset:5632\n"
    "v_mfma_f32_16x16x32_bf16 v[144:147], a[128:131], a[164:167], v[144:147]\n"
    "v_mfma_f32_16x16x32_bf16 v[152:155], a[128:131], a[172:175], v[152:155]\n"
    "v_mfma_f32_16x16x32_bf16 v[132:135], a[132:135], a[160:163], v[132:135]\n"
    "s_waitcnt vmcnt(12)\n"
    "ds_read_b64_tr_b16 a[128:129], v5\n"
    "v_mfma_f32_16x16x32_bf16 v[140:143], a[132:135], a[168:171], v[140:143]\n"
    "s_waitcnt vmcnt(8)\n"
    "ds_read_b64_tr_b16 a[130:131], v5 offset:16384\n"
    "v_mfma_f32_16x16x32_bf16 v[148:151], a[132:135], a[164:167], v[148:151]\n"
    "ds_read_b128 a[176:179], v2 offset:24576\n"
    "s_add_u32 m0, s89, 0\n"
    "v_mfma_f32_16x16x32_bf16 v[156:159], a[132:135], a[172:175], v[156:159]\n"
    "s_waitcnt lgkmcnt(4)\n"
    "buffer_load_dwordx4 v25, s[64:67], s88 offen lds\n"
    "v_mfma_f32_16x16x32_bf16 v[160:163], a[136:139], a[160:163], v[160:163]\n"
    "ds_read_b64_tr_b16 a[132:133], v6\n"
    "v_mfma_f32_16x16x32_bf16 v[168:171], a[136:139], a[168:171], v[168:171]\n"
    "ds_read_b64_tr_b16 a[134:135], v6 offset:16384\n"
    "v_mfma_f32_16x16x32_bf16 v[176:179], a[136:139], a[164:167], v[176:179]\n"
    "ds_read_b128 a[184:187], v2 offset:25600\n"
    "buffer_load_dwordx4 v26, s[64:67], s88 offen offset:1024 lds\n"
    "v_mfma_f32_16x16x32_bf16 v[184:187], a[136:139], a[172:175], v[184:187]\n"
    "v_mfma_f32_16x16x32_bf16 v[164:167], a[140:143], a[160:163], v[164:167]\n"
    "ds_read_b64_tr_b16 a[136:137], v5 offset:512\n"
    "v_mfma_f32_16x16x32_bf16 v[172:175], a[140:143], a[168:171], v[172:175]\n"
    "ds_read_b64_tr_b16 a[138:139], v5 offset:16896\n"
    "buffer_load_dwordx4 v27, s[64:67], s88 offen offset:2048 lds\n"
    "v_mfma_f32_16x16x32_bf16 v[180:183], a[140:143], a[164:167], v[180:183]\n"
    "ds_read_b128 a[180:183], v2 offset:57344\n"
    "v_mfma_f32_16x16x32_bf16 v[188:191], a[140:143], a[172:175], v[188:191]\n"
    "v_mfma_f32_16x16x32_bf16 v[192:195], a[144:147], a[160:163], v[192:195]\n"
    "ds_read_b64_tr_b16 a[140:141], v6 offset:512\n"
    "buffer_load_dwordx4 v28, s[64:67], s88 offen offset:3072 lds\n"
    "v_mfma_f32_16x16x32_bf16 v[200:203], a[144:147], a[168:171], v[200:203]\n"
    "ds_read_b64_tr_b16 a[142:143], v6 offset:16896\n"
    "s_add_u32 s88, s88, 0x4000\n"
    "v_mfma_f32_16x16x32_bf16 v[208:211], a[144:147], a[164:167], v[208:211]\n"
    "ds_read_b128 a[188:191], v2 offset:58368\n"
    "v_mfma_f32_16x16x32_bf16 v[216:219], a[144:147], a[172:175], v[216:219]\n"
    "s_add_u32 m0, s89, 0x1000\n"
    "v_mfma_f32_16x16x32_bf16 v[196:199], a[148:151], a[160:163], v[196:199]\n"
    "ds_read_b64_tr_b16 a[144:145], v5 offset:1024\n"
    "s_waitcnt lgkmcnt(4)\n"
    "buffer_load_dwordx4 v25, s[64:67], s88 offen lds\n"
    "v_mfma_f32_16x16x32_bf16 v[204:207], a[148:151], a[168:171], v[204:207]\n"
    "ds_read_b64_tr_b16 a[146:147], v5 offset:17408\n"
    "v_mfma_f32_16x16x32_bf16 v[212:215], a[148:151], a[164:167], v[212:215]\n"
    "v_mfma_f32_16x16x32_bf16 v[220:223], a[148:151], a[172:175], v[220:223]\n"
    "buffer_load_dwordx4 v26, s[64:67], s88 offen offset:1024 lds\n"
    "v_mfma_f32_16x16x32_bf16 v[224:227], a[152:155], a[160:163], v[224:227]\n"
    "ds_read_b64_tr_b16 a[148:149], v6 offset:1024\n"
    "v_mfma_f32_16x16x32_bf16 v[232:235], a[152:155], a[168:171], v[232:235]\n"
    "ds_read_b64_tr_b16 a[150:151], v6 offset:17408\n"
    "v_mfma_f32_16x16x32_bf16 v[240:243], a[152:155], a[164:167], v[240:243]\n"
    "buffer_load_dwordx4 v27, s[64:67], s88 offen offset:2048 lds\n"
    "v_mfma_f32_16x16x32_bf16 v[248:251], a[152:155], a[172:175], v[248:251]\n"
    "v_mfma_f32_16x16x32_bf16 v[228:231], a[156:159], a[160:163], v[228:231]\n"
    "ds_read_b64_tr_b16 a[152:153], v5 offset:1536\n"
    "v_mfma_f32_16x16x32_bf16 v[236:239], a[156:159], a[168:171], v[236:239]\n"
    "ds_read_b64_tr_b16 a[154:155], v5 offset:17920\n"
    "buffer_load_dwordx4 v28, s[64:67], s88 offen offset:3072 lds\n"
    "v_mfma_f32_16x16x32_bf16 v[244:247], a[156:159], a[164:167], v[244:247]\n"
    "s_add_u32 s88, s88, 0x4000\n"
    "v_mfma_f32_16x16x32_bf16 v[252:255], a[156:159], a[172:175], v[252:255]\n"
    "v_mfma_f32_16x16x32_bf16 v[128:131], a[128:131], a[176:179], v[128:131]\n"
    "ds_read_b64_tr_b16 a[156:157], v6 offset:1536\n"
    "v_mfma_f32_16x16x32_bf16 v[136:139], a[128:131], a[184:187], v[136:139]\n"
    "ds_read_b64_tr_b16 a[158:159], v6 offset:17920\n"
    "v_mfma_f32_16x16x32_bf16 v[144:147], a[128:131], a[180:183], v[144:147]\n"
    "s_waitcnt lgkmcnt(4)\n"
    "v_mfma_f32_16x16x32_bf16 v[152:155], a[128:131], a[188:191], v[152:155]\n"
    "v_mfma_f32_16x16x32_bf16 v[132:135], a[132:135], a[176:179], v[132:135]\n"
    "s_waitcnt vmcnt(12)\n"
    "ds_read_b64_tr_b16 a[128:129], v3 offset:8192\n"
    "v_mfma_f32_16x16x32_bf16 v[140:143], a[132:135], a[184:187], v[140:143]\n"
    "s_waitcnt vmcnt(8)\n"
    "ds_read_b64_tr_b16 a[130:131], v3 offset:12288\n"
    "v_mfma_f32_16x16x32_bf16 v[148:151], a[132:135], a[180:183], v[148:151]\n"
    "ds_read_b128 a[160:163], v2 offset:26624\n"
    "s_add_u32 m0, s91, 0x10000\n"
    "v_mfma_f32_16x16x32_bf16 v[156:159], a[132:135], a[188:191], v[156:159]\n"
    "s_waitcnt lgkmcnt(3)\n"
    "buffer_load_dwordx4 v25, s[64:67], s88 offen lds\n"
    "v_mfma_f32_16x16x32_bf16 v[160:163], a[136:139], a[176:179], v[160:163]\n"
    "ds_read_b64_tr_b16 a[132:133], v4 offset:8192\n"
    "v_mfma_f32_16x16x32_bf16 v[168:171], a[136:139], a[184:187], v[168:171]\n"
    "ds_read_b64_tr_b16 a[134:135], v4 offset:12288\n"
    "v_mfma_f32_16x16x32_bf16 v[176:179], a[136:139], a[180:183], v[176:179]\n"
    "ds_read_b128 a[168:171], v2 offset:27648\n"
    "buffer_load_dwordx4 v26, s[64:67], s88 offen offset:1024 lds\n"
    "v_mfma_f32_16x16x32_bf16 v[184:187], a[136:139], a[188:191], v[184:187]\n"
    "v_mfma_f32_16x16x32_bf16 v[164:167], a[140:143], a[176:179], v[164:167]\n"
    "ds_read_b64_tr_b16 a[136:137], v3 offset:8704\n"
    "v_mfma_f32_16x16x32_bf16 v[172:175], a[140:143], a[184:187], v[172:175]\n"
    "ds_read_b64_tr_b16 a[138:139], v3 offset:12800\n"
    "buffer_load_dwordx4 v27, s[64:67], s88 offen offset:2048 lds\n"
    "v_mfma_f32_16x16x32_bf16 v[180:183], a[140:143], a[180:183], v[180:183]\n"
    "ds_read_b128 a[164:167], v2 offset:59392\n"
    "v_mfma_f32_16x16x32_bf16 v[188:191], a[140:143], a[188:191], v[188:191]\n"
    "v_mfma_f32_16x16x32_bf16 v[192:195], a[144:147], a[176:179], v[192:195]\n"
    "ds_read_b64_tr_b16 a[140:141], v4 offset:8704\n"
    "buffer_load_dwordx4 v28, s[64:67], s88 offen offset:3072 lds\n"
    "v_mfma_f32_16x16x32_bf16 v[200:203], a[144:147], a[184:187], v[200:203]\n"
    "ds_read_b64_tr_b16 a[142:143], v4 offset:12800\n"
    "s_add_u32 s88, s88, 0x4000\n"
    "v_mfma_f32_16x16x32_bf16 v[208:211], a[144:147], a[180:183], v[208:211]\n"
    "ds_read_b128 a[172:175], v2 offset:60416\n"
    "v_mfma_f32_16x16x32_bf16 v[216:219], a[144:147], a[188:191], v[216:219]\n"
    "s_add_u32 m0, s91, 0x14000\n"
    "v_mfma_f32_16x16x32_bf16 v[196:199], a[148:151], a[176:179], v[196:199]\n"
    "ds_read_b64_tr_b16 a[144:145], v3 offset:9216\n"
    "buffer_load_dwordx4 v25, s[64:67], s88 offen lds\n"
    "v_mfma_f32_16x16x32_bf16 v[204:207], a[148:151], a[184:187], v[204:207]\n"
    "ds_read_b64_tr_b16 a[146:147], v3 offset:13312\n"
    "v_mfma_f32_16x16x32_bf16 v[212:215], a[148:151], a[180:183], v[212:215]\n"
    "v_mfma_f32_16x16x32_bf16 v[220:223], a[148:151], a[188:191], v[220:223]\n"
    "buffer_load_dwordx4 v26, s[64:67], s88 offen offset:1024 lds\n"
    "v_mfma_f32_16x16x32_bf16 v[224:227], a[152:155], a[176:179], v[224:227]\n"
    "ds_read_b64_tr_b16 a[148:149], v4 offset:9216\n"
    "v_mfma_f32_16x16x32_bf16 v[232:235], a[152:155], a[184:187], v[232:235]\n"
    "s_waitcnt lgkmcnt(3)\n"
    "ds_read_b64_tr_b16 a[150:151], v4 offset:13312\n"
    "v_mfma_f32_16x16x32_bf16 v[240:243], a[152:155], a[180:183], v[240:243]\n"
    "buffer_load_dwordx4 v27, s[64:67], s88 offen offset:2048 lds\n"
    "v_mfma_f32_16x16x32_bf16 v[248:251], a[152:155], a[188:191], v[248:251]\n"
    "v_mfma_f32_16x16x32_bf16 v[228:231], a[156:159], a[176:179], v[228:231]\n"
    "ds_read_b64_tr_b16 a[152:153], v3 offset:9728\n"
    "v_mfma_f32_16x16x32_bf16 v[236:239], a[156:159], a[184:187], v[236:239]\n"
    "ds_read_b64_tr_b16 a[154:155], v3 offset:13824\n"
    "buffer_load_dwordx4 v28, s[64:67], s88 offen offset:3072 lds\n"
    "v_mfma_f32_16x16x32_bf16 v[244:247], a[156:159], a[180:183], v[244:247]\n"
    "s_add_u32 s88, s88, 0x4000\n"
    "v_mfma_f32_16x16x32_bf16 v[252:255], a[156:159], a[188:191], v[252:255]\n"
    "v_mfma_f32_16x16x32_bf16 v[128:131], a[128:131], a[160:163], v[128:131]\n"
    "ds_read_b64_tr_b16 a[156:157], v4 offset:9728\n"
    "v_mfma_f32_16x16x32_bf16 v[136:139], a[128:131], a[168:171], v[136:139]\n"
    "ds_read_b64_tr_b16 a[158:159], v4 offset:13824\n"
    "v_mfma_f32_16x16x32_bf16 v[144:147], a[128:131], a[164:167], v[144:147]\n"
    "v_mfma_f32_16x16x32_bf16 v[152:155], a[128:131], a[172:175], v[152:155]\n"
    "v_mfma_f32_16x16x32_bf16 v[132:135], a[132:135], a[160:163], v[132:135]\n"
    "v_mfma_f32_16x16x32_bf16 v[140:143], a[132:135], a[168:171], v[140:143]\n"
    "v_mfma_f32_16x16x32_bf16 v[148:151], a[132:135], a[164:167], v[148:151]\n"
    "ds_read_b128 a[176:179], v2 offset:28672\n"
    "v_mfma_f32_16x16x32_bf16 v[156:159], a[132:135], a[172:175], v[156:159]\n"
    "v_mfma_f32_16x16x32_bf16 v[160:163], a[136:139], a[160:163], v[160:163]\n"
    "v_mfma_f32_16x16x32_bf16 v[168:171], a[136:139], a[168:171], v[168:171]\n"
    "v_mfma_f32_16x16x32_bf16 v[176:179], a[136:139], a[164:167], v[176:179]\n"
    "ds_read_b128 a[184:187], v2 offset:29696\n"
    "v_mfma_f32_16x16x32_bf16 v[184:187], a[136:139], a[172:175], v[184:187]\n"
    "v_mfma_f32_16x16x32_bf16 v[164:167], a[140:143], a[160:163], v[164:167]\n"
    "v_mfma_f32_16x16x32_bf16 v[172:175], a[140:143], a[168:171], v[172:175]\n"
    "v_mfma_f32_16x16x32_bf16 v[180:183], a[140:143], a[164:167], v[180:183]\n"
    "ds_read_b128 a[180:183], v2 offset:61440\n"
    "v_mfma_f32_16x16x32_bf16 v[188:191], a[140:143], a[172:175], v[188:191]\n"
    "s_waitcnt lgkmcnt(3)\n"
    "v_mfma_f32_16x16x32_bf16 v[192:195], a[144:147], a[160:163], v[192:195]\n"
    "v_mfma_f32_16x16x32_bf16 v[200:203], a[144:147], a[168:171], v[200:203]\n"
    "s_waitcnt vmcnt(12)\n"
    "ds_read_b64_tr_b16 a[128:129], v3\n"
    "v_mfma_f32_16x16x32_bf16 v[208:211], a[144:147], a[164:167], v[208:211]\n"
    "ds_read_b128 a[188:191], v2 offset:62464\n"
    "s_waitcnt vmcnt(8)\n"
    "ds_read_b64_tr_b16 a[130:131], v3 offset:4096\n"
    "v_mfma_f32_16x16x32_bf16 v[216:219], a[144:147], a[172:175], v[216:219]\n"
    "ds_read_b64_tr_b16 a[132:133], v4\n"
    "v_mfma_f32_16x16x32_bf16 v[196:199], a[148:151], a[160:163], v[196:199]\n"
    "ds_read_b64_tr_b16 a[134:135], v4 offset:4096\n"
    "v_mfma_f32_16x16x32_bf16 v[204:207], a[148:151], a[168:171], v[204:207]\n"
    "s_waitcnt vmcnt(4)\n"
    "ds_read_b64_tr_b16 a[136:137], v5\n"
    "v_mfma_f32_16x16x32_bf16 v[212:215], a[148:151], a[164:167], v[212:215]\n"
    "s_waitcnt vmcnt(0)\n"
    "ds_read_b64_tr_b16 a[138:139], v5 offset:16384\n"
    "v_mfma_f32_16x16x32_bf16 v[220:223], a[148:151], a[172:175], v[220:223]\n"
    "ds_read_b64_tr_b16 a[140:141], v6\n"
    "v_mfma_f32_16x16x32_bf16 v[224:227], a[152:155], a[160:163], v[224:227]\n"
    "ds_read_b64_tr_b16 a[142:143], v6 offset:16384\n"
    "v_mfma_f32_16x16x32_bf16 v[232:235], a[152:155], a[168:171], v[232:235]\n"
    "v_mfma_f32_16x16x32_bf16 v[240:243], a[152:155], a[164:167], v[240:243]\n"
    "v_mfma_f32_16x16x32_bf16 v[248:251], a[152:155], a[172:175], v[248:251]\n"
    "v_mfma_f32_16x16x32_bf16 v[228:231], a[156:159], a[160:163], v[228:231]\n"
    "v_mfma_f32_16x16x32_bf16 v[236:239], a[156:159], a[168:171], v[236:239]\n"
    "v_mfma_f32_16x16x32_bf16 v[244:247], a[156:159], a[164:167], v[244:247]\n"
    "v_mfma_f32_16x16x32_bf16 v[252:255], a[156:159], a[172:175], v[252:255]\n"
    "s_waitcnt lgkmcnt(3)\n"
    "v_mfma_f32_16x16x32_bf16 v[128:131], a[128:131], a[176:179], v[128:131]\n"
    "ds_read_b128 a[160:163], v2 offset:30720\n"
    "v_mfma_f32_16x16x32_bf16 v[136:139], a[128:131], a[184:187], v[136:139]\n"
    "ds_read_b128 a[168:171], v2 offset:31744\n"
    "ds_read_b64_tr_b16 a[144:145], v3 offset:512\n"
    "v_mfma_f32_16x16x32_bf16 v[144:147], a[128:131], a[180:183], v[144:147]\n"
    "ds_read_b128 a[164:167], v2 offset:63488\n"
    "ds_read_b64_tr_b16 a[146:147], v3 offset:4608\n"
    "v_mfma_f32_16x16x32_bf16 v[152:155], a[128:131], a[188:191], v[152:155]\n"
    "ds_read_b128 a[172:175], v2 offset:64512\n"
    "ds_read_b64_tr_b16 a[148:149], v4 offset:512\n"
    "v_mfma_f32_16x16x32_bf16 v[132:135], a[132:135], a[176:179], v[132:135]\n"
    "ds_read_b64_tr_b16 a[150:151], v4 offset:4608\n"
    "v_mfma_f32_16x16x32_bf16 v[140:143], a[132:135], a[184:187], v[140:143]\n"
    "ds_read_b64_tr_b16 a[152:153], v5 offset:512\n"
    "v_mfma_f32_16x16x32_bf16 v[148:151], a[132:135], a[180:183], v[148:151]\n"
    "ds_read_b64_tr_b16 a[154:155], v5 offset:16896\n"
    "v_mfma_f32_16x16x32_bf16 v[156:159], a[132:135], a[188:191], v[156:159]\n"
    "s_waitcnt lgkmcnt(7)\n"
    "ds_read_b64_tr_b16 a[156:157], v6 offset:512\n"
    "v_mfma_f32_16x16x32_bf16 v[128:131], a[136:139], a[160:163], v[128:131]\n"
    "ds_read_b64_tr_b16 a[158:159], v6 offset:16896\n"
    "v_mfma_f32_16x16x32_bf16 v[136:139], a[136:139], a[168:171], v[136:139]\n"
    "s_waitcnt lgkmcnt(6)\n"
    "v_mfma_f32_16x16x32_bf16 v[144:147], a[136:139], a[164:167], v[144:147]\n"
    "v_mfma_f32_16x16x32_bf16 v[152:155], a[136:139], a[172:175], v[152:155]\n"
    "v_mfma_f32_16x16x32_bf16 v[132:135], a[140:143], a[160:163], v[132:135]\n"
    "v_mfma_f32_16x16x32_bf16 v[140:143], a[140:143], a[168:171], v[140:143]\n"
    "v_mfma_f32_16x16x32_bf16 v[148:151], a[140:143], a[164:167], v[148:151]\n"
    "v_mfma_f32_16x16x32_bf16 v[156:159], a[140:143], a[172:175], v[156:159]\n"
    "v_mfma_f32_16x16x32_bf16 v[160:163], a[144:147], a[176:179], v[160:163]\n"
    "v_mfma_f32_16x16x32_bf16 v[168:171], a[144:147], a[184:187], v[168:171]\n"
    "ds_read_b64_tr_b16 a[128:129], v3 offset:1024\n"
    "v_mfma_f32_16x16x32_bf16 v[176:179], a[144:147], a[180:183], v[176:179]\n"
    "ds_read_b64_tr_b16 a[130:131], v3 offset:5120\n"
    "v_mfma_f32_16x16x32_bf16 v[184:187], a[144:147], a[188:191], v[184:187]\n"
    "s_waitcnt lgkmcnt(2)\n"
    "ds_read_b64_tr_b16 a[132:133], v4 offset:1024\n"
    "v_cvt_pk_bf16_f32 v128, v128, v129\n"
    "v_mfma_f32_16x16x32_bf16 v[164:167], a[148:151], a[176:179], v[164:167]\n"
    "ds_read_b64_tr_b16 a[134:135], v4 offset:5120\n"
    "v_cvt_pk_bf16_f32 v129, v130, v131\n"
    "v_mfma_f32_16x16x32_bf16 v[172:175], a[148:151], a[184:187], v[172:175]\n"
    "ds_read_b64_tr_b16 a[136:137], v5 offset:1024\n"
    "v_cvt_pk_bf16_f32 v130, v132, v133\n"
    "v_mfma_f32_16x16x32_bf16 v[180:183], a[148:151], a[180:183], v[180:183]\n"
    "ds_read_b64_tr_b16 a[138:139], v5 offset:17408\n"
    "v_cvt_pk_bf16_f32 v131, v134, v135\n"
    "v_mfma_f32_16x16x32_bf16 v[188:191], a[148:151], a[188:191], v[188:191]\n"
    "ds_read_b64_tr_b16 a[140:141], v6 offset:1024\n"
    "v_mfma_f32_16x16x32_bf16 v[160:163], a[152:155], a[160:163], v[160:163]\n"
    "ds_read_b64_tr_b16 a[142:143], v6 offset:17408\n"
    "buffer_store_dwordx4 v[128:131], v2, s[76:79], s95 offen sc1\n"
    "v_mfma_f32_16x16x32_bf16 v[168:171], a[152:155], a[168:171], v[168:171]\n"
    "v_cvt_pk_bf16_f32 v144, v144, v145\n"
    "v_cvt_pk_bf16_f32 v145, v146, v147\n"
    "v_mfma_f32_16x16x32_bf16 v[176:179], a[152:155], a[164:167], v[176:179]\n"
    "v_cvt_pk_bf16_f32 v146, v148, v149\n"
    "v_cvt_pk_bf16_f32 v147, v150, v151\n"
    "v_mfma_f32_16x16x32_bf16 v[184:187], a[152:155], a[172:175], v[184:187]\n"
    "buffer_store_dwordx4 v[144:147], v2, s[76:79], s95 offen offset:1024 sc1\n"
    "v_cvt_pk_bf16_f32 v136, v136, v137\n"
    "v_mfma_f32_16x16x32_bf16 v[164:167], a[156:159], a[160:163], v[164:167]\n"
    "v_cvt_pk_bf16_f32 v137, v138, v139\n"
    "v_mfma_f32_16x16x32_bf16 v[172:175], a[156:159], a[168:171], v[172:175]\n"
    "v_cvt_pk_bf16_f32 v138, v140, v141\n"
    "v_cvt_pk_bf16_f32 v139, v142, v143\n"
    "v_mfma_f32_16x16x32_bf16 v[180:183], a[156:159], a[164:167], v[180:183]\n"
    "buffer_store_dwordx4 v[136:139], v2, s[76:79], s95 offen offset:2048 sc1\n"
    "v_cvt_pk_bf16_f32 v152, v152, v153\n"
    "v_mfma_f32_16x16x32_bf16 v[188:191], a[156:159], a[172:175], v[188:191]\n"
    "s_waitcnt lgkmcnt(0)\n"
    "v_cvt_pk_bf16_f32 v153, v154, v155\n"
    "v_cvt_pk_bf16_f32 v154, v156, v157\n"
    "v_mfma_f32_16x16x32_bf16 v[192:195], a[128:131], a[176:179], v[192:195]\n"
    "v_cvt_pk_bf16_f32 v155, v158, v159\n"
    "v_mfma_f32_16x16x32_bf16 v[200:203], a[128:131], a[184:187], v[200:203]\n"
    "ds_read_b64_tr_b16 a[144:145], v3 offset:1536\n"
    "buffer_store_dwordx4 v[152:155], v2, s[76:79], s95 offen offset:3072 sc1\n"
    "v_mfma_f32_16x16x32_bf16 v[208:211], a[128:131], a[180:183], v[208:211]\n"
    "ds_read_b64_tr_b16 a[146:147], v3 offset:5632\n"
    "s_add_u32 s95, s95, 0x1000\n"
    "v_mfma_f32_16x16x32_bf16 v[216:219], a[128:131], a[188:191], v[216:219]\n"
    "ds_read_b64_tr_b16 a[148:149], v4 offset:1536\n"
    "v_cvt_pk_bf16_f32 v160, v160, v161\n"
    "v_mfma_f32_16x16x32_bf16 v[196:199], a[132:135], a[176:179], v[196:199]\n"
    "ds_read_b64_tr_b16 a[150:151], v4 offset:5632\n"
    "v_cvt_pk_bf16_f32 v161, v162, v163\n"
    "v_mfma_f32_16x16x32_bf16 v[204:207], a[132:135], a[184:187], v[204:207]\n"
    "ds_read_b64_tr_b16 a[152:153], v5 offset:1536\n"
    "v_mfma_f32_16x16x32_bf16 v[212:215], a[132:135], a[180:183], v[212:215]\n"
    "ds_read_b64_tr_b16 a[154:155], v5 offset:17920\n"
    "v_cvt_pk_bf16_f32 v162, v164, v165\n"
    "v_mfma_f32_16x16x32_bf16 v[220:223], a[132:135], a[188:191], v[220:223]\n"
    "ds_read_b64_tr_b16 a[156:157], v6 offset:1536\n"
    "v_cvt_pk_bf16_f32 v163, v166, v167\n"
    "v_mfma_f32_16x16x32_bf16 v[192:195], a[136:139], a[160:163], v[192:195]\n"
    "ds_read_b64_tr_b16 a[158:159], v6 offset:17920\n"
    "buffer_store_dwordx4 v[160:163], v2, s[76:79], s95 offen sc1\n"
    "v_mfma_f32_16x16x32_bf16 v[200:203], a[136:139], a[168:171], v[200:203]\n"
    "v_cvt_pk_bf16_f32 v176, v176, v177\n"
    "v_mfma_f32_16x16x32_bf16 v[208:211], a[136:139], a[164:167], v[208:211]\n"
    "v_cvt_pk_bf16_f32 v177, v178, v179\n"
    "v_cvt_pk_bf16_f32 v178, v180, v181\n"
    "v_mfma_f32_16x16x32_bf16 v[216:219], a[136:139], a[172:175], v[216:219]\n"
    "v_cvt_pk_bf16_f32 v179, v182, v183\n"
    "buffer_store_dwordx4 v[176:179], v2, s[76:79], s95 offen offset:1024 sc1\n"
    "v_mfma_f32_16x16x32_bf16 v[196:199], a[140:143], a[160:163], v[196:199]\n"
    "v_cvt_pk_bf16_f32 v168, v168, v169\n"
    "v_mfma_f32_16x16x32_bf16 v[204:207], a[140:143], a[168:171], v[204:207]\n"
    "v_cvt_pk_bf16_f32 v169, v170, v171\n"
    "v_cvt_pk_bf16_f32 v170, v172, v173\n"
    "v_mfma_f32_16x16x32_bf16 v[212:215], a[140:143], a[164:167], v[212:215]\n"
    "v_cvt_pk_bf16_f32 v171, v174, v175\n"
    "buffer_store_dwordx4 v[168:171], v2, s[76:79], s95 offen offset:2048 sc1\n"
    "v_mfma_f32_16x16x32_bf16 v[220:223], a[140:143], a[172:175], v[220:223]\n"
    "s_waitcnt lgkmcnt(0)\n"
    "v_cvt_pk_bf16_f32 v184, v184, v185\n"
    "v_mfma_f32_16x16x32_bf16 v[224:227], a[144:147], a[176:179], v[224:227]\n"
    "v_cvt_pk_bf16_f32 v185, v186, v187\n"
    "v_cvt_pk_bf16_f32 v186, v188, v189\n"
    "v_mfma_f32_16x16x32_bf16 v[232:235], a[144:147], a[184:187], v[232:235]\n"
    "v_cvt_pk_bf16_f32 v187, v190, v191\n"
    "v_mfma_f32_16x16x32_bf16 v[240:243], a[144:147], a[180:183], v[240:243]\n"
    "buffer_store_dwordx4 v[184:187], v2, s[76:79], s95 offen offset:3072 sc1\n"
    "s_add_u32 s95, s95, 0x1000\n"
    "v_mfma_f32_16x16x32_bf16 v[248:251], a[144:147], a[188:191], v[248:251]\n"
    "v_cvt_pk_bf16_f32 v192, v192, v193\n"
    "v_cvt_pk_bf16_f32 v193, v194, v195\n"
    "v_mfma_f32_16x16x32_bf16 v[228:231], a[148:151], a[176:179], v[228:231]\n"
    "v_cvt_pk_bf16_f32 v194, v196, v197\n"
    "v_mfma_f32_16x16x32_bf16 v[236:239], a[148:151], a[184:187], v[236:239]\n"
    "v_cvt_pk_bf16_f32 v195, v198, v199\n"
    "buffer_store_dwordx4 v[192:195], v2, s[76:79], s95 offen sc1\n"
    "v_mfma_f32_16x16x32_bf16 v[244:247], a[148:151], a[180:183], v[244:247]\n"
    "v_cvt_pk_bf16_f32 v208, v208, v209\n"
    "v_mfma_f32_16x16x32_bf16 v[252:255], a[148:151], a[188:191], v[252:255]\n"
    "v_cvt_pk_bf16_f32 v209, v210, v211\n"
    "v_cvt_pk_bf16_f32 v210, v212, v213\n"
    "v_mfma_f32_16x16x32_bf16 v[224:227], a[152:155], a[160:163], v[224:227]\n"
    "v_cvt_pk_bf16_f32 v211, v214, v215\n"
    "buffer_store_dwordx4 v[208:211], v2, s[76:79], s95 offen offset:1024 sc1\n"
    "v_mfma_f32_16x16x32_bf16 v[232:235], a[152:155], a[168:171], v[232:235]\n"
    "v_cvt_pk_bf16_f32 v200, v200, v201\n"
    "v_mfma_f32_16x16x32_bf16 v[240:243], a[152:155], a[164:167], v[240:243]\n"
    "v_cvt_pk_bf16_f32 v201, v202, v203\n"
    "v_cvt_pk_bf16_f32 v202, v204, v205\n"
    "v_mfma_f32_16x16x32_bf16 v[248:251], a[152:155], a[172:175], v[248:251]\n"
    "v_cvt_pk_bf16_f32 v203, v206, v207\n"
    "buffer_store_dwordx4 v[200:203], v2, s[76:79], s95 offen offset:2048 sc1\n"
    "v_mfma_f32_16x16x32_bf16 v[228:231], a[156:159], a[160:163], v[228:231]\n"
    "v_cvt_pk_bf16_f32 v216, v216, v217\n"
    "v_mfma_f32_16x16x32_bf16 v[236:239], a[156:159], a[168:171], v[236:239]\n"
    "v_cvt_pk_bf16_f32 v217, v218, v219\n"
    "v_cvt_pk_bf16_f32 v218, v220, v221\n"
    "v_mfma_f32_16x16x32_bf16 v[244:247], a[156:159], a[164:167], v[244:247]\n"
    "v_cvt_pk_bf16_f32 v219, v222, v223\n"
    "v_mfma_f32_16x16x32_bf16 v[252:255], a[156:159], a[172:175], v[252:255]\n"
    "buffer_store_dwordx4 v[216:219], v2, s[76:79], s95 offen offset:3072 sc1\n"
    "s_add_u32 s95, s95, 0x1000\n"
    "; PHASE tailA\n"
    "s_nop 3\n"
    "v_cvt_pk_bf16_f32 v224, v224, v225\n"
    "v_cvt_pk_bf16_f32 v225, v226, v227\n"
    "v_cvt_pk_bf16_f32 v226, v228, v229\n"
    "v_cvt_pk_bf16_f32 v227, v230, v231\n"
    "buffer_store_dwordx4 v[224:227], v2, s[76:79], s95 offen sc1\n"
    "v_cvt_pk_bf16_f32 v240, v240, v241\n"
    "v_cvt_pk_bf16_f32 v241, v242, v243\n"
    "v_cvt_pk_bf16_f32 v242, v244, v245\n"
    "v_cvt_pk_bf16_f32 v243, v246, v247\n"
    "buffer_store_dwordx4 v[240:243], v2, s[76:79], s95 offen offset:1024 sc1\n"
    "v_cvt_pk_bf16_f32 v232, v232, v233\n"
    "v_cvt_pk_bf16_f32 v233, v234, v235\n"
    "v_cvt_pk_bf16_f32 v234, v236, v237\n"
    "v_cvt_pk_bf16_f32 v235, v238, v239\n"
    "buffer_store_dwordx4 v[232:235], v2, s[76:79], s95 offen offset:2048 sc1\n"
    "v_cvt_pk_bf16_f32 v248, v248, v249\n"
    "v_cvt_pk_bf16_f32 v249, v250, v251\n"
    "v_cvt_pk_bf16_f32 v250, v252, v253\n"
    "v_cvt_pk_bf16_f32 v251, v254, v255\n"
    "buffer_store_dwordx4 v[248:251], v2, s[76:79], s95 offen offset:3072 sc1\n"
    "s_add_u32 s95, s95, 0x1000\n"
    "s_barrier\n"
    "s_xor_b32 s37, s32, 1\n"
    "s_mul_i32 s37, s37, 0x3000\n"
    "s_add_u32 s37, s37, 0\n"
    "v_add_u32 v53, s37, v2\n"
    "s_xor_b32 s37, s32, 2\n"
    "s_mul_i32 s37, s37, 0x3000\n"
    "s_add_u32 s37, s37, 0x1000\n"
    "v_add_u32 v54, s37, v2\n"
    "s_xor_b32 s37, s32, 3\n"
    "s_mul_i32 s37, s37, 0x3000\n"
    "s_add_u32 s37, s37, 0x2000\n"
    "v_add_u32 v55, s37, v2\n"
    "s_mul_i32 s37, s32, 0x3000\n"
    "v_add_u32 v52, s37, v2\n"
    "ds_write_b128 v53, a[208:211]\n"
    "ds_write_b128 v53, a[212:215] offset:1024\n"
    "ds_write_b128 v53, a[216:219] offset:2048\n"
    "ds_write_b128 v53, a[220:223] offset:3072\n"
    "ds_write_b128 v54, a[224:227]\n"
    "ds_write_b128 v54, a[228:231] offset:1024\n"
    "ds_write_b128 v54, a[232:235] offset:2048\n"
    "ds_write_b128 v54, a[236:239] offset:3072\n"
    "ds_write_b128 v55, a[240:243]\n"
    "ds_write_b128 v55, a[244:247] offset:1024\n"
    "ds_write_b128 v55, a[248:251] offset:2048\n"
    "ds_write_b128 v55, a[252:255] offset:3072\n"
    "v_accvgpr_read_b32 v64, a192\n"
    "v_accvgpr_read_b32 v65, a193\n"
    "v_accvgpr_read_b32 v66, a194\n"
    "v_accvgpr_read_b32 v67, a195\n"
    "v_accvgpr_read_b32 v68, a196\n"
    "v_accvgpr_read_b32 v69, a197\n"
    "v_accvgpr_read_b32 v70, a198\n"
    "v_accvgpr_read_b32 v71, a199\n"
    "v_accvgpr_read_b32 v72, a200\n"
    "v_accvgpr_read_b32 v73, a201\n"
    "v_accvgpr_read_b32 v74, a202\n"
    "v_accvgpr_read_b32 v75, a203\n"
    "v_accvgpr_read_b32 v76, a204\n"
    "v_accvgpr_read_b32 v77, a205\n"
    "v_accvgpr_read_b32 v78, a206\n"
    "v_accvgpr_read_b32 v79, a207\n"
    "s_waitcnt lgkmcnt(0)\n"
    "s_barrier\n"
    "ds_read_b128 v[80:83], v52\n"
    "ds_read_b128 v[84:87], v52 offset:1024\n"
    "ds_read_b128 v[88:91], v52 offset:2048\n"
    "ds_read_b128 v[92:95], v52 offset:3072\n"
    "ds_read_b128 v[96:99], v52 offset:4096\n"
    "ds_read_b128 v[100:103], v52 offset:5120\n"
    "ds_read_b128 v[104:107], v52 offset:6144\n"
    "ds_read_b128 v[108:111], v52 offset:7168\n"
    "s_waitcnt lgkmcnt(7)\n"
    "v_add_f32 v64, v64, v80\n"
    "v_add_f32 v65, v65, v81\n"
    "v_add_f32 v66, v66, v82\n"
    "v_add_f32 v67, v67, v83\n"
    "s_waitcnt lgkmcnt(6)\n"
    "v_add_f32 v68, v68, v84\n"
    "v_add_f32 v69, v69, v85\n"
    "v_add_f32 v70, v70, v86\n"
    "v_add_f32 v71, v71, v87\n"
    "s_waitcnt lgkmcnt(3)\n"
    "v_add_f32 v72, v72, v88\n"
    "v_add_f32 v73, v73, v89\n"
    "v_add_f32 v74, v74, v90\n"
    "v_add_f32 v75, v75, v91\n"
    "v_add_f32 v76, v76, v92\n"
    "v_add_f32 v77, v77, v93\n"
    "v_add_f32 v78, v78, v94\n"
    "v_add_f32 v79, v79, v95\n"
    "v_add_f32 v64, v64, v96\n"
    "v_add_f32 v65, v65, v97\n"
    "v_add_f32 v66, v66, v98\n"
    "v_add_f32 v67, v67, v99\n"
    "s_waitcnt lgkmcnt(0)\n"
    "v_add_f32 v68, v68, v100\n"
    "v_add_f32 v69, v69, v101\n"
    "v_add_f32 v70, v70, v102\n"
    "v_add_f32 v71, v71, v103\n"
    "v_add_f32 v72, v72, v104\n"
    "v_add_f32 v73, v73, v105\n"
    "v_add_f32 v74, v74, v106\n"
    "v_add_f32 v75, v75, v107\n"
    "v_add_f32 v76, v76, v108\n"
    "v_add_f32 v77, v77, v109\n"
    "v_add_f32 v78, v78, v110\n"
    "v_add_f32 v79, v79, v111\n"
    "ds_read_b128 v[80:83], v52 offset:8192\n"
    "ds_read_b128 v[84:87], v52 offset:9216\n"
    "ds_read_b128 v[88:91], v52 offset:10240\n"
    "ds_read_b128 v[92:95], v52 offset:11264\n"
    "s_waitcnt lgkmcnt(3)\n"
    "v_add_f32 v64, v64, v80\n"
    "v_add_f32 v65, v65, v81\n"
    "v_add_f32 v66, v66, v82\n"
    "v_add_f32 v67, v67, v83\n"
    "s_waitcnt lgkmcnt(2)\n"
    "v_add_f32 v68, v68, v84\n"
    "v_add_f32 v69, v69, v85\n"
    "v_add_f32 v70, v70, v86\n"
    "v_add_f32 v71, v71, v87\n"
    "s_waitcnt lgkmcnt(1)\n"
    "v_add_f32 v72, v72, v88\n"
    "v_add_f32 v73, v73, v89\n"
    "v_add_f32 v74, v74, v90\n"
    "v_add_f32 v75, v75, v91\n"
    "s_waitcnt lgkmcnt(0)\n"
    "v_add_f32 v76, v76, v92\n"
    "v_add_f32 v77, v77, v93\n"
    "v_add_f32 v78, v78, v94\n"
    "v_add_f32 v79, v79, v95\n"
    "v_mul_f32 v80, 0xbfb8aa3b, v64\n"
    "v_mul_f32 v81, 0xbfb8aa3b, v65\n"
    "v_mul_f32 v82, 0xbfb8aa3b, v66\n"
    "v_mul_f32 v83, 0xbfb8aa3b, v67\n"
    "v_mul_f32 v84, 0xbfb8aa3b, v68\n"
    "v_mul_f32 v85, 0xbfb8aa3b, v69\n"
    "v_mul_f32 v86, 0xbfb8aa3b, v70\n"
    "v_mul_f32 v87, 0xbfb8aa3b, v71\n"
    "v_mul_f32 v88, 0xbfb8aa3b, v72\n"
    "v_mul_f32 v89, 0xbfb8aa3b, v73\n"
    "v_mul_f32 v90, 0xbfb8aa3b, v74\n"
    "v_mul_f32 v91, 0xbfb8aa3b, v75\n"
    "v_mul_f32 v92, 0xbfb8aa3b, v76\n"
    "v_mul_f32 v93, 0xbfb8aa3b, v77\n"
    "v_mul_f32 v94, 0xbfb8aa3b, v78\n"
    "v_mul_f32 v95, 0xbfb8aa3b, v79\n"
    "v_exp_f32 v80, v80\n"
    "v_exp_f32 v81, v81\n"
    "v_exp_f32 v82, v82\n"
    "v_exp_f32 v83, v83\n"
    "v_exp_f32 v84, v84\n"
    "v_exp_f32 v85, v85\n"
    "v_exp_f32 v86, v86\n"
    "v_exp_f32 v87, v87\n"
    "v_exp_f32 v88, v88\n"
    "v_exp_f32 v89, v89\n"
    "v_exp_f32 v90, v90\n"
    "v_exp_f32 v91, v91\n"
    "v_exp_f32 v92, v92\n"
    "v_exp_f32 v93, v93\n"
    "v_exp_f32 v94, v94\n"
    "v_exp_f32 v95, v95\n"
    "v_add_f32 v80, 1.0, v80\n"
    "v_add_f32 v81, 1.0, v81\n"
    "v_add_f32 v82, 1.0, v82\n"
    "v_add_f32 v83, 1.0, v83\n"
    "v_add_f32 v84, 1.0, v84\n"
    "v_add_f32 v85, 1.0, v85\n"
    "v_add_f32 v86, 1.0, v86\n"
    "v_add_f32 v87, 1.0, v87\n"
    "v_add_f32 v88, 1.0, v88\n"
    "v_add_f32 v89, 1.0, v89\n"
    "v_add_f32 v90, 1.0, v90\n"
    "v_add_f32 v91, 1.0, v91\n"
    "v_add_f32 v92, 1.0, v92\n"
    "v_add_f32 v93, 1.0, v93\n"
    "v_add_f32 v94, 1.0, v94\n"
    "v_add_f32 v95, 1.0, v95\n"
    "v_rcp_f32 v80, v80\n"
    "v_rcp_f32 v81, v81\n"
    "v_rcp_f32 v82, v82\n"
    "v_rcp_f32 v83, v83\n"
    "v_rcp_f32 v84, v84\n"
    "v_rcp_f32 v85, v85\n"
    "v_rcp_f32 v86, v86\n"
    "v_rcp_f32 v87, v87\n"
    "v_rcp_f32 v88, v88\n"
    "v_rcp_f32 v89, v89\n"
    "v_rcp_f32 v90, v90\n"
    "v_rcp_f32 v91, v91\n"
    "v_rcp_f32 v92, v92\n"
    "v_rcp_f32 v93, v93\n"
    "v_rcp_f32 v94, v94\n"
    "v_rcp_f32 v95, v95\n"
    "v_mul_f32 v64, v64, v80\n"
    "v_mul_f32 v65, v65, v81\n"
    "v_mul_f32 v66, v66, v82\n"
    "v_mul_f32 v67, v67, v83\n"
    "v_mul_f32 v68, v68, v84\n"
    "v_mul_f32 v69, v69, v85\n"
    "v_mul_f32 v70, v70, v86\n"
    "v_mul_f32 v71, v71, v87\n"
    "v_mul_f32 v72, v72, v88\n"
    "v_mul_f32 v73, v73, v89\n"
    "v_mul_f32 v74, v74, v90\n"
    "v_mul_f32 v75, v75, v91\n"
    "v_mul_f32 v76, v76, v92\n"
    "v_mul_f32 v77, v77, v93\n"
    "v_mul_f32 v78, v78, v94\n"
    "v_mul_f32 v79, v79, v95\n"
    "s_waitcnt vmcnt(0)\n"
    "s_barrier\n"
    "s_lshl_b32 s37, s34, 8\n"
    "s_lshl_b32 s38, s33, 3\n"
    "s_add_u32 s37, s37, s38\n"
    "s_add_u32 s37, s37, 0x80\n"
    "s_cmp_eq_u32 s32, 0\n"
    "s_cselect_b32 s37, s37, 0x40000000\n"
    "v_cmp_eq_u32 vcc, 0, v1\n"
    "v_mov_b32 v54, 0x40000000\n"
    "v_mov_b32 v53, s37\n"
    "v_cndmask_b32 v53, v54, v53\n"
    "v_add_co_u32 v50, vcc, 0x7f4a7c15, v38\n"
    "v_mov_b32 v54, 0x9e3779b9\n"
    "s_nop 0\n"
    "v_addc_co_u32 v51, vcc, v54, v39, vcc\n"
    "buffer_store_dwordx2 v[50:51], v53, s[84:87], 0 offen sc1\n"
    "; PHASE tailB\n"
    "s_lshl_b32 s95, s35, 19\n"
    "s_lshl_b32 s39, s36, 13\n"
    "s_add_u32 s95, s95, s39\n"
    "s_lshr_b32 s39, s32, 1\n"
    "s_lshl_b32 s39, s39, 12\n"
    "s_add_u32 s95, s95, s39\n"
    "s_and_b32 s39, s32, 1\n"
    "s_lshl_b32 s39, s39, 10\n"
    "s_add_u32 s95, s95, s39\n"
    "s_mov_b32 s97, 0\n"
    "s_mov_b32 s90, 0\n"
    "s_mov_b32 s92, 0\n"
    "s_mov_b32 s94, 0x3f80\n"
    "s_mov_b32 s88, 0x3f800000\n"
    "v_cmp_gt_u32 vcc, 8, v1\n"
    "v_lshlrev_b32 v52, 3, v1\n"
    "v_add_u32 v52, 0x80, v52\n"
    "v_cndmask_b32 v52, v0, v52\n"
    "s_lshl_b32 s39, s35, 8\n"
    "buffer_load_dwordx2 v[48:49], v52, s[84:87], s39 offen sc1\n"
    "v_mov_b32 v80, 0\n"
    "v_mov_b32 v81, 0\n"
    "v_mov_b32 v82, 0\n"
    "v_mov_b32 v83, 0\n"
    "v_mov_b32 v84, 0\n"
    "v_mov_b32 v85, 0\n"
    "v_mov_b32 v86, 0\n"
    "v_mov_b32 v87, 0\n"
    "v_mov_b32 v88, 0\n"
    "v_mov_b32 v89, 0\n"
    "v_mov_b32 v90, 0\n"
    "v_mov_b32 v91, 0\n"
    "v_mov_b32 v92, 0\n"
    "v_mov_b32 v93, 0\n"
    "v_mov_b32 v94, 0\n"
    "v_mov_b32 v95, 0\n"
    "GL_%=:\n"
    "s_waitcnt vmcnt(0)\n"
    "s_waitcnt vmcnt(0)\n"
    "s_mov_b32 s96, s97\n"
    "s_cmp_eq_u32 s97, 0xff\n"
    "s_cbranch_scc1 GC_%=\n"
    "v_readlane_b32 s98, v48, 8\n"
    "v_readlane_b32 s99, v49, 8\n"
    "s_add_u32 s98, s98, 0x7f4a7c14\n"
    "s_addc_u32 s99, s99, 0x9e3779b9\n"
    "v_mov_b32 v51, s99\n"
    "v_sub_co_u32 v50, vcc, s98, v48\n"
    "s_nop 1\n"
    "v_subb_co_u32 v51, vcc, v51, v49, vcc\n"
    "v_and_b32 v50, 0xfff8fff8, v50\n"
    "v_or_b32 v50, v50, v51\n"
    "v_cmp_ne_u32 vcc, 0, v50\n"
    "s_and_b32 s39, vcc_lo, 0xff\n"
    "s_or_b32 s39, s39, s97\n"
    "s_xor_b32 s39, s39, 0xff\n"
    "s_bitcmp1_b32 s39, 0\n"
    "s_cbranch_scc0 GS0_%=\n"
    "s_add_u32 s37, s95, 0\n"
    "buffer_load_dwordx4 v[128:131], v2, s[76:79], s37 offen sc1\n"
    "buffer_load_dwordx4 v[132:135], v2, s[76:79], s37 offen offset:2048 sc1\n"
    "GS0_%=:\n"
    "s_bitcmp1_b32 s39, 1\n"
    "s_cbranch_scc0 GS1_%=\n"
    "s_add_u32 s37, s95, 0x10000\n"
    "buffer_load_dwordx4 v[136:139], v2, s[76:79], s37 offen sc1\n"
    "buffer_load_dwordx4 v[140:143], v2, s[76:79], s37 offen offset:2048 sc1\n"
    "GS1_%=:\n"
    "s_bitcmp1_b32 s39, 2\n"
    "s_cbranch_scc0 GS2_%=\n"
    "s_add_u32 s37, s95, 0x20000\n"
    "buffer_load_dwordx4 v[144:147], v2, s[76:79], s37 offen sc1\n"
    "buffer_load_dwordx4 v[148:151], v2, s[76:79], s37 offen offset:2048 sc1\n"
    "GS2_%=:\n"
    "s_bitcmp1_b32 s39, 3\n"
    "s_cbranch_scc0 GS3_%=\n"
    "s_add_u32 s37, s95, 0x30000\n"
    "buffer_load_dwordx4 v[152:155], v2, s[76:79], s37 offen sc1\n"
    "buffer_load_dwordx4 v[156:159], v2, s[76:79], s37 offen offset:2048 sc1\n"
    "GS3_%=:\n"
    "s_bitcmp1_b32 s39, 4\n"
    "s_cbranch_scc0 GS4_%=\n"
    "s_add_u32 s37, s95, 0x40000\n"
    "buffer_load_dwordx4 v[160:163], v2, s[76:79], s37 offen sc1\n"
    "buffer_load_dwordx4 v[164:167], v2, s[76:79], s37 offen offset:2048 sc1\n"
    "GS4_%=:\n"
    "s_bitcmp1_b32 s39, 5\n"
    "s_cbranch_scc0 GS5_%=\n"
    "s_add_u32 s37, s95, 0x50000\n"
    "buffer_load_dwordx4 v[168:171], v2, s[76:79], s37 offen sc1\n"
    "buffer_load_dwordx4 v[172:175], v2, s[76:79], s37 offen offset:2048 sc1\n"
    "GS5_%=:\n"
    "s_bitcmp1_b32 s39, 6\n"
    "s_cbranch_scc0 GS6_%=\n"
    "s_add_u32 s37, s95, 0x60000\n"
    "buffer_load_dwordx4 v[176:179], v2, s[76:79], s37 offen sc1\n"
    "buffer_load_dwordx4 v[180:183], v2, s[76:79], s37 offen offset:2048 sc1\n"
    "GS6_%=:\n"
    "s_bitcmp1_b32 s39, 7\n"
    "s_cbranch_scc0 GS7_%=\n"
    "s_add_u32 s37, s95, 0x70000\n"
    "buffer_load_dwordx4 v[184:187], v2, s[76:79], s37 offen sc1\n"
    "buffer_load_dwordx4 v[188:191], v2, s[76:79], s37 offen offset:2048 sc1\n"
    "GS7_%=:\n"
    "s_or_b32 s97, s97, s39\n"
    "s_cmp_eq_u32 s97, 0xff\n"
    "s_cbranch_scc1 GC_%=\n"
    "s_add_u32 s92, s92, 1\n"
    "s_cmp_lt_u32 s92, 0x10000\n"
    "s_cbranch_scc1 GP_%=\n"
    "s_mov_b32 s97, 0xff\n"
    "s_branch GC_%=\n"
    "GP_%=:\n"
    "s_lshl_b32 s39, s35, 8\n"
    "buffer_load_dwordx2 v[48:49], v52, s[84:87], s39 offen sc1\n"
    "GC_%=:\n"
    "s_andn2_b32 s96, s96, s90\n"
    "s_bitcmp1_b32 s96, 0\n"
    "s_cbranch_scc0 GA0_%=\n"
    "v_dot2_f32_bf16 v80, v128, s94, v80\n"
    "v_dot2_f32_bf16 v81, v128, s88, v81\n"
    "v_dot2_f32_bf16 v82, v129, s94, v82\n"
    "v_dot2_f32_bf16 v83, v129, s88, v83\n"
    "v_dot2_f32_bf16 v84, v130, s94, v84\n"
    "v_dot2_f32_bf16 v85, v130, s88, v85\n"
    "v_dot2_f32_bf16 v86, v131, s94, v86\n"
    "v_dot2_f32_bf16 v87, v131, s88, v87\n"
    "v_dot2_f32_bf16 v88, v132, s94, v88\n"
    "v_dot2_f32_bf16 v89, v132, s88, v89\n"
    "v_dot2_f32_bf16 v90, v133, s94, v90\n"
    "v_dot2_f32_bf16 v91, v133, s88, v91\n"
    "v_dot2_f32_bf16 v92, v134, s94, v92\n"
    "v_dot2_f32_bf16 v93, v134, s88, v93\n"
    "v_dot2_f32_bf16 v94, v135, s94, v94\n"
    "v_dot2_f32_bf16 v95, v135, s88, v95\n"
    "GA0_%=:\n"
    "s_nop 0\n"
    "s_bitcmp1_b32 s96, 1\n"
    "s_cbranch_scc0 GA1_%=\n"
    "v_dot2_f32_bf16 v80, v136, s94, v80\n"
    "v_dot2_f32_bf16 v81, v136, s88, v81\n"
    "v_dot2_f32_bf16 v82, v137, s94, v82\n"
    "v_dot2_f32_bf16 v83, v137, s88, v83\n"
    "v_dot2_f32_bf16 v84, v138, s94, v84\n"
    "v_dot2_f32_bf16 v85, v138, s88, v85\n"
    "v_dot2_f32_bf16 v86, v139, s94, v86\n"
    "v_dot2_f32_bf16 v87, v139, s88, v87\n"
    "v_dot2_f32_bf16 v88, v140, s94, v88\n"
    "v_dot2_f32_bf16 v89, v140, s88, v89\n"
    "v_dot2_f32_bf16 v90, v141, s94, v90\n"
    "v_dot2_f32_bf16 v91, v141, s88, v91\n"
    "v_dot2_f32_bf16 v92, v142, s94, v92\n"
    "v_dot2_f32_bf16 v93, v142, s88, v93\n"
    "v_dot2_f32_bf16 v94, v143, s94, v94\n"
    "v_dot2_f32_bf16 v95, v143, s88, v95\n"
    "GA1_%=:\n"
    "s_nop 0\n"
    "s_bitcmp1_b32 s96, 2\n"
    "s_cbranch_scc0 GA2_%=\n"
    "v_dot2_f32_bf16 v80, v144, s94, v80\n"
    "v_dot2_f32_bf16 v81, v144, s88, v81\n"
    "v_dot2_f32_bf16 v82, v145, s94, v82\n"
    "v_dot2_f32_bf16 v83, v145, s88, v83\n"
    "v_dot2_f32_bf16 v84, v146, s94, v84\n"
    "v_dot2_f32_bf16 v85, v146, s88, v85\n"
    "v_dot2_f32_bf16 v86, v147, s94, v86\n"
    "v_dot2_f32_bf16 v87, v147, s88, v87\n"
    "v_dot2_f32_bf16 v88, v148, s94, v88\n"
    "v_dot2_f32_bf16 v89, v148, s88, v89\n"
    "v_dot2_f32_bf16 v90, v149, s94, v90\n"
    "v_dot2_f32_bf16 v91, v149, s88, v91\n"
    "v_dot2_f32_bf16 v92, v150, s94, v92\n"
    "v_dot2_f32_bf16 v93, v150, s88, v93\n"
    "v_dot2_f32_bf16 v94, v151, s94, v94\n"
    "v_dot2_f32_bf16 v95, v151, s88, v95\n"
    "GA2_%=:\n"
    "s_nop 0\n"
    "s_bitcmp1_b32 s96, 3\n"
    "s_cbranch_scc0 GA3_%=\n"
    "v_dot2_f32_bf16 v80, v152, s94, v80\n"
    "v_dot2_f32_bf16 v81, v152, s88, v81\n"
    "v_dot2_f32_bf16 v82, v153, s94, v82\n"
    "v_dot2_f32_bf16 v83, v153, s88, v83\n"
    "v_dot2_f32_bf16 v84, v154, s94, v84\n"
    "v_dot2_f32_bf16 v85, v154, s88, v85\n"
    "v_dot2_f32_bf16 v86, v155, s94, v86\n"
    "v_dot2_f32_bf16 v87, v155, s88, v87\n"
    "v_dot2_f32_bf16 v88, v156, s94, v88\n"
    "v_dot2_f32_bf16 v89, v156, s88, v89\n"
    "v_dot2_f32_bf16 v90, v157, s94, v90\n"
    "v_dot2_f32_bf16 v91, v157, s88, v91\n"
    "v_dot2_f32_bf16 v92, v158, s94, v92\n"
    "v_dot2_f32_bf16 v93, v158, s88, v93\n"
    "v_dot2_f32_bf16 v94, v159, s94, v94\n"
    "v_dot2_f32_bf16 v95, v159, s88, v95\n"
    "GA3_%=:\n"
    "s_nop 0\n"
    "s_bitcmp1_b32 s96, 4\n"
    "s_cbranch_scc0 GA4_%=\n"
    "v_dot2_f32_bf16 v80, v160, s94, v80\n"
    "v_dot2_f32_bf16 v81, v160, s88, v81\n"
    "v_dot2_f32_bf16 v82, v161, s94, v82\n"
    "v_dot2_f32_bf16 v83, v161, s88, v83\n"
    "v_dot2_f32_bf16 v84, v162, s94, v84\n"
    "v_dot2_f32_bf16 v85, v162, s88, v85\n"
    "v_dot2_f32_bf16 v86, v163, s94, v86\n"
    "v_dot2_f32_bf16 v87, v163, s88, v87\n"
    "v_dot2_f32_bf16 v88, v164, s94, v88\n"
    "v_dot2_f32_bf16 v89, v164, s88, v89\n"
    "v_dot2_f32_bf16 v90, v165, s94, v90\n"
    "v_dot2_f32_bf16 v91, v165, s88, v91\n"
    "v_dot2_f32_bf16 v92, v166, s94, v92\n"
    "v_dot2_f32_bf16 v93, v166, s88, v93\n"
    "v_dot2_f32_bf16 v94, v167, s94, v94\n"
    "v_dot2_f32_bf16 v95, v167, s88, v95\n"
    "GA4_%=:\n"
    "s_nop 0\n"
    "s_bitcmp1_b32 s96, 5\n"
    "s_cbranch_scc0 GA5_%=\n"
    "v_dot2_f32_bf16 v80, v168, s94, v80\n"
    "v_dot2_f32_bf16 v81, v168, s88, v81\n"
    "v_dot2_f32_bf16 v82, v169, s94, v82\n"
    "v_dot2_f32_bf16 v83, v169, s88, v83\n"
    "v_dot2_f32_bf16 v84, v170, s94, v84\n"
    "v_dot2_f32_bf16 v85, v170, s88, v85\n"
    "v_dot2_f32_bf16 v86, v171, s94, v86\n"
    "v_dot2_f32_bf16 v87, v171, s88, v87\n"
    "v_dot2_f32_bf16 v88, v172, s94, v88\n"
    "v_dot2_f32_bf16 v89, v172, s88, v89\n"
    "v_dot2_f32_bf16 v90, v173, s94, v90\n"
    "v_dot2_f32_bf16 v91, v173, s88, v91\n"
    "v_dot2_f32_bf16 v92, v174, s94, v92\n"
    "v_dot2_f32_bf16 v93, v174, s88, v93\n"
    "v_dot2_f32_bf16 v94, v175, s94, v94\n"
    "v_dot2_f32_bf16 v95, v175, s88, v95\n"
    "GA5_%=:\n"
    "s_nop 0\n"
    "s_bitcmp1_b32 s96, 6\n"
    "s_cbranch_scc0 GA6_%=\n"
    "v_dot2_f32_bf16 v80, v176, s94, v80\n"
    "v_dot2_f32_bf16 v81, v176, s88, v81\n"
    "v_dot2_f32_bf16 v82, v177, s94, v82\n"
    "v_dot2_f32_bf16 v83, v177, s88, v83\n"
    "v_dot2_f32_bf16 v84, v178, s94, v84\n"
    "v_dot2_f32_bf16 v85, v178, s88, v85\n"
    "v_dot2_f32_bf16 v86, v179, s94, v86\n"
    "v_dot2_f32_bf16 v87, v179, s88, v87\n"
    "v_dot2_f32_bf16 v88, v180, s94, v88\n"
    "v_dot2_f32_bf16 v89, v180, s88, v89\n"
    "v_dot2_f32_bf16 v90, v181, s94, v90\n"
    "v_dot2_f32_bf16 v91, v181, s88, v91\n"
    "v_dot2_f32_bf16 v92, v182, s94, v92\n"
    "v_dot2_f32_bf16 v93, v182, s88, v93\n"
    "v_dot2_f32_bf16 v94, v183, s94, v94\n"
    "v_dot2_f32_bf16 v95, v183, s88, v95\n"
    "GA6_%=:\n"
    "s_nop 0\n"
    "s_bitcmp1_b32 s96, 7\n"
    "s_cbranch_scc0 GA7_%=\n"
    "v_dot2_f32_bf16 v80, v184, s94, v80\n"
    "v_dot2_f32_bf16 v81, v184, s88, v81\n"
    "v_dot2_f32_bf16 v82, v185, s94, v82\n"
    "v_dot2_f32_bf16 v83, v185, s88, v83\n"
    "v_dot2_f32_bf16 v84, v186, s94, v84\n"
    "v_dot2_f32_bf16 v85, v186, s88, v85\n"
    "v_dot2_f32_bf16 v86, v187, s94, v86\n"
    "v_dot2_f32_bf16 v87, v187, s88, v87\n"
    "v_dot2_f32_bf16 v88, v188, s94, v88\n"
    "v_dot2_f32_bf16 v89, v188, s88, v89\n"
    "v_dot2_f32_bf16 v90, v189, s94, v90\n"
    "v_dot2_f32_bf16 v91, v189, s88, v91\n"
    "v_dot2_f32_bf16 v92, v190, s94, v92\n"
    "v_dot2_f32_bf16 v93, v190, s88, v93\n"
    "v_dot2_f32_bf16 v94, v191, s94, v94\n"
    "v_dot2_f32_bf16 v95, v191, s88, v95\n"
    "GA7_%=:\n"
    "s_nop 0\n"
    "s_or_b32 s90, s90, s96\n"
    "s_cmp_lg_u32 s90, 0xff\n"
    "s_cbranch_scc1 GL_%=\n"
    "s_waitcnt vmcnt(0)\n"
    "s_nop 3\n"
    "v_fma_f32 v112, v80, v64, v112\n"
    "v_fma_f32 v113, v81, v65, v113\n"
    "v_fma_f32 v114, v82, v66, v114\n"
    "v_fma_f32 v115, v83, v67, v115\n"
    "v_fma_f32 v116, v84, v68, v116\n"
    "v_fma_f32 v117, v85, v69, v117\n"
    "v_fma_f32 v118, v86, v70, v118\n"
    "v_fma_f32 v119, v87, v71, v119\n"
    "v_fma_f32 v120, v88, v72, v120\n"
    "v_fma_f32 v121, v89, v73, v121\n"
    "v_fma_f32 v122, v90, v74, v122\n"
    "v_fma_f32 v123, v91, v75, v123\n"
    "v_fma_f32 v124, v92, v76, v124\n"
    "v_fma_f32 v125, v93, v77, v125\n"
    "v_fma_f32 v126, v94, v78, v126\n"
    "v_fma_f32 v127, v95, v79, v127\n"
    "s_add_u32 s39, s93, 0x4000\n"
    "v_cvt_pk_bf16_f32 v96, v112, v113\n"
    "v_cvt_pk_bf16_f32 v97, v114, v115\n"
    "buffer_store_dwordx2 v[96:97], v56, s[80:83], s93 offen\n"
    "v_cvt_pk_bf16_f32 v98, v116, v117\n"
    "v_cvt_pk_bf16_f32 v99, v118, v119\n"
    "buffer_store_dwordx2 v[98:99], v56, s[80:83], s93 offen offset:32\n"
    "v_cvt_pk_bf16_f32 v100, v120, v121\n"
    "v_cvt_pk_bf16_f32 v101, v122, v123\n"
    "buffer_store_dwordx2 v[100:101], v56, s[80:83], s39 offen\n"
    "v_cvt_pk_bf16_f32 v102, v124, v125\n"
    "v_cvt_pk_bf16_f32 v103, v126, v127\n"
    "buffer_store_dwordx2 v[102:103], v56, s[80:83], s39 offen offset:32\n"
    "s_cmp_eq_u32 s32, 0\n"
    "s_cselect_b32 s37, 0x10000, 0\n"
    "v_mov_b32 v44, s37\n"
    "v_mov_b32 v45, 0\n"
    "s_lshl_b32 s38, s35, 8\n"
    "s_mov_b64 exec, 1\n"
    "buffer_atomic_add_x2 v[44:45], v0, s[84:87], s38 offen\n"
    "s_mov_b64 exec, -1\n"
    "; PHASE end\n"
    "s_endpgm\n"
    "SLOW0_%=:\n"
    "s_mov_b32 s92, 0\n"
    "SLOWL0_%=:\n"
    "s_sleep 2\n"
    "s_lshl_b32 s39, s35, 8\n"
    "buffer_load_dwordx2 v[48:49], v52, s[84:87], s39 offen sc1\n"
    "s_waitcnt vmcnt(0)\n"
    "v_readlane_b32 s98, v48, 8\n"
    "v_readlane_b32 s99, v49, 8\n"
    "s_add_u32 s98, s98, 0x7f4a7c14\n"
    "s_addc_u32 s99, s99, 0x9e3779b9\n"
    "v_mov_b32 v51, s99\n"
    "v_sub_co_u32 v50, vcc, s98, v48\n"
    "s_nop 1\n"
    "v_subb_co_u32 v51, vcc, v51, v49, vcc\n"
    "v_and_b32 v50, 0xfff8fff8, v50\n"
    "v_or_b32 v50, v50, v51\n"
    "v_cmp_ne_u32 vcc, 0, v50\n"
    "s_and_b32 s39, vcc_lo, 0xff\n"
    "s_cmp_lg_u32 s39, 0\n"
    "s_cbranch_scc0 SLOWE0_%=\n"
    "s_add_u32 s92, s92, 1\n"
    "s_cmp_lt_u32 s92, 0x10000\n"
    "s_cbranch_scc1 SLOWL0_%=\n"
    "SLOWE0_%=:\n"
    "s_nop 15\n"
    "s_branch BACK0_%=\n"
    "FB_%=:\n"
    "s_nop 15\n"
    "s_mov_b32 s97, m0\n"
    "s_barrier\n"
    "v_mov_b32 v44, 0xff800000\n"
    "v_mov_b32 v45, 0xff800000\n"
    "v_mov_b32 v46, 0xff800000\n"
    "v_mov_b32 v47, 0xff800000\n"
    "s_mov_b32 s90, s91\n"
    "s_mov_b32 s92, 0\n"
    "FB1_%=:\n"
    "s_add_u32 m0, s91, 0x8000\n"
    "s_nop 0\n"
    "buffer_load_dwordx4 v19, s[56:59], s90 offen lds\n"
    "buffer_load_dwordx4 v20, s[56:59], s90 offen offset:1024 lds\n"
    "buffer_load_dwordx4 v21, s[56:59], s90 offen offset:2048 lds\n"
    "buffer_load_dwordx4 v22, s[56:59], s90 offen offset:3072 lds\n"
    "s_waitcnt vmcnt(0)\n"
    "s_barrier\n"
    "ds_read_b128 a[128:131], v11\n"
    "ds_read_b128 a[132:135], v12\n"
    "ds_read_b128 a[136:139], v13\n"
    "ds_read_b128 a[140:143], v14\n"
    "s_waitcnt lgkmcnt(3)\n"
    "v_mfma_f32_16x16x32_bf16 v[64:67], a[128:131], v[128:131], 0\n"
    "v_mfma_f32_16x16x32_bf16 v[80:83], a[128:131], v[144:147], 0\n"
    "v_mfma_f32_16x16x32_bf16 v[72:75], a[128:131], v[136:139], 0\n"
    "v_mfma_f32_16x16x32_bf16 v[88:91], a[128:131], v[152:155], 0\n"
    "s_waitcnt lgkmcnt(2)\n"
    "v_mfma_f32_16x16x32_bf16 v[64:67], a[132:135], v[160:163], v[64:67]\n"
    "v_mfma_f32_16x16x32_bf16 v[80:83], a[132:135], v[176:179], v[80:83]\n"
    "v_mfma_f32_16x16x32_bf16 v[72:75], a[132:135], v[168:171], v[72:75]\n"
    "v_mfma_f32_16x16x32_bf16 v[88:91], a[132:135], v[184:187], v[88:91]\n"
    "s_waitcnt lgkmcnt(1)\n"
    "v_mfma_f32_16x16x32_bf16 v[64:67], a[136:139], v[192:195], v[64:67]\n"
    "v_mfma_f32_16x16x32_bf16 v[80:83], a[136:139], v[208:211], v[80:83]\n"
    "v_mfma_f32_16x16x32_bf16 v[72:75], a[136:139], v[200:203], v[72:75]\n"
    "v_mfma_f32_16x16x32_bf16 v[88:91], a[136:139], v[216:219], v[88:91]\n"
    "s_waitcnt lgkmcnt(0)\n"
    "v_mfma_f32_16x16x32_bf16 v[64:67], a[140:143], v[224:227], v[64:67]\n"
    "v_mfma_f32_16x16x32_bf16 v[80:83], a[140:143], v[240:243], v[80:83]\n"
    "v_mfma_f32_16x16x32_bf16 v[72:75], a[140:143], v[232:235], v[72:75]\n"
    "v_mfma_f32_16x16x32_bf16 v[88:91], a[140:143], v[248:251], v[88:91]\n"
    "ds_read_b128 a[128:131], v11 offset:4096\n"
    "ds_read_b128 a[132:135], v12 offset:4096\n"
    "ds_read_b128 a[136:139], v13 offset:4096\n"
    "ds_read_b128 a[140:143], v14 offset:4096\n"
    "s_waitcnt lgkmcnt(3)\n"
    "v_mfma_f32_16x16x32_bf16 v[68:71], a[128:131], v[128:131], 0\n"
    "v_mfma_f32_16x16x32_bf16 v[84:87], a[128:131], v[144:147], 0\n"
    "v_mfma_f32_16x16x32_bf16 v[76:79], a[128:131], v[136:139], 0\n"
    "v_mfma_f32_16x16x32_bf16 v[92:95], a[128:131], v[152:155], 0\n"
    "s_waitcnt lgkmcnt(2)\n"
    "v_mfma_f32_16x16x32_bf16 v[68:71], a[132:135], v[160:163], v[68:71]\n"
    "v_mfma_f32_16x16x32_bf16 v[84:87], a[132:135], v[176:179], v[84:87]\n"
    "v_mfma_f32_16x16x32_bf16 v[76:79], a[132:135], v[168:171], v[76:79]\n"
    "v_mfma_f32_16x16x32_bf16 v[92:95], a[132:135], v[184:187], v[92:95]\n"
    "s_waitcnt lgkmcnt(1)\n"
    "v_mfma_f32_16x16x32_bf16 v[68:71], a[136:139], v[192:195], v[68:71]\n"
    "v_mfma_f32_16x16x32_bf16 v[84:87], a[136:139], v[208:211], v[84:87]\n"
    "v_mfma_f32_16x16x32_bf16 v[76:79], a[136:139], v[200:203], v[76:79]\n"
    "v_mfma_f32_16x16x32_bf16 v[92:95], a[136:139], v[216:219], v[92:95]\n"
    "s_waitcnt lgkmcnt(0)\n"
    "v_mfma_f32_16x16x32_bf16 v[68:71], a[140:143], v[224:227], v[68:71]\n"
    "v_mfma_f32_16x16x32_bf16 v[84:87], a[140:143], v[240:243], v[84:87]\n"
    "v_mfma_f32_16x16x32_bf16 v[76:79], a[140:143], v[232:235], v[76:79]\n"
    "v_mfma_f32_16x16x32_bf16 v[92:95], a[140:143], v[248:251], v[92:95]\n"
    "v_max3_f32 v44, v44, v64, v65\n"
    "v_max3_f32 v44, v44, v66, v67\n"
    "s_nop 2\n"
    "v_max3_f32 v44, v44, v68, v69\n"
    "v_max3_f32 v44, v44, v70, v71\n"
    "v_max3_f32 v45, v45, v72, v73\n"
    "v_max3_f32 v45, v45, v74, v75\n"
    "v_max3_f32 v45, v45, v76, v77\n"
    "v_max3_f32 v45, v45, v78, v79\n"
    "v_max3_f32 v46, v46, v80, v81\n"
    "v_max3_f32 v46, v46, v82, v83\n"
    "v_max3_f32 v46, v46, v84, v85\n"
    "v_max3_f32 v46, v46, v86, v87\n"
    "v_max3_f32 v47, v47, v88, v89\n"
    "v_max3_f32 v47, v47, v90, v91\n"
    "v_max3_f32 v47, v47, v92, v93\n"
    "v_max3_f32 v47, v47, v94, v95\n"
    "ds_read_b128 a[128:131], v11 offset:8192\n"
    "ds_read_b128 a[132:135], v12 offset:8192\n"
    "ds_read_b128 a[136:139], v13 offset:8192\n"
    "ds_read_b128 a[140:143], v14 offset:8192\n"
    "s_waitcnt lgkmcnt(3)\n"
    "v_mfma_f32_16x16x32_bf16 v[64:67], a[128:131], v[128:131], 0\n"
    "v_mfma_f32_16x16x32_bf16 v[80:83], a[128:131], v[144:147], 0\n"
    "v_mfma_f32_16x16x32_bf16 v[72:75], a[128:131], v[136:139], 0\n"
    "v_mfma_f32_16x16x32_bf16 v[88:91], a[128:131], v[152:155], 0\n"
    "s_waitcnt lgkmcnt(2)\n"
    "v_mfma_f32_16x16x32_bf16 v[64:67], a[132:135], v[160:163], v[64:67]\n"
    "v_mfma_f32_16x16x32_bf16 v[80:83], a[132:135], v[176:179], v[80:83]\n"
    "v_mfma_f32_16x16x32_bf16 v[72:75], a[132:135], v[168:171], v[72:75]\n"
    "v_mfma_f32_16x16x32_bf16 v[88:91], a[132:135], v[184:187], v[88:91]\n"
    "s_waitcnt lgkmcnt(1)\n"
    "v_mfma_f32_16x16x32_bf16 v[64:67], a[136:139], v[192:195], v[64:67]\n"
    "v_mfma_f32_16x16x32_bf16 v[80:83], a[136:139], v[208:211], v[80:83]\n"
    "v_mfma_f32_16x16x32_bf16 v[72:75], a[136:139], v[200:203], v[72:75]\n"
    "v_mfma_f32_16x16x32_bf16 v[88:91], a[136:139], v[216:219], v[88:91]\n"
    "s_waitcnt lgkmcnt(0)\n"
    "v_mfma_f32_16x16x32_bf16 v[64:67], a[140:143], v[224:227], v[64:67]\n"
    "v_mfma_f32_16x16x32_bf16 v[80:83], a[140:143], v[240:243], v[80:83]\n"
    "v_mfma_f32_16x16x32_bf16 v[72:75], a[140:143], v[232:235], v[72:75]\n"
    "v_mfma_f32_16x16x32_bf16 v[88:91], a[140:143], v[248:251], v[88:91]\n"
    "ds_read_b128 a[128:131], v11 offset:12288\n"
    "ds_read_b128 a[132:135], v12 offset:12288\n"
    "ds_read_b128 a[136:139], v13 offset:12288\n"
    "ds_read_b128 a[140:143], v14 offset:12288\n"
    "s_waitcnt lgkmcnt(3)\n"
    "v_mfma_f32_16x16x32_bf16 v[68:71], a[128:131], v[128:131], 0\n"
    "v_mfma_f32_16x16x32_bf16 v[84:87], a[128:131], v[144:147], 0\n"
    "v_mfma_f32_16x16x32_bf16 v[76:79], a[128:131], v[136:139], 0\n"
    "v_mfma_f32_16x16x32_bf16 v[92:95], a[128:131], v[152:155], 0\n"
    "s_waitcnt lgkmcnt(2)\n"
    "v_mfma_f32_16x16x32_bf16 v[68:71], a[132:135], v[160:163], v[68:71]\n"
    "v_mfma_f32_16x16x32_bf16 v[84:87], a[132:135], v[176:179], v[84:87]\n"
    "v_mfma_f32_16x16x32_bf16 v[76:79], a[132:135], v[168:171], v[76:79]\n"
    "v_mfma_f32_16x16x32_bf16 v[92:95], a[132:135], v[184:187], v[92:95]\n"
    "s_waitcnt lgkmcnt(1)\n"
    "v_mfma_f32_16x16x32_bf16 v[68:71], a[136:139], v[192:195], v[68:71]\n"
    "v_mfma_f32_16x16x32_bf16 v[84:87], a[136:139], v[208:211], v[84:87]\n"
    "v_mfma_f32_16x16x32_bf16 v[76:79], a[136:139], v[200:203], v[76:79]\n"
    "v_mfma_f32_16x16x32_bf16 v[92:95], a[136:139], v[216:219], v[92:95]\n"
    "s_waitcnt lgkmcnt(0)\n"
    "v_mfma_f32_16x16x32_bf16 v[68:71], a[140:143], v[224:227], v[68:71]\n"
    "v_mfma_f32_16x16x32_bf16 v[84:87], a[140:143], v[240:243], v[84:87]\n"
    "v_mfma_f32_16x16x32_bf16 v[76:79], a[140:143], v[232:235], v[76:79]\n"
    "v_mfma_f32_16x16x32_bf16 v[92:95], a[140:143], v[248:251], v[92:95]\n"
    "v_max3_f32 v44, v44, v64, v65\n"
    "v_max3_f32 v44, v44, v66, v67\n"
    "s_nop 2\n"
    "v_max3_f32 v44, v44, v68, v69\n"
    "v_max3_f32 v44, v44, v70, v71\n"
    "v_max3_f32 v45, v45, v72, v73\n"
    "v_max3_f32 v45, v45, v74, v75\n"
    "v_max3_f32 v45, v45, v76, v77\n"
    "v_max3_f32 v45, v45, v78, v79\n"
    "v_max3_f32 v46, v46, v80, v81\n"
    "v_max3_f32 v46, v46, v82, v83\n"
    "v_max3_f32 v46, v46, v84, v85\n"
    "v_max3_f32 v46, v46, v86, v87\n"
    "v_max3_f32 v47, v47, v88, v89\n"
    "v_max3_f32 v47, v47, v90, v91\n"
    "v_max3_f32 v47, v47, v92, v93\n"
    "v_max3_f32 v47, v47, v94, v95\n"
    "s_barrier\n"
    "s_add_u32 s90, s90, 0x4000\n"
    "s_add_u32 s92, s92, 1\n"
    "s_cmp_lt_u32 s92, 8\n"
    "s_nop 15\n"
    "s_cbranch_scc1 FB1_%=\n"
    "v_mov_b32 v55, v44\n"
    "s_nop 1\n"
    "v_permlane16_swap_b32 v44, v55\n"
    "v_max_f32 v44, v44, v55\n"
    "v_mov_b32 v55, v45\n"
    "s_nop 1\n"
    "v_permlane16_swap_b32 v45, v55\n"
    "v_max_f32 v45, v45, v55\n"
    "v_mov_b32 v55, v46\n"
    "s_nop 1\n"
    "v_permlane16_swap_b32 v46, v55\n"
    "v_max_f32 v46, v46, v55\n"
    "v_mov_b32 v55, v47\n"
    "s_nop 1\n"
    "v_permlane16_swap_b32 v47, v55\n"
    "v_max_f32 v47, v47, v55\n"
    "v_mov_b32 v55, v44\n"
    "s_nop 1\n"
    "v_permlane32_swap_b32 v44, v55\n"
    "v_max_f32 v44, v44, v55\n"
    "v_mov_b32 v55, v45\n"
    "s_nop 1\n"
    "v_permlane32_swap_b32 v45, v55\n"
    "v_max_f32 v45, v45, v55\n"
    "v_mov_b32 v55, v46\n"
    "s_nop 1\n"
    "v_permlane32_swap_b32 v46, v55\n"
    "v_max_f32 v46, v46, v55\n"
    "v_mov_b32 v55, v47\n"
    "s_nop 1\n"
    "v_permlane32_swap_b32 v47, v55\n"
    "v_max_f32 v47, v47, v55\n"
    "v_accvgpr_write_b32 a0, 0\n"
    "v_accvgpr_write_b32 a1, 0\n"
    "v_accvgpr_write_b32 a2, 0\n"
    "v_accvgpr_write_b32 a3, 0\n"
    "v_accvgpr_write_b32 a4, 0\n"
    "v_accvgpr_write_b32 a5, 0\n"
    "v_accvgpr_write_b32 a6, 0\n"
    "v_accvgpr_write_b32 a7, 0\n"
    "v_accvgpr_write_b32 a8, 0\n"
    "v_accvgpr_write_b32 a9, 0\n"
    "v_accvgpr_write_b32 a10, 0\n"
    "v_accvgpr_write_b32 a11, 0\n"
    "v_accvgpr_write_b32 a12, 0\n"
    "v_accvgpr_write_b32 a13, 0\n"
    "v_accvgpr_write_b32 a14, 0\n"
    "v_accvgpr_write_b32 a15, 0\n"
    "v_accvgpr_write_b32 a16, 0\n"
    "v_accvgpr_write_b32 a17, 0\n"
    "v_accvgpr_write_b32 a18, 0\n"
    "v_accvgpr_write_b32 a19, 0\n"
    "v_accvgpr_write_b32 a20, 0\n"
    "v_accvgpr_write_b32 a21, 0\n"
    "v_accvgpr_write_b32 a22, 0\n"
    "v_accvgpr_write_b32 a23, 0\n"
    "v_accvgpr_write_b32 a24, 0\n"
    "v_accvgpr_write_b32 a25, 0\n"
    "v_accvgpr_write_b32 a26, 0\n"
    "v_accvgpr_write_b32 a27, 0\n"
    "v_accvgpr_write_b32 a28, 0\n"
    "v_accvgpr_write_b32 a29, 0\n"
    "v_accvgpr_write_b32 a30, 0\n"
    "v_accvgpr_write_b32 a31, 0\n"
    "v_accvgpr_write_b32 a32, 0\n"
    "v_accvgpr_write_b32 a33, 0\n"
    "v_accvgpr_write_b32 a34, 0\n"
    "v_accvgpr_write_b32 a35, 0\n"
    "v_accvgpr_write_b32 a36, 0\n"
    "v_accvgpr_write_b32 a37, 0\n"
    "v_accvgpr_write_b32 a38, 0\n"
    "v_accvgpr_write_b32 a39, 0\n"
    "v_accvgpr_write_b32 a40, 0\n"
    "v_accvgpr_write_b32 a41, 0\n"
    "v_accvgpr_write_b32 a42, 0\n"
    "v_accvgpr_write_b32 a43, 0\n"
    "v_accvgpr_write_b32 a44, 0\n"
    "v_accvgpr_write_b32 a45, 0\n"
    "v_accvgpr_write_b32 a46, 0\n"
    "v_accvgpr_write_b32 a47, 0\n"
    "v_accvgpr_write_b32 a48, 0\n"
    "v_accvgpr_write_b32 a49, 0\n"
    "v_accvgpr_write_b32 a50, 0\n"
    "v_accvgpr_write_b32 a51, 0\n"
    "v_accvgpr_write_b32 a52, 0\n"
    "v_accvgpr_write_b32 a53, 0\n"
    "v_accvgpr_write_b32 a54, 0\n"
    "v_accvgpr_write_b32 a55, 0\n"
    "v_accvgpr_write_b32 a56, 0\n"
    "v_accvgpr_write_b32 a57, 0\n"
    "v_accvgpr_write_b32 a58, 0\n"
    "v_accvgpr_write_b32 a59, 0\n"
    "v_accvgpr_write_b32 a60, 0\n"
    "v_accvgpr_write_b32 a61, 0\n"
    "v_accvgpr_write_b32 a62, 0\n"
    "v_accvgpr_write_b32 a63, 0\n"
    "v_accvgpr_write_b32 a64, 0\n"
    "v_accvgpr_write_b32 a65, 0\n"
    "v_accvgpr_write_b32 a66, 0\n"
    "v_accvgpr_write_b32 a67, 0\n"
    "v_accvgpr_write_b32 a68, 0\n"
    "v_accvgpr_write_b32 a69, 0\n"
    "v_accvgpr_write_b32 a70, 0\n"
    "v_accvgpr_write_b32 a71, 0\n"
    "v_accvgpr_write_b32 a72, 0\n"
    "v_accvgpr_write_b32 a73, 0\n"
    "v_accvgpr_write_b32 a74, 0\n"
    "v_accvgpr_write_b32 a75, 0\n"
    "v_accvgpr_write_b32 a76, 0\n"
    "v_accvgpr_write_b32 a77, 0\n"
    "v_accvgpr_write_b32 a78, 0\n"
    "v_accvgpr_write_b32 a79, 0\n"
    "v_accvgpr_write_b32 a80, 0\n"
    "v_accvgpr_write_b32 a81, 0\n"
    "v_accvgpr_write_b32 a82, 0\n"
    "v_accvgpr_write_b32 a83, 0\n"
    "v_accvgpr_write_b32 a84, 0\n"
    "v_accvgpr_write_b32 a85, 0\n"
    "v_accvgpr_write_b32 a86, 0\n"
    "v_accvgpr_write_b32 a87, 0\n"
    "v_accvgpr_write_b32 a88, 0\n"
    "v_accvgpr_write_b32 a89, 0\n"
    "v_accvgpr_write_b32 a90, 0\n"
    "v_accvgpr_write_b32 a91, 0\n"
    "v_accvgpr_write_b32 a92, 0\n"
    "v_accvgpr_write_b32 a93, 0\n"
    "v_accvgpr_write_b32 a94, 0\n"
    "v_accvgpr_write_b32 a95, 0\n"
    "v_accvgpr_write_b32 a96, 0\n"
    "v_accvgpr_write_b32 a97, 0\n"
    "v_accvgpr_write_b32 a98, 0\n"
    "v_accvgpr_write_b32 a99, 0\n"
    "v_accvgpr_write_b32 a100, 0\n"
    "v_accvgpr_write_b32 a101, 0\n"
    "v_accvgpr_write_b32 a102, 0\n"
    "v_accvgpr_write_b32 a103, 0\n"
    "v_accvgpr_write_b32 a104, 0\n"
    "v_accvgpr_write_b32 a105, 0\n"
    "v_accvgpr_write_b32 a106, 0\n"
    "v_accvgpr_write_b32 a107, 0\n"
    "v_accvgpr_write_b32 a108, 0\n"
    "v_accvgpr_write_b32 a109, 0\n"
    "v_accvgpr_write_b32 a110, 0\n"
    "v_accvgpr_write_b32 a111, 0\n"
    "v_accvgpr_write_b32 a112, 0\n"
    "v_accvgpr_write_b32 a113, 0\n"
    "v_accvgpr_write_b32 a114, 0\n"
    "v_accvgpr_write_b32 a115, 0\n"
    "v_accvgpr_write_b32 a116, 0\n"
    "v_accvgpr_write_b32 a117, 0\n"
    "v_accvgpr_write_b32 a118, 0\n"
    "v_accvgpr_write_b32 a119, 0\n"
    "v_accvgpr_write_b32 a120, 0\n"
    "v_accvgpr_write_b32 a121, 0\n"
    "v_accvgpr_write_b32 a122, 0\n"
    "v_accvgpr_write_b32 a123, 0\n"
    "v_accvgpr_write_b32 a124, 0\n"
    "v_accvgpr_write_b32 a125, 0\n"
    "v_accvgpr_write_b32 a126, 0\n"
    "v_accvgpr_write_b32 a127, 0\n"
    "v_accvgpr_write_b32 a144, 0\n"
    "v_accvgpr_write_b32 a145, 0\n"
    "v_accvgpr_write_b32 a146, 0\n"
    "v_accvgpr_write_b32 a147, 0\n"
    "v_accvgpr_write_b32 a148, 0\n"
    "v_accvgpr_write_b32 a149, 0\n"
    "v_accvgpr_write_b32 a150, 0\n"
    "v_accvgpr_write_b32 a151, 0\n"
    "v_accvgpr_write_b32 a152, 0\n"
    "v_accvgpr_write_b32 a153, 0\n"
    "v_accvgpr_write_b32 a154, 0\n"
    "v_accvgpr_write_b32 a155, 0\n"
    "v_accvgpr_write_b32 a156, 0\n"
    "v_accvgpr_write_b32 a157, 0\n"
    "v_accvgpr_write_b32 a158, 0\n"
    "v_accvgpr_write_b32 a159, 0\n"
    "s_mov_b32 s90, s91\n"
    "s_mov_b32 s92, 0\n"
    "FB2_%=:\n"
    "s_add_u32 m0, s91, 0x8000\n"
    "s_nop 0\n"
    "buffer_load_dwordx4 v19, s[56:59], s90 offen lds\n"
    "buffer_load_dwordx4 v20, s[56:59], s90 offen offset:1024 lds\n"
    "buffer_load_dwordx4 v21, s[56:59], s90 offen offset:2048 lds\n"
    "buffer_load_dwordx4 v22, s[56:59], s90 offen offset:3072 lds\n"
    "s_add_u32 m0, s91, 0\n"
    "s_nop 0\n"
    "buffer_load_dwordx4 v23, s[60:63], s90 offen lds\n"
    "buffer_load_dwordx4 v24, s[60:63], s90 offen offset:1024 lds\n"
    "buffer_load_dwordx4 v23, s[60:63], s90 offen offset:2048 lds\n"
    "buffer_load_dwordx4 v24, s[60:63], s90 offen offset:3072 lds\n"
    "s_waitcnt vmcnt(0)\n"
    "s_barrier\n"
    "ds_read_b128 a[128:131], v11\n"
    "ds_read_b128 a[132:135], v12\n"
    "ds_read_b128 a[136:139], v13\n"
    "ds_read_b128 a[140:143], v14\n"
    "s_waitcnt lgkmcnt(3)\n"
    "v_mfma_f32_16x16x32_bf16 v[64:67], a[128:131], v[128:131], 0\n"
    "v_mfma_f32_16x16x32_bf16 v[80:83], a[128:131], v[144:147], 0\n"
    "v_mfma_f32_16x16x32_bf16 v[72:75], a[128:131], v[136:139], 0\n"
    "v_mfma_f32_16x16x32_bf16 v[88:91], a[128:131], v[152:155], 0\n"
    "s_waitcnt lgkmcnt(2)\n"
    "v_mfma_f32_16x16x32_bf16 v[64:67], a[132:135], v[160:163], v[64:67]\n"
    "v_mfma_f32_16x16x32_bf16 v[80:83], a[132:135], v[176:179], v[80:83]\n"
    "v_mfma_f32_16x16x32_bf16 v[72:75], a[132:135], v[168:171], v[72:75]\n"
    "v_mfma_f32_16x16x32_bf16 v[88:91], a[132:135], v[184:187], v[88:91]\n"
    "s_waitcnt lgkmcnt(1)\n"
    "v_mfma_f32_16x16x32_bf16 v[64:67], a[136:139], v[192:195], v[64:67]\n"
    "v_mfma_f32_16x16x32_bf16 v[80:83], a[136:139], v[208:211], v[80:83]\n"
    "v_mfma_f32_16x16x32_bf16 v[72:75], a[136:139], v[200:203], v[72:75]\n"
    "v_mfma_f32_16x16x32_bf16 v[88:91], a[136:139], v[216:219], v[88:91]\n"
    "s_waitcnt lgkmcnt(0)\n"
    "v_mfma_f32_16x16x32_bf16 v[64:67], a[140:143], v[224:227], v[64:67]\n"
    "v_mfma_f32_16x16x32_bf16 v[80:83], a[140:143], v[240:243], v[80:83]\n"
    "v_mfma_f32_16x16x32_bf16 v[72:75], a[140:143], v[232:235], v[72:75]\n"
    "v_mfma_f32_16x16x32_bf16 v[88:91], a[140:143], v[248:251], v[88:91]\n"
    "ds_read_b128 a[128:131], v11 offset:4096\n"
    "ds_read_b128 a[132:135], v12 offset:4096\n"
    "ds_read_b128 a[136:139], v13 offset:4096\n"
    "ds_read_b128 a[140:143], v14 offset:4096\n"
    "s_waitcnt lgkmcnt(3)\n"
    "v_mfma_f32_16x16x32_bf16 v[68:71], a[128:131], v[128:131], 0\n"
    "v_mfma_f32_16x16x32_bf16 v[84:87], a[128:131], v[144:147], 0\n"
    "v_mfma_f32_16x16x32_bf16 v[76:79], a[128:131], v[136:139], 0\n"
    "v_mfma_f32_16x16x32_bf16 v[92:95], a[128:131], v[152:155], 0\n"
    "s_waitcnt lgkmcnt(2)\n"
    "v_mfma_f32_16x16x32_bf16 v[68:71], a[132:135], v[160:163], v[68:71]\n"
    "v_mfma_f32_16x16x32_bf16 v[84:87], a[132:135], v[176:179], v[84:87]\n"
    "v_mfma_f32_16x16x32_bf16 v[76:79], a[132:135], v[168:171], v[76:79]\n"
    "v_mfma_f32_16x16x32_bf16 v[92:95], a[132:135], v[184:187], v[92:95]\n"
    "s_waitcnt lgkmcnt(1)\n"
    "v_mfma_f32_16x16x32_bf16 v[68:71], a[136:139], v[192:195], v[68:71]\n"
    "v_mfma_f32_16x16x32_bf16 v[84:87], a[136:139], v[208:211], v[84:87]\n"
    "v_mfma_f32_16x16x32_bf16 v[76:79], a[136:139], v[200:203], v[76:79]\n"
    "v_mfma_f32_16x16x32_bf16 v[92:95], a[136:139], v[216:219], v[92:95]\n"
    "s_waitcnt lgkmcnt(0)\n"
    "v_mfma_f32_16x16x32_bf16 v[68:71], a[140:143], v[224:227], v[68:71]\n"
    "v_mfma_f32_16x16x32_bf16 v[84:87], a[140:143], v[240:243], v[84:87]\n"
    "v_mfma_f32_16x16x32_bf16 v[76:79], a[140:143], v[232:235], v[76:79]\n"
    "v_mfma_f32_16x16x32_bf16 v[92:95], a[140:143], v[248:251], v[92:95]\n"
    "v_sub_f32 v64, v64, v44\n"
    "v_sub_f32 v65, v65, v44\n"
    "v_sub_f32 v66, v66, v44\n"
    "v_sub_f32 v67, v67, v44\n"
    "s_nop 0\n"
    "v_sub_f32 v68, v68, v44\n"
    "v_sub_f32 v69, v69, v44\n"
    "v_sub_f32 v70, v70, v44\n"
    "v_sub_f32 v71, v71, v44\n"
    "v_sub_f32 v72, v72, v45\n"
    "v_sub_f32 v73, v73, v45\n"
    "v_sub_f32 v74, v74, v45\n"
    "v_sub_f32 v75, v75, v45\n"
    "v_sub_f32 v76, v76, v45\n"
    "v_sub_f32 v77, v77, v45\n"
    "v_sub_f32 v78, v78, v45\n"
    "v_sub_f32 v79, v79, v45\n"
    "v_exp_f32 v64, v64\n"
    "v_exp_f32 v65, v65\n"
    "v_exp_f32 v66, v66\n"
    "v_exp_f32 v67, v67\n"
    "v_exp_f32 v68, v68\n"
    "v_exp_f32 v69, v69\n"
    "v_exp_f32 v70, v70\n"
    "v_exp_f32 v71, v71\n"
    "v_exp_f32 v72, v72\n"
    "v_exp_f32 v73, v73\n"
    "v_exp_f32 v74, v74\n"
    "v_exp_f32 v75, v75\n"
    "v_exp_f32 v76, v76\n"
    "v_exp_f32 v77, v77\n"
    "v_exp_f32 v78, v78\n"
    "v_exp_f32 v79, v79\n"
    "v_cvt_pk_bf16_f32 v64, v64, v65\n"
    "v_cvt_pk_bf16_f32 v65, v66, v67\n"
    "v_cvt_pk_bf16_f32 v66, v68, v69\n"
    "v_cvt_pk_bf16_f32 v67, v70, v71\n"
    "v_cvt_pk_bf16_f32 v72, v72, v73\n"
    "v_cvt_pk_bf16_f32 v73, v74, v75\n"
    "v_cvt_pk_bf16_f32 v74, v76, v77\n"
    "v_cvt_pk_bf16_f32 v75, v78, v79\n"
    "v_sub_f32 v80, v80, v46\n"
    "v_sub_f32 v81, v81, v46\n"
    "v_sub_f32 v82, v82, v46\n"
    "v_sub_f32 v83, v83, v46\n"
    "v_sub_f32 v84, v84, v46\n"
    "v_sub_f32 v85, v85, v46\n"
    "v_sub_f32 v86, v86, v46\n"
    "v_sub_f32 v87, v87, v46\n"
    "v_sub_f32 v88, v88, v47\n"
    "v_sub_f32 v89, v89, v47\n"
    "v_sub_f32 v90, v90, v47\n"
    "v_sub_f32 v91, v91, v47\n"
    "v_sub_f32 v92, v92, v47\n"
    "v_sub_f32 v93, v93, v47\n"
    "v_sub_f32 v94, v94, v47\n"
    "v_sub_f32 v95, v95, v47\n"
    "v_exp_f32 v80, v80\n"
    "v_exp_f32 v81, v81\n"
    "v_exp_f32 v82, v82\n"
    "v_exp_f32 v83, v83\n"
    "v_exp_f32 v84, v84\n"
    "v_exp_f32 v85, v85\n"
    "v_exp_f32 v86, v86\n"
    "v_exp_f32 v87, v87\n"
    "v_exp_f32 v88, v88\n"
    "v_exp_f32 v89, v89\n"
    "v_exp_f32 v90, v90\n"
    "v_exp_f32 v91, v91\n"
    "v_exp_f32 v92, v92\n"
    "v_exp_f32 v93, v93\n"
    "v_exp_f32 v94, v94\n"
    "v_exp_f32 v95, v95\n"
    "v_cvt_pk_bf16_f32 v80, v80, v81\n"
    "v_cvt_pk_bf16_f32 v81, v82, v83\n"
    "v_cvt_pk_bf16_f32 v82, v84, v85\n"
    "v_cvt_pk_bf16_f32 v83, v86, v87\n"
    "v_cvt_pk_bf16_f32 v88, v88, v89\n"
    "v_cvt_pk_bf16_f32 v89, v90, v91\n"
    "v_cvt_pk_bf16_f32 v90, v92, v93\n"
    "v_cvt_pk_bf16_f32 v91, v94, v95\n"
    "ds_read_b64_tr_b16 a[160:161], v9\n"
    "ds_read_b64_tr_b16 a[162:163], v9 offset:4096\n"
    "ds_read_b64_tr_b16 a[164:165], v10\n"
    "ds_read_b64_tr_b16 a[166:167], v10 offset:4096\n"
    "ds_read_b64_tr_b16 a[168:169], v9 offset:512\n"
    "ds_read_b64_tr_b16 a[170:171], v9 offset:4608\n"
    "ds_read_b64_tr_b16 a[172:173], v10 offset:512\n"
    "ds_read_b64_tr_b16 a[174:175], v10 offset:4608\n"
    "ds_read_b64_tr_b16 a[176:177], v9 offset:1024\n"
    "ds_read_b64_tr_b16 a[178:179], v9 offset:5120\n"
    "ds_read_b64_tr_b16 a[180:181], v10 offset:1024\n"
    "ds_read_b64_tr_b16 a[182:183], v10 offset:5120\n"
    "ds_read_b64_tr_b16 a[184:185], v9 offset:1536\n"
    "ds_read_b64_tr_b16 a[186:187], v9 offset:5632\n"
    "ds_read_b64_tr_b16 a[188:189], v10 offset:1536\n"
    "s_waitcnt lgkmcnt(13)\n"
    "ds_read_b64_tr_b16 a[190:191], v10 offset:5632\n"
    "v_mfma_f32_16x16x32_bf16 a[144:147], v[34:37], v[64:67], a[144:147]\n"
    "v_mfma_f32_16x16x32_bf16 a[152:155], v[34:37], v[80:83], a[152:155]\n"
    "v_mfma_f32_16x16x32_bf16 a[148:151], v[34:37], v[72:75], a[148:151]\n"
    "v_mfma_f32_16x16x32_bf16 a[156:159], v[34:37], v[88:91], a[156:159]\n"
    "v_mfma_f32_16x16x32_bf16 a[0:3], a[160:163], v[64:67], a[0:3]\n"
    "v_mfma_f32_16x16x32_bf16 a[16:19], a[160:163], v[80:83], a[16:19]\n"
    "v_mfma_f32_16x16x32_bf16 a[8:11], a[160:163], v[72:75], a[8:11]\n"
    "v_mfma_f32_16x16x32_bf16 a[24:27], a[160:163], v[88:91], a[24:27]\n"
    "s_waitcnt lgkmcnt(4)\n"
    "v_mfma_f32_16x16x32_bf16 a[4:7], a[164:167], v[64:67], a[4:7]\n"
    "v_mfma_f32_16x16x32_bf16 a[20:23], a[164:167], v[80:83], a[20:23]\n"
    "v_mfma_f32_16x16x32_bf16 a[12:15], a[164:167], v[72:75], a[12:15]\n"
    "v_mfma_f32_16x16x32_bf16 a[28:31], a[164:167], v[88:91], a[28:31]\n"
    "v_mfma_f32_16x16x32_bf16 a[32:35], a[168:171], v[64:67], a[32:35]\n"
    "v_mfma_f32_16x16x32_bf16 a[48:51], a[168:171], v[80:83], a[48:51]\n"
    "v_mfma_f32_16x16x32_bf16 a[40:43], a[168:171], v[72:75], a[40:43]\n"
    "v_mfma_f32_16x16x32_bf16 a[56:59], a[168:171], v[88:91], a[56:59]\n"
    "v_mfma_f32_16x16x32_bf16 a[36:39], a[172:175], v[64:67], a[36:39]\n"
    "v_mfma_f32_16x16x32_bf16 a[52:55], a[172:175], v[80:83], a[52:55]\n"
    "v_mfma_f32_16x16x32_bf16 a[44:47], a[172:175], v[72:75], a[44:47]\n"
    "v_mfma_f32_16x16x32_bf16 a[60:63], a[172:175], v[88:91], a[60:63]\n"
    "v_mfma_f32_16x16x32_bf16 a[64:67], a[176:179], v[64:67], a[64:67]\n"
    "v_mfma_f32_16x16x32_bf16 a[80:83], a[176:179], v[80:83], a[80:83]\n"
    "v_mfma_f32_16x16x32_bf16 a[72:75], a[176:179], v[72:75], a[72:75]\n"
    "v_mfma_f32_16x16x32_bf16 a[88:91], a[176:179], v[88:91], a[88:91]\n"
    "v_mfma_f32_16x16x32_bf16 a[68:71], a[180:183], v[64:67], a[68:71]\n"
    "v_mfma_f32_16x16x32_bf16 a[84:87], a[180:183], v[80:83], a[84:87]\n"
    "v_mfma_f32_16x16x32_bf16 a[76:79], a[180:183], v[72:75], a[76:79]\n"
    "v_mfma_f32_16x16x32_bf16 a[92:95], a[180:183], v[88:91], a[92:95]\n"
    "s_waitcnt lgkmcnt(0)\n"
    "v_mfma_f32_16x16x32_bf16 a[96:99], a[184:187], v[64:67], a[96:99]\n"
    "v_mfma_f32_16x16x32_bf16 a[112:115], a[184:187], v[80:83], a[112:115]\n"
    "v_mfma_f32_16x16x32_bf16 a[104:107], a[184:187], v[72:75], a[104:107]\n"
    "v_mfma_f32_16x16x32_bf16 a[120:123], a[184:187], v[88:91], a[120:123]\n"
    "v_mfma_f32_16x16x32_bf16 a[100:103], a[188:191], v[64:67], a[100:103]\n"
    "v_mfma_f32_16x16x32_bf16 a[116:119], a[188:191], v[80:83], a[116:119]\n"
    "v_mfma_f32_16x16x32_bf16 a[108:111], a[188:191], v[72:75], a[108:111]\n"
    "v_mfma_f32_16x16x32_bf16 a[124:127], a[188:191], v[88:91], a[124:127]\n"
    "ds_read_b128 a[128:131], v11 offset:8192\n"
    "ds_read_b128 a[132:135], v12 offset:8192\n"
    "ds_read_b128 a[136:139], v13 offset:8192\n"
    "ds_read_b128 a[140:143], v14 offset:8192\n"
    "s_waitcnt lgkmcnt(3)\n"
    "v_mfma_f32_16x16x32_bf16 v[64:67], a[128:131], v[128:131], 0\n"
    "v_mfma_f32_16x16x32_bf16 v[80:83], a[128:131], v[144:147], 0\n"
    "v_mfma_f32_16x16x32_bf16 v[72:75], a[128:131], v[136:139], 0\n"
    "v_mfma_f32_16x16x32_bf16 v[88:91], a[128:131], v[152:155], 0\n"
    "s_waitcnt lgkmcnt(2)\n"
    "v_mfma_f32_16x16x32_bf16 v[64:67], a[132:135], v[160:163], v[64:67]\n"
    "v_mfma_f32_16x16x32_bf16 v[80:83], a[132:135], v[176:179], v[80:83]\n"
    "v_mfma_f32_16x16x32_bf16 v[72:75], a[132:135], v[168:171], v[72:75]\n"
    "v_mfma_f32_16x16x32_bf16 v[88:91], a[132:135], v[184:187], v[88:91]\n"
    "s_waitcnt lgkmcnt(1)\n"
    "v_mfma_f32_16x16x32_bf16 v[64:67], a[136:139], v[192:195], v[64:67]\n"
    "v_mfma_f32_16x16x32_bf16 v[80:83], a[136:139], v[208:211], v[80:83]\n"
    "v_mfma_f32_16x16x32_bf16 v[72:75], a[136:139], v[200:203], v[72:75]\n"
    "v_mfma_f32_16x16x32_bf16 v[88:91], a[136:139], v[216:219], v[88:91]\n"
    "s_waitcnt lgkmcnt(0)\n"
    "v_mfma_f32_16x16x32_bf16 v[64:67], a[140:143], v[224:227], v[64:67]\n"
    "v_mfma_f32_16x16x32_bf16 v[80:83], a[140:143], v[240:243], v[80:83]\n"
    "v_mfma_f32_16x16x32_bf16 v[72:75], a[140:143], v[232:235], v[72:75]\n"
    "v_mfma_f32_16x16x32_bf16 v[88:91], a[140:143], v[248:251], v[88:91]\n"
    "ds_read_b128 a[128:131], v11 offset:12288\n"
    "ds_read_b128 a[132:135], v12 offset:12288\n"
    "ds_read_b128 a[136:139], v13 offset:12288\n"
    "ds_read_b128 a[140:143], v14 offset:12288\n"
    "s_waitcnt lgkmcnt(3)\n"
    "v_mfma_f32_16x16x32_bf16 v[68:71], a[128:131], v[128:131], 0\n"
    "v_mfma_f32_16x16x32_bf16 v[84:87], a[128:131], v[144:147], 0\n"
    "v_mfma_f32_16x16x32_bf16 v[76:79], a[128:131], v[136:139], 0\n"
    "v_mfma_f32_16x16x32_bf16 v[92:95], a[128:131], v[152:155], 0\n"
    "s_waitcnt lgkmcnt(2)\n"
    "v_mfma_f32_16x16x32_bf16 v[68:71], a[132:135], v[160:163], v[68:71]\n"
    "v_mfma_f32_16x16x32_bf16 v[84:87], a[132:135], v[176:179], v[84:87]\n"
    "v_mfma_f32_16x16x32_bf16 v[76:79], a[132:135], v[168:171], v[76:79]\n"
    "v_mfma_f32_16x16x32_bf16 v[92:95], a[132:135], v[184:187], v[92:95]\n"
    "s_waitcnt lgkmcnt(1)\n"
    "v_mfma_f32_16x16x32_bf16 v[68:71], a[136:139], v[192:195], v[68:71]\n"
    "v_mfma_f32_16x16x32_bf16 v[84:87], a[136:139], v[208:211], v[84:87]\n"
    "v_mfma_f32_16x16x32_bf16 v[76:79], a[136:139], v[200:203], v[76:79]\n"
    "v_mfma_f32_16x16x32_bf16 v[92:95], a[136:139], v[216:219], v[92:95]\n"
    "s_waitcnt lgkmcnt(0)\n"
    "v_mfma_f32_16x16x32_bf16 v[68:71], a[140:143], v[224:227], v[68:71]\n"
    "v_mfma_f32_16x16x32_bf16 v[84:87], a[140:143], v[240:243], v[84:87]\n"
    "v_mfma_f32_16x16x32_bf16 v[76:79], a[140:143], v[232:235], v[76:79]\n"
    "v_mfma_f32_16x16x32_bf16 v[92:95], a[140:143], v[248:251], v[92:95]\n"
    "v_sub_f32 v64, v64, v44\n"
    "v_sub_f32 v65, v65, v44\n"
    "v_sub_f32 v66, v66, v44\n"
    "v_sub_f32 v67, v67, v44\n"
    "s_nop 0\n"
    "v_sub_f32 v68, v68, v44\n"
    "v_sub_f32 v69, v69, v44\n"
    "v_sub_f32 v70, v70, v44\n"
    "v_sub_f32 v71, v71, v44\n"
    "v_sub_f32 v72, v72, v45\n"
    "v_sub_f32 v73, v73, v45\n"
    "v_sub_f32 v74, v74, v45\n"
    "v_sub_f32 v75, v75, v45\n"
    "v_sub_f32 v76, v76, v45\n"
    "v_sub_f32 v77, v77, v45\n"
    "v_sub_f32 v78, v78, v45\n"
    "v_sub_f32 v79, v79, v45\n"
    "v_exp_f32 v64, v64\n"
    "v_exp_f32 v65, v65\n"
    "v_exp_f32 v66, v66\n"
    "v_exp_f32 v67, v67\n"
    "v_exp_f32 v68, v68\n"
    "v_exp_f32 v69, v69\n"
    "v_exp_f32 v70, v70\n"
    "v_exp_f32 v71, v71\n"
    "v_exp_f32 v72, v72\n"
    "v_exp_f32 v73, v73\n"
    "v_exp_f32 v74, v74\n"
    "v_exp_f32 v75, v75\n"
    "v_exp_f32 v76, v76\n"
    "v_exp_f32 v77, v77\n"
    "v_exp_f32 v78, v78\n"
    "v_exp_f32 v79, v79\n"
    "v_cvt_pk_bf16_f32 v64, v64, v65\n"
    "v_cvt_pk_bf16_f32 v65, v66, v67\n"
    "v_cvt_pk_bf16_f32 v66, v68, v69\n"
    "v_cvt_pk_bf16_f32 v67, v70, v71\n"
    "v_cvt_pk_bf16_f32 v72, v72, v73\n"
    "v_cvt_pk_bf16_f32 v73, v74, v75\n"
    "v_cvt_pk_bf16_f32 v74, v76, v77\n"
    "v_cvt_pk_bf16_f32 v75, v78, v79\n"
    "v_sub_f32 v80, v80, v46\n"
    "v_sub_f32 v81, v81, v46\n"
    "v_sub_f32 v82, v82, v46\n"
    "v_sub_f32 v83, v83, v46\n"
    "v_sub_f32 v84, v84, v46\n"
    "v_sub_f32 v85, v85, v46\n"
    "v_sub_f32 v86, v86, v46\n"
    "v_sub_f32 v87, v87, v46\n"
    "v_sub_f32 v88, v88, v47\n"
    "v_sub_f32 v89, v89, v47\n"
    "v_sub_f32 v90, v90, v47\n"
    "v_sub_f32 v91, v91, v47\n"
    "v_sub_f32 v92, v92, v47\n"
    "v_sub_f32 v93, v93, v47\n"
    "v_sub_f32 v94, v94, v47\n"
    "v_sub_f32 v95, v95, v47\n"
    "v_exp_f32 v80, v80\n"
    "v_exp_f32 v81, v81\n"
    "v_exp_f32 v82, v82\n"
    "v_exp_f32 v83, v83\n"
    "v_exp_f32 v84, v84\n"
    "v_exp_f32 v85, v85\n"
    "v_exp_f32 v86, v86\n"
    "v_exp_f32 v87, v87\n"
    "v_exp_f32 v88, v88\n"
    "v_exp_f32 v89, v89\n"
    "v_exp_f32 v90, v90\n"
    "v_exp_f32 v91, v91\n"
    "v_exp_f32 v92, v92\n"
    "v_exp_f32 v93, v93\n"
    "v_exp_f32 v94, v94\n"
    "v_exp_f32 v95, v95\n"
    "v_cvt_pk_bf16_f32 v80, v80, v81\n"
    "v_cvt_pk_bf16_f32 v81, v82, v83\n"
    "v_cvt_pk_bf16_f32 v82, v84, v85\n"
    "v_cvt_pk_bf16_f32 v83, v86, v87\n"
    "v_cvt_pk_bf16_f32 v88, v88, v89\n"
    "v_cvt_pk_bf16_f32 v89, v90, v91\n"
    "v_cvt_pk_bf16_f32 v90, v92, v93\n"
    "v_cvt_pk_bf16_f32 v91, v94, v95\n"
    "ds_read_b64_tr_b16 a[160:161], v9 offset:8192\n"
    "ds_read_b64_tr_b16 a[162:163], v9 offset:12288\n"
    "ds_read_b64_tr_b16 a[164:165], v10 offset:8192\n"
    "ds_read_b64_tr_b16 a[166:167], v10 offset:12288\n"
    "ds_read_b64_tr_b16 a[168:169], v9 offset:8704\n"
    "ds_read_b64_tr_b16 a[170:171], v9 offset:12800\n"
    "ds_read_b64_tr_b16 a[172:173], v10 offset:8704\n"
    "ds_read_b64_tr_b16 a[174:175], v10 offset:12800\n"
    "ds_read_b64_tr_b16 a[176:177], v9 offset:9216\n"
    "ds_read_b64_tr_b16 a[178:179], v9 offset:13312\n"
    "ds_read_b64_tr_b16 a[180:181], v10 offset:9216\n"
    "ds_read_b64_tr_b16 a[182:183], v10 offset:13312\n"
    "ds_read_b64_tr_b16 a[184:185], v9 offset:9728\n"
    "ds_read_b64_tr_b16 a[186:187], v9 offset:13824\n"
    "ds_read_b64_tr_b16 a[188:189], v10 offset:9728\n"
    "s_waitcnt lgkmcnt(13)\n"
    "ds_read_b64_tr_b16 a[190:191], v10 offset:13824\n"
    "v_mfma_f32_16x16x32_bf16 a[144:147], v[34:37], v[64:67], a[144:147]\n"
    "v_mfma_f32_16x16x32_bf16 a[152:155], v[34:37], v[80:83], a[152:155]\n"
    "v_mfma_f32_16x16x32_bf16 a[148:151], v[34:37], v[72:75], a[148:151]\n"
    "v_mfma_f32_16x16x32_bf16 a[156:159], v[34:37], v[88:91], a[156:159]\n"
    "v_mfma_f32_16x16x32_bf16 a[0:3], a[160:163], v[64:67], a[0:3]\n"
    "v_mfma_f32_16x16x32_bf16 a[16:19], a[160:163], v[80:83], a[16:19]\n"
    "v_mfma_f32_16x16x32_bf16 a[8:11], a[160:163], v[72:75], a[8:11]\n"
    "v_mfma_f32_16x16x32_bf16 a[24:27], a[160:163], v[88:91], a[24:27]\n"
    "s_waitcnt lgkmcnt(4)\n"
    "v_mfma_f32_16x16x32_bf16 a[4:7], a[164:167], v[64:67], a[4:7]\n"
    "v_mfma_f32_16x16x32_bf16 a[20:23], a[164:167], v[80:83], a[20:23]\n"
    "v_mfma_f32_16x16x32_bf16 a[12:15], a[164:167], v[72:75], a[12:15]\n"
    "v_mfma_f32_16x16x32_bf16 a[28:31], a[164:167], v[88:91], a[28:31]\n"
    "v_mfma_f32_16x16x32_bf16 a[32:35], a[168:171], v[64:67], a[32:35]\n"
    "v_mfma_f32_16x16x32_bf16 a[48:51], a[168:171], v[80:83], a[48:51]\n"
    "v_mfma_f32_16x16x32_bf16 a[40:43], a[168:171], v[72:75], a[40:43]\n"
    "v_mfma_f32_16x16x32_bf16 a[56:59], a[168:171], v[88:91], a[56:59]\n"
    "v_mfma_f32_16x16x32_bf16 a[36:39], a[172:175], v[64:67], a[36:39]\n"
    "v_mfma_f32_16x16x32_bf16 a[52:55], a[172:175], v[80:83], a[52:55]\n"
    "v_mfma_f32_16x16x32_bf16 a[44:47], a[172:175], v[72:75], a[44:47]\n"
    "v_mfma_f32_16x16x32_bf16 a[60:63], a[172:175], v[88:91], a[60:63]\n"
    "v_mfma_f32_16x16x32_bf16 a[64:67], a[176:179], v[64:67], a[64:67]\n"
    "v_mfma_f32_16x16x32_bf16 a[80:83], a[176:179], v[80:83], a[80:83]\n"
    "v_mfma_f32_16x16x32_bf16 a[72:75], a[176:179], v[72:75], a[72:75]\n"
    "v_mfma_f32_16x16x32_bf16 a[88:91], a[176:179], v[88:91], a[88:91]\n"
    "v_mfma_f32_16x16x32_bf16 a[68:71], a[180:183], v[64:67], a[68:71]\n"
    "v_mfma_f32_16x16x32_bf16 a[84:87], a[180:183], v[80:83], a[84:87]\n"
    "v_mfma_f32_16x16x32_bf16 a[76:79], a[180:183], v[72:75], a[76:79]\n"
    "v_mfma_f32_16x16x32_bf16 a[92:95], a[180:183], v[88:91], a[92:95]\n"
    "s_waitcnt lgkmcnt(0)\n"
    "v_mfma_f32_16x16x32_bf16 a[96:99], a[184:187], v[64:67], a[96:99]\n"
    "v_mfma_f32_16x16x32_bf16 a[112:115], a[184:187], v[80:83], a[112:115]\n"
    "v_mfma_f32_16x16x32_bf16 a[104:107], a[184:187], v[72:75], a[104:107]\n"
    "v_mfma_f32_16x16x32_bf16 a[120:123], a[184:187], v[88:91], a[120:123]\n"
    "v_mfma_f32_16x16x32_bf16 a[100:103], a[188:191], v[64:67], a[100:103]\n"
    "v_mfma_f32_16x16x32_bf16 a[116:119], a[188:191], v[80:83], a[116:119]\n"
    "v_mfma_f32_16x16x32_bf16 a[108:111], a[188:191], v[72:75], a[108:111]\n"
    "v_mfma_f32_16x16x32_bf16 a[124:127], a[188:191], v[88:91], a[124:127]\n"
    "s_barrier\n"
    "s_add_u32 s90, s90, 0x4000\n"
    "s_add_u32 s92, s92, 1\n"
    "s_cmp_lt_u32 s92, 8\n"
    "s_nop 15\n"
    "s_cbranch_scc1 FB2_%=\n"
    "v_mov_b32 v96, 0\n"
    "v_mov_b32 v97, 0\n"
    "v_mov_b32 v98, 0\n"
    "v_mov_b32 v99, 0\n"
    "v_mov_b32 v104, 0\n"
    "v_mov_b32 v105, 0\n"
    "v_mov_b32 v106, 0\n"
    "v_mov_b32 v107, 0\n"
    "v_mov_b32 v112, 0\n"
    "v_mov_b32 v113, 0\n"
    "v_mov_b32 v114, 0\n"
    "v_mov_b32 v115, 0\n"
    "v_mov_b32 v120, 0\n"
    "v_mov_b32 v121, 0\n"
    "v_mov_b32 v122, 0\n"
    "v_mov_b32 v123, 0\n"
    "s_mov_b32 m0, s97\n"
    "s_nop 15\n"
    "s_nop 15\n"
    "s_branch FBR_%=\n"
    "CEND_%=:\n"
    :
    : "s"(x), "s"(kc), "s"(vc), "s"(wq), "s"(wo), "s"(wg), "s"(wu), "s"(wd), "s"(out), "s"(ws), "s"(blockIdx.x), "v"(threadIdx.x)
    : "v1", "v2", "v3", "v4", "v5", "v6", "v7", "v8", "v9", "v10", "v11", "v12", "v13", "v14", "v15", "v16", "v17", "v18", "v19", "v20", "v21", "v22", "v23", "v24", "v25", "v26", "v27", "v28", "v29", "v30", "v31", "v32", "v33", "v34", "v35", "v36", "v37", "v38", "v39", "v40", "v41", "v42", "v43", "v44", "v45", "v46", "v47", "v48", "v49", "v50", "v51", "v52", "v53", "v54", "v55", "v56", "v57", "v58", "v59", "v60", "v61", "v62", "v63", "v64", "v65", "v66", "v67", "v68", "v69", "v70", "v71", "v72", "v73", "v74", "v75", "v76", "v77", "v78", "v79", "v80", "v81", "v82", "v83", "v84", "v85", "v86", "v87", "v88", "v89", "v90", "v91", "v92", "v93", "v94", "v95", "v96", "v97", "v98", "v99", "v100", "v101", "v102", "v103", "v104", "v105", "v106", "v107", "v108", "v109", "v110", "v111", "v112", "v113", "v114", "v115", "v116", "v117", "v118", "v119", "v120", "v121", "v122", "v123", "v124", "v125", "v126", "v127", "v128", "v129", "v130", "v131", "v132", "v133", "v134", "v135", "v136", "v137", "v138", "v139", "v140", "v141", "v142", "v143", "v144", "v145", "v146", "v147", "v148", "v149", "v150", "v151", "v152", "v153", "v154", "v155", "v156", "v157", "v158", "v159", "v160", "v161", "v162", "v163", "v164", "v165", "v166", "v167", "v168", "v169", "v170", "v171", "v172", "v173", "v174", "v175", "v176", "v177", "v178", "v179", "v180", "v181", "v182", "v183", "v184", "v185", "v186", "v187", "v188", "v189", "v190", "v191", "v192", "v193", "v194", "v195", "v196", "v197", "v198", "v199", "v200", "v201", "v202", "v203", "v204", "v205", "v206", "v207", "v208", "v209", "v210", "v211", "v212", "v213", "v214", "v215", "v216", "v217", "v218", "v219", "v220", "v221", "v222", "v223", "v224", "v225", "v226", "v227", "v228", "v229", "v230", "v231", "v232", "v233", "v234", "v235", "v236", "v237", "v238", "v239", "v240", "v241", "v242", "v243", "v244", "v245", "v246", "v247", "v248", "v249", "v250", "v251", "v252", "v253", "v254", "v255", "a0", "a1", "a2", "a3", "a4", "a5", "a6", "a7", "a8", "a9", "a10", "a11", "a12", "a13", "a14", "a15", "a16", "a17", "a18", "a19", "a20", "a21", "a22", "a23", "a24", "a25", "a26", "a27", "a28", "a29", "a30", "a31", "a32", "a33", "a34", "a35", "a36", "a37", "a38", "a39", "a40", "a41", "a42", "a43", "a44", "a45", "a46", "a47", "a48", "a49", "a50", "a51", "a52", "a53", "a54", "a55", "a56", "a57", "a58", "a59", "a60", "a61", "a62", "a63", "a64", "a65", "a66", "a67", "a68", "a69", "a70", "a71", "a72", "a73", "a74", "a75", "a76", "a77", "a78", "a79", "a80", "a81", "a82", "a83", "a84", "a85", "a86", "a87", "a88", "a89", "a90", "a91", "a92", "a93", "a94", "a95", "a96", "a97", "a98", "a99", "a100", "a101", "a102", "a103", "a104", "a105", "a106", "a107", "a108", "a109", "a110", "a111", "a112", "a113", "a114", "a115", "a116", "a117", "a118", "a119", "a120", "a121", "a122", "a123", "a124", "a125", "a126", "a127", "a128", "a129", "a130", "a131", "a132", "a133", "a134", "a135", "a136", "a137", "a138", "a139", "a140", "a141", "a142", "a143", "a144", "a145", "a146", "a147", "a148", "a149", "a150", "a151", "a152", "a153", "a154", "a155", "a156", "a157", "a158", "a159", "a160", "a161", "a162", "a163", "a164", "a165", "a166", "a167", "a168", "a169", "a170", "a171", "a172", "a173", "a174", "a175", "a176", "a177", "a178", "a179", "a180", "a181", "a182", "a183", "a184", "a185", "a186", "a187", "a188", "a189", "a190", "a191", "a192", "a193", "a194", "a195", "a196", "a197", "a198", "a199", "a200", "a201", "a202", "a203", "a204", "a205", "a206", "a207", "a208", "a209", "a210", "a211", "a212", "a213", "a214", "a215", "a216", "a217", "a218", "a219", "a220", "a221", "a222", "a223", "a224", "a225", "a226", "a227", "a228", "a229", "a230", "a231", "a232", "a233", "a234", "a235", "a236", "a237", "a238", "a239", "a240", "a241", "a242", "a243", "a244", "a245", "a246", "a247", "a248", "a249", "a250", "a251", "a252", "a253", "a254", "a255", "s32", "s33", "s34", "s35", "s36", "s37", "s38", "s39", "s40", "s41", "s42", "s43", "s44", "s45", "s46", "s47", "s48", "s49", "s50", "s51", "s52", "s53", "s54", "s55", "s56", "s57", "s58", "s59", "s60", "s61", "s62", "s63", "s64", "s65", "s66", "s67", "s68", "s69", "s70", "s71", "s72", "s73", "s74", "s75", "s76", "s77", "s78", "s79", "s80", "s81", "s82", "s83", "s84", "s85", "s86", "s87", "s88", "s89", "s90", "s91", "s92", "s93", "s94", "s95", "s96", "s97", "s98", "s99", "vcc", "scc", "memory");
}

extern "C" void kernel_launch(void* const* d_in, const int* in_sizes, int n_in, void* d_out, int out_size, void* d_ws, size_t ws_size,
                              hipStream_t stream) {
  (void)in_sizes; (void)n_in; (void)out_size; (void)ws_size;
  hipFuncSetAttribute((const void*)fused_block_kernel, hipFuncAttributeMaxDynamicSharedMemorySize, 163840);
  hipLaunchKernelGGL(fused_block_kernel, dim3(256), dim3(256), 163840, stream, d_in[0], d_in[1], d_in[2], d_in[3], d_in[4], d_in[6], d_in[7], d_in[8], d_out, d_ws);
}
```
